```python
import torch

def _softmax(x, axis=-1):
    return torch.softmax(x, dim=axis)

def _gelu(x):
    return torch.nn.functional.gelu(x, approximate='tanh')

def _silu(x):
    return torch.nn.functional.silu(x)

def _sigmoid(x):
    return torch.sigmoid(x)

def _layernorm(x, axis=-1, eps=1e-05):
    m = x.mean(dim=axis, keepdim=True)
    v = x.var(dim=axis, keepdim=True, unbiased=False)
    return (x - m) / torch.sqrt(v + eps)

def _scatter_add(tgt, idx, src, axis=0):
    return tgt.clone().index_add_(axis, idx, src)

def _segment_sum(src, seg, n):
    out = torch.zeros((n,) + src.shape[1:], dtype=src.dtype)
    return out.index_add_(0, seg, src)

def _pad(x, pw):
    flat = tuple((v for pair in reversed(pw) for v in pair))
    return torch.nn.functional.pad(x, flat)
kernel_name = 'adaLN_cond__gqa_block_cached__glu_gated_reglu__decode'

def setup_inputs():

    _w = lambda a: (lambda t: t.to(torch.bfloat16) if t.is_floating_point() else t)(torch.as_tensor(a))

    def _A_setup():
        B, Sq, D = (8, 1, 1024)
        Hq, Hkv, Sk, Dh = (32, 4, 8192, 128)
        assert Hq % Hkv == 0, (Hq, Hkv)
        return {'x': _w((lambda *s: torch.randn(*s, dtype=torch.bfloat16) * 0.1)(B, Sq, D)), 'k_cache': _w((lambda *s: torch.randn(*s, dtype=torch.bfloat16) * 0.8)(B, Hkv, Sk, Dh)), 'v_cache': _w((lambda *s: torch.randn(*s, dtype=torch.bfloat16) * 3.2)(B, Hkv, Sk, Dh)), 'wq': _w((lambda *s: torch.randn(*s, dtype=torch.bfloat16) * 0.8)(D, Hq * Dh) * D ** (-0.5)), 'wo': _w((lambda *s: torch.randn(*s, dtype=torch.bfloat16) * 3.2)(Hq * Dh, D) * (Hq * Dh) ** (-0.5))}
    _A_in = _A_setup()

    def _B_setup():
        B, S, D, F = (8, 1, 1024, 3584)
        return {'x': _w((lambda *s: torch.randn(*s, dtype=torch.bfloat16) * 0.1)(B, S, D)), 'W_gate': _w((lambda *s: torch.randn(*s, dtype=torch.bfloat16) * 3.2)(D, F) * (2.0 / D) ** 0.5), 'W_up': _w((lambda *s: torch.randn(*s, dtype=torch.bfloat16) * 3.2)(D, F) * (2.0 / D) ** 0.5), 'W_down': _w((lambda *s: torch.randn(*s, dtype=torch.bfloat16) * 3.2)(F, D) * (1.0 / F) ** 0.5)}
    _B_in = _B_setup()
    return {'A_x': _A_in['x'], 'A_k_cache': _A_in['k_cache'], 'A_v_cache': _A_in['v_cache'], 'A_wq': _A_in['wq'], 'A_wo': _A_in['wo'], 'B_x': _B_in['x'], 'B_W_gate': _B_in['W_gate'], 'B_W_up': _B_in['W_up'], 'B_W_down': _B_in['W_down'], 'c': _w((lambda *s: torch.randn(*s, dtype=torch.bfloat16) * 0.4)(8, 1024) * 8.0), 'w_mod': _w((lambda *s: torch.randn(*s, dtype=torch.bfloat16) * 0.4)(1024, 6 * 1024) * 1024 ** (-0.5) * 8.0)}

def reference(A_x, A_k_cache, A_v_cache, A_wq, A_wo, B_x, B_W_gate, B_W_up, B_W_down, c, w_mod):

    def _A_ref(x, k_cache, v_cache, wq, wo):
        B, Sq, D = x.shape
        Hq, Dh = (32, 128)
        Hkv = k_cache.shape[1]
        rep = Hq // Hkv
        q = (lambda x, *d: x.permute(*d))((x @ wq).reshape(B, Sq, Hq, Dh), 0, 2, 1, 3)
        k = (lambda x, n, axis: torch.repeat_interleave(x, n, dim=axis))(k_cache, rep, axis=1)
        v = (lambda x, n, axis: torch.repeat_interleave(x, n, dim=axis))(v_cache, rep, axis=1)
        scores = torch.einsum('bhqd,bhkd->bhqk', q, k) * Dh ** (-0.5)
        p = _softmax(scores, axis=-1)
        o = torch.einsum('bhqk,bhkd->bhqd', p, v)
        return (lambda x, *d: x.permute(*d))(o, 0, 2, 1, 3).reshape(B, Sq, Hq * Dh) @ wo

    def _B_ref(x, W_gate, W_up, W_down):
        gate = torch.einsum('bsd,df->bsf', x, W_gate)
        up = torch.einsum('bsd,df->bsf', x, W_up)
        gate = gate * (lambda x, dt: x.to(dt))(gate > 0, gate.dtype)
        h = gate * up
        return torch.einsum('bsf,fd->bsd', h, W_down)
    x = A_x
    mod = (_silu(c) @ w_mod).reshape(c.shape[0], 6, 1, -1)
    sa, ca, ga, sb, cb, gb = (mod[:, 0].clone(), mod[:, 1].clone(), mod[:, 2].clone(), mod[:, 3].clone(), mod[:, 4].clone(), mod[:, 5].clone())
    h = _layernorm(x, axis=-1) * (1 + ca) + sa
    x = x + ga * _A_ref(h, A_k_cache, A_v_cache, A_wq, A_wo)
    h = _layernorm(x, axis=-1) * (1 + cb) + sb
    x = x + gb * _B_ref(h, B_W_gate, B_W_up, B_W_down)
    return x
```

```cpp
#include <hip/hip_runtime.h>
#include <hip/hip_bf16.h>
#include <stdint.h>

typedef unsigned v4u __attribute__((ext_vector_type(4)));
typedef float f32x4 __attribute__((ext_vector_type(4)));

__device__ __forceinline__ float bf2f(unsigned short b) { return __builtin_bit_cast(float, (unsigned)b << 16); }
__device__ __forceinline__ float bflo(unsigned u) { return __builtin_bit_cast(float, u << 16); }
__device__ __forceinline__ float bfhi(unsigned u) { return __builtin_bit_cast(float, u & 0xffff0000u); }
__device__ __forceinline__ float rbf(float f) { return __bfloat162float(__float2bfloat16(f)); }

constexpr int D = 1024, NB = 8, HQ = 32, HKV = 4, SK = 8192, DH = 128, FF = 3584;

template <int K>
__global__ void __launch_bounds__(256) gemv8(const float* __restrict__ X, const unsigned short* __restrict__ W, float* __restrict__ Y, int N) {
  __shared__ float xs[NB][K];
  __shared__ float red[32][NB][64 + 1];
  for (int i = threadIdx.x; i < NB * K; i += 256) xs[i / K][i % K] = X[i];
  __syncthreads();
  const int kq = threadIdx.x >> 3, cq = threadIdx.x & 7;
  const int n0 = blockIdx.x * 64 + cq * 8;
  float acc[NB][8];
#pragma unroll
  for (int b = 0; b < NB; ++b)
#pragma unroll
    for (int j = 0; j < 8; ++j) acc[b][j] = 0.f;
  for (int k = kq; k < K; k += 32) {
    v4u w = *(const v4u*)(W + (size_t)k * N + n0);
    float wf[8] = {bflo(w.x), bfhi(w.x), bflo(w.y), bfhi(w.y), bflo(w.z), bfhi(w.z), bflo(w.w), bfhi(w.w)};
#pragma unroll
    for (int b = 0; b < NB; ++b) {
      float xv = xs[b][k];
#pragma unroll
      for (int j = 0; j < 8; ++j) acc[b][j] = fmaf(xv, wf[j], acc[b][j]);
    }
  }
#pragma unroll
  for (int b = 0; b < NB; ++b)
#pragma unroll
    for (int j = 0; j < 8; ++j) red[kq][b][cq * 8 + j] = acc[b][j];
  __syncthreads();
  for (int i = threadIdx.x; i < NB * 64; i += 256) {
    int b = i >> 6, c = i & 63;
    float s = 0.f;
    for (int q = 0; q < 32; ++q) s += red[q][b][c];
    Y[(size_t)b * N + blockIdx.x * 64 + c] = s;
  }
}

__global__ void k_silu(const unsigned short* c, float* sc) {
  int i = blockIdx.x * 256 + threadIdx.x;
  float v = bf2f(c[i]);
  sc[i] = rbf(v / (1.f + __expf(-v)));
}

__global__ void __launch_bounds__(256) k_lnmod(const float* x, const unsigned short* xb, const float* mod, int soff, int coff, float* h) {
  __shared__ float red[256];
  int b = blockIdx.x, t = threadIdx.x;
  float v[4];
  float s = 0.f;
  for (int i = 0; i < 4; ++i) { v[i] = xb ? bf2f(xb[b * D + t + 256 * i]) : x[b * D + t + 256 * i]; s += v[i]; }
  red[t] = s; __syncthreads();
  for (int o = 128; o > 0; o >>= 1) { if (t < o) red[t] += red[t + o]; __syncthreads(); }
  float m = red[0] / D; __syncthreads();
  s = 0.f;
  for (int i = 0; i < 4; ++i) s += (v[i] - m) * (v[i] - m);
  red[t] = s; __syncthreads();
  for (int o = 128; o > 0; o >>= 1) { if (t < o) red[t] += red[t + o]; __syncthreads(); }
  float var = red[0] / D;
  float r = 1.f / sqrtf(var + 1e-5f);
  for (int i = 0; i < 4; ++i) {
    int d = t + 256 * i;
    float cm = rbf(mod[b * 6 * D + coff + d]), sm = rbf(mod[b * 6 * D + soff + d]);
    h[b * D + d] = rbf((v[i] - m) * r * (1.f + cm) + sm);
  }
}

constexpr int NSPLIT = 8;
__global__ void __launch_bounds__(256) k_attn(const float* q, const unsigned short* Kc, const unsigned short* Vc, float* part) {
  __shared__ float qs[DH];
  __shared__ float sc[SK / NSPLIT];
  __shared__ float red[256];
  __shared__ float osum[2][DH];
  int s = blockIdx.x, h = blockIdx.y, b = blockIdx.z, t = threadIdx.x;
  int kvh = h / (HQ / HKV);
  if (t < DH) qs[t] = rbf(q[b * (HQ * DH) + h * DH + t]);
  __syncthreads();
  const unsigned short* Kb = Kc + ((size_t)(b * HKV + kvh) * SK + s * (SK / NSPLIT)) * DH;
  const unsigned short* Vb = Vc + ((size_t)(b * HKV + kvh) * SK + s * (SK / NSPLIT)) * DH;
  float mx = -INFINITY;
  for (int k = t; k < SK / NSPLIT; k += 256) {
    const v4u* kr = (const v4u*)(Kb + (size_t)k * DH);
    float a = 0.f;
    for (int j = 0; j < 16; ++j) {
      v4u w = kr[j];
      a += bflo(w.x) * qs[j * 8] + bfhi(w.x) * qs[j * 8 + 1] + bflo(w.y) * qs[j * 8 + 2] + bfhi(w.y) * qs[j * 8 + 3]
         + bflo(w.z) * qs[j * 8 + 4] + bfhi(w.z) * qs[j * 8 + 5] + bflo(w.w) * qs[j * 8 + 6] + bfhi(w.w) * qs[j * 8 + 7];
    }
    a *= 0.08838834764831845f;
    sc[k] = a;
    mx = fmaxf(mx, a);
  }
  red[t] = mx; __syncthreads();
  for (int o = 128; o > 0; o >>= 1) { if (t < o) red[t] = fmaxf(red[t], red[t + o]); __syncthreads(); }
  mx = red[0]; __syncthreads();
  float l = 0.f;
  for (int k = t; k < SK / NSPLIT; k += 256) { float p = __expf(sc[k] - mx); sc[k] = p; l += p; }
  red[t] = l; __syncthreads();
  for (int o = 128; o > 0; o >>= 1) { if (t < o) red[t] += red[t + o]; __syncthreads(); }
  l = red[0];
  int d = t & 127, half = t >> 7;
  float o = 0.f;
  for (int k = half; k < SK / NSPLIT; k += 2) o += sc[k] * bf2f(Vb[(size_t)k * DH + d]);
  osum[half][d] = o;
  __syncthreads();
  float* pp = part + ((size_t)(b * HQ + h) * NSPLIT + s) * (DH + 2);
  if (t < DH) pp[t] = osum[0][t] + osum[1][t];
  if (t == 0) { pp[DH] = mx; pp[DH + 1] = l; }
}

__global__ void k_comb(const float* part, float* o) {
  int bh = blockIdx.x, t = threadIdx.x;
  const float* pp = part + (size_t)bh * NSPLIT * (DH + 2);
  float M = -INFINITY;
  for (int s = 0; s < NSPLIT; ++s) M = fmaxf(M, pp[s * (DH + 2) + DH]);
  float L = 0.f, a = 0.f;
  for (int s = 0; s < NSPLIT; ++s) { float w = __expf(pp[s * (DH + 2) + DH] - M); L += w * pp[s * (DH + 2) + DH + 1]; a += w * pp[s * (DH + 2) + t]; }
  o[bh * DH + t] = rbf(a / L);
}

__global__ void k_res1(const unsigned short* x, const float* mod, const float* ao, float* x1) {
  int i = blockIdx.x * 256 + threadIdx.x;
  int b = i / D, d = i % D;
  x1[i] = rbf(bf2f(x[i]) + rbf(rbf(mod[b * 6 * D + 2 * D + d]) * rbf(ao[i])));
}
__global__ void k_glu(const float* g, const float* u, float* hm) {
  int i = blockIdx.x * 256 + threadIdx.x;
  float gg = rbf(g[i]);
  gg = gg > 0.f ? gg : 0.f;
  hm[i] = rbf(gg * rbf(u[i]));
}
__global__ void k_res2(const float* x1, const float* mod, const float* dn, __hip_bfloat16* out) {
  int i = blockIdx.x * 256 + threadIdx.x;
  int b = i / D, d = i % D;
  out[i] = __float2bfloat16(x1[i] + rbf(rbf(mod[b * 6 * D + 5 * D + d]) * rbf(dn[i])));
}

extern "C" void kernel_launch(void* const* d_in, const int* in_sizes, int n_in, void* d_out, int out_size, void* d_ws, size_t ws_size,
                              hipStream_t stream) {
  const unsigned short* x = (const unsigned short*)d_in[0];
  const unsigned short* Kc = (const unsigned short*)d_in[1];
  const unsigned short* Vc = (const unsigned short*)d_in[2];
  const unsigned short* wq = (const unsigned short*)d_in[3];
  const unsigned short* wo = (const unsigned short*)d_in[4];
  const unsigned short* wg = (const unsigned short*)d_in[6];
  const unsigned short* wu = (const unsigned short*)d_in[7];
  const unsigned short* wd = (const unsigned short*)d_in[8];
  const unsigned short* c = (const unsigned short*)d_in[9];
  const unsigned short* wm = (const unsigned short*)d_in[10];
  float* ws = (float*)d_ws;
  float* sc = ws;
  float* mod = sc + 8192;
  float* h = mod + 49152;
  float* q = h + 8192;
  float* part = q + 32768;
  float* o = part + 256 * 8 * 130;
  float* ao = o + 32768;
  float* x1 = ao + 8192;
  float* h2 = x1 + 8192;
  float* g = h2 + 8192;
  float* u = g + 28672;
  float* hm = u + 28672;
  float* dn = hm + 28672;
  k_silu<<<32, 256, 0, stream>>>(c, sc);
  gemv8<1024><<<96, 256, 0, stream>>>(sc, wm, mod, 6144);
  k_lnmod<<<8, 256, 0, stream>>>(nullptr, x, mod, 0, D, h);
  gemv8<1024><<<64, 256, 0, stream>>>(h, wq, q, 4096);
  k_attn<<<dim3(NSPLIT, HQ, NB), 256, 0, stream>>>(q, Kc, Vc, part);
  k_comb<<<256, 128, 0, stream>>>(part, o);
  float* tmp = dn + 8192;
  {
  }
  extern __global__ void k_slice(const float*, float*, int, int, int);
  extern __global__ void k_sum4(const float*, float*, int);
  for (int s = 0; s < 4; ++s) {
    k_slice<<<32, 256, 0, stream>>>(o, tmp + s * 8192, 4096, s * 1024, 1024);
    gemv8<1024><<<16, 256, 0, stream>>>(tmp + s * 8192, wo + (size_t)s * 1024 * 1024, tmp + 32768 + s * 8192, 1024);
  }
  k_sum4<<<32, 256, 0, stream>>>(tmp + 32768, ao, 4);
  k_res1<<<32, 256, 0, stream>>>(x, mod, ao, x1);
  k_lnmod<<<8, 256, 0, stream>>>(x1, nullptr, mod, 3 * D, 4 * D, h2);
  gemv8<1024><<<56, 256, 0, stream>>>(h2, wg, g, FF);
  gemv8<1024><<<56, 256, 0, stream>>>(h2, wu, u, FF);
  k_glu<<<112, 256, 0, stream>>>(g, u, hm);
  for (int s = 0; s < 4; ++s) {
    k_slice<<<28, 256, 0, stream>>>(hm, tmp + s * 8192, FF, s * 896, 896);
    gemv8<896><<<16, 256, 0, stream>>>(tmp + s * 8192, wd + (size_t)s * 896 * 1024, tmp + 32768 + s * 8192, 1024);
  }
  k_sum4<<<32, 256, 0, stream>>>(tmp + 32768, dn, 4);
  k_res2<<<32, 256, 0, stream>>>(x1, mod, dn, (__hip_bfloat16*)d_out);
}

__global__ void k_slice(const float* src, float* dst, int ld, int off, int w) {
  int i = blockIdx.x * 256 + threadIdx.x;
  int b = i / w, k = i % w;
  dst[i] = src[b * ld + off + k];
}
__global__ void k_sum4(const float* src, float* dst, int n) {
  int i = blockIdx.x * 256 + threadIdx.x;
  float s = 0.f;
  for (int j = 0; j < n; ++j) s += src[j * 8192 + i];
  dst[i] = s;
}
```

```cpp
#include <hip/hip_runtime.h>
#include <hip/hip_bf16.h>
#include <stdint.h>

typedef unsigned v4u __attribute__((ext_vector_type(4)));
typedef float f32x4 __attribute__((ext_vector_type(4)));

__device__ __forceinline__ float bf2f(unsigned short b) { return __builtin_bit_cast(float, (unsigned)b << 16); }
__device__ __forceinline__ float bflo(unsigned u) { return __builtin_bit_cast(float, u << 16); }
__device__ __forceinline__ float bfhi(unsigned u) { return __builtin_bit_cast(float, u & 0xffff0000u); }
__device__ __forceinline__ float rbf(float f) { unsigned r; asm volatile("v_cvt_pk_bf16_f32 %0, %1, %1" : "=v"(r) : "v"(f)); return __builtin_bit_cast(float, r << 16); }

constexpr int D = 1024, NB = 8, HQ = 32, HKV = 4, SK = 8192, DH = 128, FF = 3584;

template <int K>
__global__ void __launch_bounds__(256) gemv8(const float* __restrict__ X, const unsigned short* __restrict__ W, float* __restrict__ Y, int N) {
  __shared__ float xs[NB][K];
  __shared__ float red[32][NB][64 + 1];
  for (int i = threadIdx.x; i < NB * K; i += 256) xs[i / K][i % K] = X[i];
  __syncthreads();
  const int kq = threadIdx.x >> 3, cq = threadIdx.x & 7;
  const int n0 = blockIdx.x * 64 + cq * 8;
  float acc[NB][8];
#pragma unroll
  for (int b = 0; b < NB; ++b)
#pragma unroll
    for (int j = 0; j < 8; ++j) acc[b][j] = 0.f;
  for (int k = kq; k < K; k += 32) {
    v4u w = *(const v4u*)(W + (size_t)k * N + n0);
    float wf[8] = {bflo(w.x), bfhi(w.x), bflo(w.y), bfhi(w.y), bflo(w.z), bfhi(w.z), bflo(w.w), bfhi(w.w)};
#pragma unroll
    for (int b = 0; b < NB; ++b) {
      float xv = xs[b][k];
#pragma unroll
      for (int j = 0; j < 8; ++j) acc[b][j] = fmaf(xv, wf[j], acc[b][j]);
    }
  }
#pragma unroll
  for (int b = 0; b < NB; ++b)
#pragma unroll
    for (int j = 0; j < 8; ++j) red[kq][b][cq * 8 + j] = acc[b][j];
  __syncthreads();
  for (int i = threadIdx.x; i < NB * 64; i += 256) {
    int b = i >> 6, c = i & 63;
    float s = 0.f;
    for (int q = 0; q < 32; ++q) s += red[q][b][c];
    Y[(size_t)b * N + blockIdx.x * 64 + c] = s;
  }
}

__global__ void k_silu(const unsigned short* c, float* sc) {
  int i = blockIdx.x * 256 + threadIdx.x;
  float v = bf2f(c[i]);
  sc[i] = rbf(v / (1.f + __expf(-v)));
}

__global__ void __launch_bounds__(256) k_lnmod(const float* x, const unsigned short* xb, const float* mod, int soff, int coff, float* h) {
  __shared__ float red[256];
  int b = blockIdx.x, t = threadIdx.x;
  float v[4];
  float s = 0.f;
  for (int i = 0; i < 4; ++i) { v[i] = xb ? bf2f(xb[b * D + t + 256 * i]) : x[b * D + t + 256 * i]; s += v[i]; }
  red[t] = s; __syncthreads();
  for (int o = 128; o > 0; o >>= 1) { if (t < o) red[t] += red[t + o]; __syncthreads(); }
  float m = red[0] / D; __syncthreads();
  s = 0.f;
  for (int i = 0; i < 4; ++i) s += (v[i] - m) * (v[i] - m);
  red[t] = s; __syncthreads();
  for (int o = 128; o > 0; o >>= 1) { if (t < o) red[t] += red[t + o]; __syncthreads(); }
  float var = rbf(red[0] / D);
  float sd = rbf(sqrtf(rbf(var + 1e-5f)));
  float mb = rbf(m);
  for (int i = 0; i < 4; ++i) {
    int d = t + 256 * i;
    float cm = rbf(mod[b * 6 * D + coff + d]), sm = rbf(mod[b * 6 * D + soff + d]);
    float ln = rbf(rbf(v[i] - mb) / sd);
    h[b * D + d] = rbf(rbf(ln * rbf(1.f + cm)) + sm);
  }
}

constexpr int NSPLIT = 8;
__global__ void __launch_bounds__(256) k_attn(const float* q, const unsigned short* Kc, const unsigned short* Vc, float* part) {
  __shared__ float qs[DH];
  __shared__ float sc[SK / NSPLIT];
  __shared__ float red[256];
  __shared__ float osum[2][DH];
  int s = blockIdx.x, h = blockIdx.y, b = blockIdx.z, t = threadIdx.x;
  int kvh = h / (HQ / HKV);
  if (t < DH) qs[t] = rbf(q[b * (HQ * DH) + h * DH + t]);
  __syncthreads();
  const unsigned short* Kb = Kc + ((size_t)(b * HKV + kvh) * SK + s * (SK / NSPLIT)) * DH;
  const unsigned short* Vb = Vc + ((size_t)(b * HKV + kvh) * SK + s * (SK / NSPLIT)) * DH;
  float mx = -INFINITY;
  for (int k = t; k < SK / NSPLIT; k += 256) {
    const v4u* kr = (const v4u*)(Kb + (size_t)k * DH);
    float a = 0.f;
    for (int j = 0; j < 16; ++j) {
      v4u w = kr[j];
      a += bflo(w.x) * qs[j * 8] + bfhi(w.x) * qs[j * 8 + 1] + bflo(w.y) * qs[j * 8 + 2] + bfhi(w.y) * qs[j * 8 + 3]
         + bflo(w.z) * qs[j * 8 + 4] + bfhi(w.z) * qs[j * 8 + 5] + bflo(w.w) * qs[j * 8 + 6] + bfhi(w.w) * qs[j * 8 + 7];
    }
    a = rbf(rbf(a) * 0.08838834764831845f);
    sc[k] = a;
    mx = fmaxf(mx, a);
  }
  red[t] = mx; __syncthreads();
  for (int o = 128; o > 0; o >>= 1) { if (t < o) red[t] = fmaxf(red[t], red[t + o]); __syncthreads(); }
  mx = red[0]; __syncthreads();
  float l = 0.f;
  for (int k = t; k < SK / NSPLIT; k += 256) { float p = __expf(sc[k] - mx); sc[k] = p; l += p; }
  red[t] = l; __syncthreads();
  for (int o = 128; o > 0; o >>= 1) { if (t < o) red[t] += red[t + o]; __syncthreads(); }
  l = red[0];
  int d = t & 127, half = t >> 7;
  float o = 0.f;
  for (int k = half; k < SK / NSPLIT; k += 2) o += sc[k] * bf2f(Vb[(size_t)k * DH + d]);
  osum[half][d] = o;
  __syncthreads();
  float* pp = part + ((size_t)(b * HQ + h) * NSPLIT + s) * (DH + 2);
  if (t < DH) pp[t] = osum[0][t] + osum[1][t];
  if (t == 0) { pp[DH] = mx; pp[DH + 1] = l; }
}

__global__ void k_comb(const float* part, float* o) {
  int bh = blockIdx.x, t = threadIdx.x;
  const float* pp = part + (size_t)bh * NSPLIT * (DH + 2);
  float M = -INFINITY;
  for (int s = 0; s < NSPLIT; ++s) M = fmaxf(M, pp[s * (DH + 2) + DH]);
  float L = 0.f, a = 0.f;
  for (int s = 0; s < NSPLIT; ++s) { float w = __expf(pp[s * (DH + 2) + DH] - M); L += w * pp[s * (DH + 2) + DH + 1]; a += w * pp[s * (DH + 2) + t]; }
  o[bh * DH + t] = rbf(a / L);
}

__global__ void k_res1(const unsigned short* x, const float* mod, const float* ao, float* x1) {
  int i = blockIdx.x * 256 + threadIdx.x;
  int b = i / D, d = i % D;
  x1[i] = rbf(bf2f(x[i]) + rbf(rbf(mod[b * 6 * D + 2 * D + d]) * rbf(ao[i])));
}
__global__ void k_glu(const float* g, const float* u, float* hm) {
  int i = blockIdx.x * 256 + threadIdx.x;
  float gg = rbf(g[i]);
  gg = gg > 0.f ? gg : 0.f;
  hm[i] = rbf(gg * rbf(u[i]));
}
__global__ void k_res2(const float* x1, const float* mod, const float* dn, __hip_bfloat16* out) {
  int i = blockIdx.x * 256 + threadIdx.x;
  int b = i / D, d = i % D;
  out[i] = __float2bfloat16(x1[i] + rbf(rbf(mod[b * 6 * D + 5 * D + d]) * rbf(dn[i])));
}

extern "C" void kernel_launch(void* const* d_in, const int* in_sizes, int n_in, void* d_out, int out_size, void* d_ws, size_t ws_size,
                              hipStream_t stream) {
  const unsigned short* x = (const unsigned short*)d_in[0];
  const unsigned short* Kc = (const unsigned short*)d_in[1];
  const unsigned short* Vc = (const unsigned short*)d_in[2];
  const unsigned short* wq = (const unsigned short*)d_in[3];
  const unsigned short* wo = (const unsigned short*)d_in[4];
  const unsigned short* wg = (const unsigned short*)d_in[6];
  const unsigned short* wu = (const unsigned short*)d_in[7];
  const unsigned short* wd = (const unsigned short*)d_in[8];
  const unsigned short* c = (const unsigned short*)d_in[9];
  const unsigned short* wm = (const unsigned short*)d_in[10];
  float* ws = (float*)d_ws;
  float* sc = ws;
  float* mod = sc + 8192;
  float* h = mod + 49152;
  float* q = h + 8192;
  float* part = q + 32768;
  float* o = part + 256 * 8 * 130;
  float* ao = o + 32768;
  float* x1 = ao + 8192;
  float* h2 = x1 + 8192;
  float* g = h2 + 8192;
  float* u = g + 28672;
  float* hm = u + 28672;
  float* dn = hm + 28672;
  k_silu<<<32, 256, 0, stream>>>(c, sc);
  gemv8<1024><<<96, 256, 0, stream>>>(sc, wm, mod, 6144);
  k_lnmod<<<8, 256, 0, stream>>>(nullptr, x, mod, 0, D, h);
  gemv8<1024><<<64, 256, 0, stream>>>(h, wq, q, 4096);
  k_attn<<<dim3(NSPLIT, HQ, NB), 256, 0, stream>>>(q, Kc, Vc, part);
  k_comb<<<256, 128, 0, stream>>>(part, o);
  float* tmp = dn + 8192;
  {
  }
  extern __global__ void k_slice(const float*, float*, int, int, int);
  extern __global__ void k_sum4(const float*, float*, int);
  for (int s = 0; s < 4; ++s) {
    k_slice<<<32, 256, 0, stream>>>(o, tmp + s * 8192, 4096, s * 1024, 1024);
    gemv8<1024><<<16, 256, 0, stream>>>(tmp + s * 8192, wo + (size_t)s * 1024 * 1024, tmp + 32768 + s * 8192, 1024);
  }
  k_sum4<<<32, 256, 0, stream>>>(tmp + 32768, ao, 4);
  k_res1<<<32, 256, 0, stream>>>(x, mod, ao, x1);
  k_lnmod<<<8, 256, 0, stream>>>(x1, nullptr, mod, 3 * D, 4 * D, h2);
  gemv8<1024><<<56, 256, 0, stream>>>(h2, wg, g, FF);
  gemv8<1024><<<56, 256, 0, stream>>>(h2, wu, u, FF);
  k_glu<<<112, 256, 0, stream>>>(g, u, hm);
  for (int s = 0; s < 4; ++s) {
    k_slice<<<28, 256, 0, stream>>>(hm, tmp + s * 8192, FF, s * 896, 896);
    gemv8<896><<<16, 256, 0, stream>>>(tmp + s * 8192, wd + (size_t)s * 896 * 1024, tmp + 32768 + s * 8192, 1024);
  }
  k_sum4<<<32, 256, 0, stream>>>(tmp + 32768, dn, 4);
  k_res2<<<32, 256, 0, stream>>>(x1, mod, dn, (__hip_bfloat16*)d_out);
}

__global__ void k_slice(const float* src, float* dst, int ld, int off, int w) {
  int i = blockIdx.x * 256 + threadIdx.x;
  int b = i / w, k = i % w;
  dst[i] = src[b * ld + off + k];
}
__global__ void k_sum4(const float* src, float* dst, int n) {
  int i = blockIdx.x * 256 + threadIdx.x;
  float s = 0.f;
  for (int j = 0; j < n; ++j) s += src[j * 8192 + i];
  dst[i] = s;
}
```

```cpp
#include <hip/hip_runtime.h>
#include <stdint.h>

typedef unsigned v4u __attribute__((ext_vector_type(4)));
typedef unsigned v2u __attribute__((ext_vector_type(2)));
typedef float v4f __attribute__((ext_vector_type(4)));
typedef float v2f __attribute__((ext_vector_type(2)));
typedef __bf16 v2bf __attribute__((ext_vector_type(2)));
typedef __bf16 v8bf __attribute__((ext_vector_type(8)));
typedef short v4s __attribute__((ext_vector_type(4)));
#define LAS __attribute__((address_space(3)))
typedef __amdgpu_buffer_rsrc_t rsrc_t;
#define DEV __device__ __forceinline__

#define KWIN 2
#define D1 8
#define D2 8
#define D3 8
#define D4 8
#define D5 8

constexpr int NWG = 256, NTHR = 768;
constexpr unsigned SENT = 0xFFFFFFFFu;
constexpr unsigned WS_EPOCH = 0, WS_FLAGS = 4096;
constexpr unsigned RS0 = 65536;
constexpr unsigned O_MOD = RS0;
constexpr unsigned O_Q = O_MOD + 98304;
constexpr unsigned O_ML = O_Q + 65536;
constexpr unsigned O_X1 = O_ML + 16384;
constexpr unsigned O_AO = O_X1 + 16384;
constexpr unsigned O_WP = O_AO + 1048576;
constexpr unsigned O_GP = O_WP + 1048576;
constexpr unsigned O_DP = O_GP + 28 * 32768;
constexpr unsigned RS_END = RS0 + NWG * 16384;
static_assert(O_DP + 256 * 28 * 128 <= RS_END, "ws map");
constexpr unsigned O_STAMP = RS_END;
constexpr unsigned WS_BYTES = O_STAMP + NWG * 12 * 16 * 8;
constexpr unsigned L_G = 0, L_W = 65536, L_DN = 98304;
constexpr unsigned L_ACT = 131072;
constexpr unsigned L_SB = L_ACT, L_CB = L_ACT + 4096;
constexpr unsigned L_A1 = L_ACT + 8192;
constexpr unsigned L_A4 = L_A1 + 2304;
constexpr unsigned L_QB = L_ACT + 16640;
constexpr unsigned L_A3 = L_QB + 2304;
constexpr unsigned L_MX = L_A3 + 4352;
constexpr unsigned L_LS = L_MX + 256;
constexpr unsigned L_FL = L_LS + 256;
constexpr unsigned L_ST = L_FL + 128;
constexpr unsigned L_END = L_ST + 12 * 16 * 8;
enum { F_INIT, F_GOK, F_CBAR, F_HEAD, F_Q, F_KMAX, F_KML, F_SIDE, F_WFREE, F_MERGE, F_ATT, F_T2, F_A3, F_GU };

DEV float bflo(unsigned u) { return __builtin_bit_cast(float, u << 16); }
DEV float bfhi(unsigned u) { return __builtin_bit_cast(float, u & 0xffff0000u); }
DEV unsigned pk(float a, float b) {
  v2f f = {a, b};
  unsigned u = __builtin_bit_cast(unsigned, __builtin_convertvector(f, v2bf));
  asm("" : "+v"(u));
  return u;
}
DEV float rbf(float a) { return bflo(pk(a, a)); }
DEV unsigned fbits(float a) { return __builtin_bit_cast(unsigned, a); }
DEV float bitsf(unsigned a) { return __builtin_bit_cast(float, a); }
DEV unsigned umax(unsigned a, unsigned b) { return a > b ? a : b; }
DEV unsigned canon(unsigned a) { return a < 0xFFFFFFFEu ? a : 0xFFFFFFFEu; }
DEV unsigned max4(v4u v) { return umax(umax(v.x, v.y), umax(v.z, v.w)); }
DEV v4u canon4(v4f v) { return (v4u){canon(fbits(v.x)), canon(fbits(v.y)), canon(fbits(v.z)), canon(fbits(v.w))}; }
DEV v4f sel4(bool c, v4f a, v4f b) { return (v4f){c ? a.x : b.x, c ? a.y : b.y, c ? a.z : b.z, c ? a.w : b.w}; }
DEV v4f asf4(v4u v) { return __builtin_bit_cast(v4f, v); }

template <class T> DEV T lds_ld(unsigned a) { return *(LAS T*)(size_t)a; }
template <class T> DEV void lds_st(unsigned a, T v) { *(LAS T*)(size_t)a = v; }
DEV void cfence() { asm volatile("" ::: "memory"); }
DEV void lds_signal(int f, int lane) {
  cfence();
  if (lane == 0) __hip_atomic_fetch_add((LAS unsigned*)(size_t)(L_FL + 4 * f), 1u, __ATOMIC_RELAXED, __HIP_MEMORY_SCOPE_WORKGROUP);
  cfence();
}
DEV unsigned lds_flag(int f) { return __builtin_amdgcn_readfirstlane(*(volatile LAS unsigned*)(size_t)(L_FL + 4 * f)); }
template <int SLEEP> DEV void lds_wait(int f, unsigned target) {
  cfence();
#pragma nounroll
  for (int i = 0; i < 200000; ++i) {
    if (lds_flag(f) >= target) break;
    if (SLEEP) __builtin_amdgcn_s_sleep(SLEEP);
  }
  cfence();
}
constexpr int POLL_MAX = 8000;

DEV rsrc_t mk(const void* p, unsigned bytes) { return __builtin_amdgcn_make_buffer_rsrc((void*)p, 0, bytes, 0x00020000); }
DEV v4u ld_nt(rsrc_t r, unsigned v, unsigned s) { return __builtin_amdgcn_raw_buffer_load_b128(r, v, s, 2); }
DEV v4u ld_dev(rsrc_t r, unsigned v, unsigned s) { return __builtin_amdgcn_raw_buffer_load_b128(r, v, s, 16); }
DEV v2u ld_dev2(rsrc_t r, unsigned v, unsigned s) { return __builtin_amdgcn_raw_buffer_load_b64(r, v, s, 16); }
DEV void st_dev(v4u d, rsrc_t r, unsigned v, unsigned s) { __builtin_amdgcn_raw_buffer_store_b128(d, r, v, s, 16); }
DEV void st_dev2(v2u d, rsrc_t r, unsigned v, unsigned s) { __builtin_amdgcn_raw_buffer_store_b64(d, r, v, s, 16); }
DEV void dma16(rsrc_t r, unsigned v, unsigned s, unsigned lds) {
  unsigned keep;
  asm volatile("s_mov_b32 %0, m0\n\ts_mov_b32 m0, %4\n\ts_nop 0\n\tbuffer_load_dwordx4 %1, %2, %3 offen nt lds\n\ts_mov_b32 m0, %0"
               : "=&s"(keep) : "v"(v), "s"(r), "s"(s), "s"(lds) : "memory");
}
#define VMWAIT(n) asm volatile("s_waitcnt vmcnt(" #n ")" ::: "memory")
DEV v8bf asbf(v4u v) { return __builtin_bit_cast(v8bf, v); }
DEV v4f mfma(v4u a, v4u b, v4f c) { return __builtin_amdgcn_mfma_f32_16x16x32_bf16(asbf(a), asbf(b), c, 0, 0, 0); }
DEV v2u trrd(unsigned a) { return __builtin_bit_cast(v2u, __builtin_amdgcn_ds_read_tr16_b64_v4i16((LAS v4s*)(size_t)a)); }
DEV v4u trfrag(unsigned a, unsigned step) { v2u x = trrd(a), y = trrd(a + step); return (v4u){x.x, x.y, y.x, y.y}; }

template <int CTRL> DEV float dpp(float x) { return __builtin_bit_cast(float, __builtin_amdgcn_mov_dpp(__builtin_bit_cast(int, x), CTRL, 0xf, 0xf, true)); }
DEV float x16sum(float x) { auto s = __builtin_amdgcn_permlane16_swap(fbits(x), fbits(x), false, false); return bitsf(s[0]) + bitsf(s[1]); }
DEV float x32sum(float x) { auto s = __builtin_amdgcn_permlane32_swap(fbits(x), fbits(x), false, false); return bitsf(s[0]) + bitsf(s[1]); }
DEV float x16max(float x) { auto s = __builtin_amdgcn_permlane16_swap(fbits(x), fbits(x), false, false); return fmaxf(bitsf(s[0]), bitsf(s[1])); }
DEV float x32max(float x) { auto s = __builtin_amdgcn_permlane32_swap(fbits(x), fbits(x), false, false); return fmaxf(bitsf(s[0]), bitsf(s[1])); }
DEV float wave_sum(float x) {
  x += dpp<0xB1>(x); x += dpp<0x4E>(x); x += dpp<0x141>(x); x += dpp<0x140>(x);
  return x32sum(x16sum(x));
}
DEV float fdiv(float a, float d, float rd) { float q = a * rd; return fmaf(fmaf(-q, d, a), rd, q); }
struct Stat { float mb, sd, rd; };
template <int N> DEV Stat ln_stat(const unsigned (&w)[N]) {
  float s = 0.f;
#pragma unroll
  for (int i = 0; i < N; ++i) s += bflo(w[i]) + bfhi(w[i]);
  const float m = wave_sum(s) * (1.f / 1024.f);
  float q = 0.f;
#pragma unroll
  for (int i = 0; i < N; ++i) { float a = bflo(w[i]) - m, b = bfhi(w[i]) - m; q = fmaf(a, a, q); q = fmaf(b, b, q); }
  const float var = rbf(wave_sum(q) * (1.f / 1024.f));
  Stat st;
  st.mb = rbf(m);
  st.sd = rbf(__builtin_amdgcn_sqrtf(rbf(var + 1e-5f)));
  st.rd = __builtin_amdgcn_rcpf(st.sd);
  return st;
}
DEV unsigned ln_pair(unsigned x, const Stat& st) {
  unsigned d = pk(bflo(x) - st.mb, bfhi(x) - st.mb);
  return pk(fdiv(bflo(d), st.sd, st.rd), fdiv(bfhi(d), st.sd, st.rd));
}
DEV unsigned onep(unsigned c) { return pk(1.f + bflo(c), 1.f + bfhi(c)); }
DEV unsigned mod_pair(unsigned ln, unsigned c1, unsigned s) {
  unsigned p = pk(bflo(ln) * bflo(c1), bfhi(ln) * bfhi(c1));
  return pk(bflo(p) + bflo(s), bfhi(p) + bfhi(s));
}
DEV unsigned res_pair(unsigned x, unsigned g, float a, float b) {
  unsigned y = pk(a, b);
  unsigned p = pk(bflo(g) * bflo(y), bfhi(g) * bfhi(y));
  return pk(bflo(x) + bflo(p), bfhi(x) + bfhi(p));
}
DEV float silu1(float v) { return v / (1.f + __expf(-v)); }

#define STAMP(k)
#define STAMP_DUMP()

DEV v4f unit_reduce(v4f a) {
#pragma unroll
  for (int i = 0; i < 4; ++i) { float t = a[i]; t += dpp<0x128>(t); a[i] = x32sum(x16sum(t)); }
  return a;
}

__global__ void __launch_bounds__(NTHR, 3)
fused_kernel(const void* __restrict__ px, const void* __restrict__ pk_, const void* __restrict__ pv, const void* __restrict__ pwq,
             const void* __restrict__ pwo, const void* __restrict__ pwg, const void* __restrict__ pwu, const void* __restrict__ pwd,
             const void* __restrict__ pc, const void* __restrict__ pwm, void* __restrict__ pout, void* __restrict__ pws) {
  const int lane = threadIdx.x & 63;
  const int wave = __builtin_amdgcn_readfirstlane(threadIdx.x >> 6);
  const int wg = blockIdx.x;
  const int n = lane & 15, g = lane >> 4;
  const rsrc_t rws = mk(pws, WS_BYTES);
  const int bk = wg & 31, sp = wg >> 5;
  const int ab = bk >> 2, kvh = bk & 3;
  const int role = wg < 96 ? 0 : wg < 160 ? 1 : 2;
  const bool ffn = wg >= 32;
  const int fi = wg - 32;
  const int gks = fi & 3, gct = fi >> 2;
  const int dj = fi & 7, dft = fi >> 3;
  const int oh = wg >> 3, oj = wg & 7;
  const int ub = wg >> 5, uu = wg & 31;

  if (threadIdx.x < 32) lds_st<unsigned>(L_FL + 4 * threadIdx.x, 0u);
  __syncthreads();
  STAMP(0);

  const int tq = (lane >> 2) & 3, tp = lane & 3;

  if (wave < 8) {
    const rsrc_t rk = mk((const char*)pk_ + (size_t)bk * 2097152 + sp * 262144 + wave * 32768, 32768);
    const rsrc_t rv = mk((const char*)pv + (size_t)bk * 2097152 + sp * 262144 + wave * 32768, 32768);
    if (wg < 32 || role == 1) lds_wait<2>(F_GOK, 1);
    v4u kf[8][4];
    const unsigned kvo = n * 256 + g * 16;
#pragma unroll
    for (int t = 0; t < 8; ++t) {
      if (t >= KWIN) {
        if (KWIN == 1) VMWAIT(0); else if (KWIN == 2) VMWAIT(4); else if (KWIN == 3) VMWAIT(8); else if (KWIN == 4) VMWAIT(12);
      }
#pragma unroll
      for (int s = 0; s < 4; ++s) kf[t][s] = ld_nt(rk, kvo + s * 64, t * 4096);
      cfence();
    }
    STAMP(1);
    if (ffn) {
      if (role != 2) lds_wait<2>(F_HEAD, 4);
      const rsrc_t rg = mk(pwg, 1024 * 3584 * 2), ru = mk(pwu, 1024 * 3584 * 2);
      const unsigned v0 = (lane >> 3) * 7168 + (((lane & 7) ^ (((lane >> 4) & 1) << 1)) << 4), v1 = v0 ^ 64;
#pragma unroll
      for (int i = 0; i < 4; ++i) {
        if (i == 0 || i == 2) { if (KWIN == 1) VMWAIT(0); else if (KWIN == 2) VMWAIT(4); else if (KWIN == 3) VMWAIT(8); else if (KWIN == 4) VMWAIT(12); }
        const unsigned so = (gks * 256 + wave * 32 + i * 8) * 7168 + gct * 128;
        dma16(rg, (i & 1) ? v1 : v0, so, L_G + (wave * 4 + i) * 1024);
        dma16(ru, (i & 1) ? v1 : v0, so, L_G + 32768 + (wave * 4 + i) * 1024);
      }
    }
    lds_wait<2>(F_Q, 1);
    STAMP(2);
    v4u qf[4];
#pragma unroll
    for (int s = 0; s < 4; ++s) qf[s] = lds_ld<v4u>(L_QB + (n & 7) * 288 + s * 64 + g * 16);
    const unsigned vvo = g * 1024 + n * 16;
    v4u vf[4][8];
    unsigned sc[8][2];
    float mx = -INFINITY;
    v4f prev;
#pragma unroll
    for (int t = 0; t < 9; ++t) {
      v4f a = {0.f, 0.f, 0.f, 0.f};
      if (t < 8) {
#pragma unroll
        for (int s = 0; s < 4; ++s) a = mfma(kf[t][s], qf[s], a);
      }
      if (t >= 1) {
#pragma unroll
        for (int r = 0; r < 2; ++r) {
          unsigned u = pk(prev[2 * r], prev[2 * r + 1]);
          u = pk(bflo(u) * 0.08838834764831845f, bfhi(u) * 0.08838834764831845f);
          sc[t - 1][r] = u;
          mx = fmaxf(mx, fmaxf(bflo(u), bfhi(u)));
        }
      }
      prev = a;
      if (t >= 2 && t <= 7) {
        __builtin_amdgcn_sched_barrier(0);
#pragma unroll
        for (int j = 0; j < 4; ++j) vf[(t - 2) >> 1][((t - 2) & 1) * 4 + j] = ld_nt(rv, vvo + j * 256, (t - 2) * 4096);
        __builtin_amdgcn_sched_barrier(0);
      }
    }
    STAMP(3);
    mx = x32max(x16max(mx));
    if (lane < 8) lds_st<float>(L_MX + wave * 32 + lane * 4, mx);
    lds_signal(F_KMAX, lane);
    lds_wait<1>(F_KMAX, 8);
#pragma unroll
    for (int w = 0; w < 8; ++w) mx = fmaxf(mx, lds_ld<float>(L_MX + w * 32 + (n & 7) * 4));
    float ls = 0.f;
    v4u pf[4];
#pragma unroll
    for (int t = 0; t < 8; ++t) {
      float p0 = __expf(bflo(sc[t][0]) - mx), p1 = __expf(bfhi(sc[t][0]) - mx), p2 = __expf(bflo(sc[t][1]) - mx), p3 = __expf(bfhi(sc[t][1]) - mx);
      ls += (p0 + p1) + (p2 + p3);
      pf[t >> 1][(t & 1) * 2] = pk(p0, p1);
      pf[t >> 1][(t & 1) * 2 + 1] = pk(p2, p3);
    }
    ls = x32sum(x16sum(ls));
    if (lane < 8) lds_st<float>(L_LS + wave * 32 + lane * 4, ls);
    lds_signal(F_KML, lane);
    STAMP(4);
    v4f acc[8];
#pragma unroll
    for (int i = 0; i < 8; ++i) acc[i] = (v4f){0.f, 0.f, 0.f, 0.f};
#pragma unroll
    for (int p = 0; p < 4; ++p) {
#pragma unroll
      for (int i = 0; i < 8; ++i) {
        v4u a;
#pragma unroll
        for (int e = 0; e < 4; ++e) a[e] = __builtin_amdgcn_perm(vf[p][2 * e + 1][i >> 1], vf[p][2 * e][i >> 1], (i & 1) ? 0x07060302u : 0x05040100u);
        acc[i] = mfma(a, pf[p], acc[i]);
      }
      if (p == 0) {
        __builtin_amdgcn_sched_barrier(0);
#pragma unroll
        for (int j = 0; j < 8; ++j) vf[3][j] = ld_nt(rv, vvo + (j & 3) * 256, (6 + (j >> 2)) * 4096);
        __builtin_amdgcn_sched_barrier(0);
      }
      if (p == 2) lds_wait<1>(F_WFREE, 4);
    }
    STAMP(5);
    {
      const int hd = n & 7, hi = n >> 3;
#pragma unroll
      for (int i = 0; i < 4; ++i) {
        const v4f v = sel4(hi, acc[i + 4], acc[i]);
        lds_st<v4f>(L_W + wave * 4096 + hd * 512 + (g * 8 + ((i + 4 * hi) ^ hd)) * 16, v);
      }
      lds_signal(F_MERGE, lane);
      lds_wait<0>(F_MERGE, 8);
      const int ci = lane & 31;
      const unsigned a0 = L_W + (lane >> 5) * 16384 + wave * 512 + (ci ^ wave) * 16;
      v4f s0 = lds_ld<v4f>(a0), s1 = lds_ld<v4f>(a0 + 4096), s2 = lds_ld<v4f>(a0 + 8192), s3 = lds_ld<v4f>(a0 + 12288);
      v4f s = (s0 + s1) + (s2 + s3);
#pragma unroll
      for (int i = 0; i < 4; ++i) s[i] = x32sum(s[i]);
      if (lane < 32) st_dev(canon4(s), rws, ci * 16, O_AO + (kvh * 8 + wave) * 32768 + sp * 4096 + ab * 512);
      lds_signal(F_ATT, lane);
    }
    STAMP(6);
    if (ffn) {
      v4u wf[8];
      {
        const int nt = wave & 3;
        const unsigned sw = ((tq >> 1) & 1) | ((g & 1) << 1);
        const unsigned base = L_G + (wave >> 2) * 32768 + (8 * g + tq) * 128 + (((2 * nt + (tp >> 1)) ^ (sw << 1)) << 4) + 8 * (tp & 1);
#pragma unroll
        for (int kb = 0; kb < 8; ++kb) wf[kb] = trfrag(base + kb * 4096, 512);
      }
      lds_wait<1>(F_SIDE, 4);
      const v2u sbv = lds_ld<v2u>(L_SB + wave * 512 + lane * 8);
      v2u cbv = lds_ld<v2u>(L_CB + wave * 512 + lane * 8);
      cbv.x = onep(cbv.x); cbv.y = onep(cbv.y);
      lds_wait<1>(F_T2, 1);
      if (D3) __builtin_amdgcn_s_sleep(D3);
      STAMP(7);
      unsigned xr[8];
#pragma nounroll
      for (int it = 0; it < POLL_MAX; ++it) {
        cfence();
        unsigned m = 0;
#pragma unroll
        for (int q = 0; q < 4; ++q) {
          v2u v = ld_dev2(rws, lane * 8, O_X1 + wave * 2048 + ((gks + q) & 3) * 512);
          xr[2 * q] = v.x; xr[2 * q + 1] = v.y;
          m = umax(m, umax(v.x, v.y));
        }
        if (!__any(m == SENT)) break;
      }
      STAMP(8);
      const Stat st = ln_stat<8>(xr);
      v2u h2;
      h2.x = mod_pair(ln_pair(xr[0], st), cbv.x, sbv.x);
      h2.y = mod_pair(ln_pair(xr[1], st), cbv.y, sbv.y);
      lds_st<v2u>(L_A3 + wave * 544 + lane * 8, h2);
      lds_signal(F_A3, lane);
      lds_wait<0>(F_A3, 8);
      v4f d0 = {0.f, 0.f, 0.f, 0.f}, d1 = d0;
#pragma unroll
      for (int kb = 0; kb < 8; kb += 2) {
        d0 = mfma(wf[kb], lds_ld<v4u>(L_A3 + (n & 7) * 544 + kb * 64 + g * 16), d0);
        d1 = mfma(wf[kb + 1], lds_ld<v4u>(L_A3 + (n & 7) * 544 + kb * 64 + 64 + g * 16), d1);
      }
      d0 += d1;
      if (n < 8)
        st_dev(canon4(d0), rws, n * 64 + g * 16,
               O_GP + (gct >> 1) * 32768 + ((gct & 1) * 2 + ((wave & 3) >> 1)) * 8192 + gks * 2048 + (wave >> 2) * 1024 + (wave & 1) * 512);
      lds_signal(F_GU, lane);
      STAMP(9);
    }
    STAMP_DUMP();
    return;
  }

  const int cw = wave - 8;
  __builtin_amdgcn_s_setprio(2);
  unsigned cb_n = 0;
  v2u ep;
  v4u in[4];
  {
    const unsigned z = 0;
    asm volatile("buffer_load_dwordx2 %0, %1, %2, 0 offen sc1" : "=v"(ep) : "v"(z), "s"(rws) : "memory");
    const rsrc_t ri = mk(role == 0 ? pc : px, 16384);
    const unsigned vo = cw * 4096 + lane * 16;
    asm volatile("buffer_load_dwordx4 %0, %4, %5, 0 offen\n\tbuffer_load_dwordx4 %1, %4, %5, 0 offen offset:1024\n\t"
                 "buffer_load_dwordx4 %2, %4, %5, 0 offen offset:2048\n\tbuffer_load_dwordx4 %3, %4, %5, 0 offen offset:3072"
                 : "=&v"(in[0]), "=&v"(in[1]), "=&v"(in[2]), "=&v"(in[3]) : "v"(vo), "s"(ri) : "memory");
  }
  if (cw == 0) {
    const v4u sv = {SENT, SENT, SENT, SENT};
#pragma unroll
    for (int i = 0; i < 16; ++i) st_dev(sv, rws, lane * 16, RS0 + wg * 16384 + i * 1024);
  }
  cfence();
  if (role != 2) {
    const unsigned rowb = role == 0 ? 12288 : 8192;
    const rsrc_t rh = mk(role == 0 ? pwm : pwq, 1024 * rowb);
    const unsigned col = (role == 0 ? wg : wg - 96) * 128;
    const unsigned v0 = (lane >> 3) * rowb + (((lane & 7) ^ (((lane >> 4) & 1) << 1)) << 4), v1 = v0 ^ 64;
#pragma unroll
    for (int k = 0; k < 32; ++k) dma16(rh, (k & 1) ? v1 : v0, (cw * 256 + k * 8) * rowb + col, (cw * 32 + k) * 1024);
    VMWAIT(32);
  } else {
    VMWAIT(0);
  }
  asm volatile("" : "+v"(ep), "+v"(in[0]), "+v"(in[1]), "+v"(in[2]), "+v"(in[3]) : : "memory");
  const unsigned long long tag64 = (((unsigned long long)ep.y << 32) | ep.x) + 1ull;
  const v2u tag = {(unsigned)__builtin_amdgcn_readfirstlane((unsigned)tag64), (unsigned)__builtin_amdgcn_readfirstlane((unsigned)(tag64 >> 32))};
  if (cw == 0 && lane == 0) st_dev2(tag, rws, 0, WS_FLAGS + wg * 8);
  STAMP(1);
  if (role == 0) {
#pragma unroll
    for (int k = 0; k < 4; ++k) {
      v4u o;
#pragma unroll
      for (int e = 0; e < 4; ++e) o[e] = pk(silu1(bflo(in[k][e])), silu1(bfhi(in[k][e])));
      lds_st<v4u>(L_ACT + (2 * cw + (k >> 1)) * 2080 + (k & 1) * 1024 + lane * 16, o);
    }
  } else if (role == 1) {
#pragma unroll
    for (int r = 0; r < 2; ++r) {
      const unsigned w[8] = {in[2 * r].x, in[2 * r].y, in[2 * r].z, in[2 * r].w, in[2 * r + 1].x, in[2 * r + 1].y, in[2 * r + 1].z, in[2 * r + 1].w};
      const Stat st = ln_stat<8>(w);
#pragma unroll
      for (int e = 0; e < 4; ++e) { in[2 * r][e] = ln_pair(w[e], st); in[2 * r + 1][e] = ln_pair(w[4 + e], st); }
    }
  }
  if (cw) __builtin_amdgcn_s_sleep(12);
  if (cw > 1) __builtin_amdgcn_s_sleep(12);
  if (cw > 2) __builtin_amdgcn_s_sleep(12);
#pragma nounroll
  for (int it = 0; it < POLL_MAX; ++it) {
    cfence();
    if (lds_flag(F_INIT)) break;
    v4u a = ld_dev(rws, lane * 16, WS_FLAGS), b = ld_dev(rws, lane * 16, WS_FLAGS + 1024);
    if (cw == 3 && it == 0 && wg < 32) lds_signal(F_GOK, lane);
    bool ok = a.x == tag.x && a.y == tag.y && a.z == tag.x && a.w == tag.y && b.x == tag.x && b.y == tag.y && b.z == tag.x && b.w == tag.y;
    if (__all(ok)) { lds_signal(F_INIT, lane); break; }
  }
  if (wg < 32) lds_signal(F_GOK, lane);
  if (wg == 0 && cw == 0 && lane == 0) st_dev2(tag, rws, 0, WS_EPOCH);
  STAMP(2);
  if (role == 1) {
    v4u sa[4], ca[4];
#pragma nounroll
    for (int it = 0; it < POLL_MAX; ++it) {
      cfence();
      unsigned m = 0;
#pragma unroll
      for (int k = 0; k < 4; ++k) {
        const unsigned so = O_MOD + (2 * cw + (k >> 1)) * 12288 + (k & 1) * 1024;
        sa[k] = ld_dev(rws, lane * 16, so);
        ca[k] = ld_dev(rws, lane * 16, so + 2048);
      }
#pragma unroll
      for (int k = 0; k < 4; ++k) m = umax(m, umax(max4(sa[k]), max4(ca[k])));
      if (!__any(m == SENT)) break;
      __builtin_amdgcn_s_sleep(2);
    }
    lds_signal(F_GOK, lane);
#pragma unroll
    for (int k = 0; k < 4; ++k) {
      v4u o;
#pragma unroll
      for (int e = 0; e < 4; ++e) o[e] = mod_pair(in[k][e], onep(ca[k][e]), sa[k][e]);
      lds_st<v4u>(L_ACT + (2 * cw + (k >> 1)) * 2080 + (k & 1) * 1024 + lane * 16, o);
    }
  }
  STAMP(3);
  if (role != 2) {
    VMWAIT(0);
    lds_signal(F_CBAR, lane); cb_n += 4; lds_wait<0>(F_CBAR, cb_n);
    const unsigned sw = ((tq >> 1) & 1) | ((g & 1) << 1);
    const unsigned base = (8 * g + tq) * 128 + (((2 * cw + (tp >> 1)) ^ (sw << 1)) << 4) + 8 * (tp & 1);
    const unsigned bb = L_ACT + (n & 7) * 2080 + g * 16;
    v4f d0 = {0.f, 0.f, 0.f, 0.f}, d1 = d0;
#pragma unroll
    for (int kb = 0; kb < 32; kb += 2) {
      d0 = mfma(trfrag(base + kb * 4096, 512), lds_ld<v4u>(bb + kb * 64), d0);
      d1 = mfma(trfrag(base + kb * 4096 + 4096, 512), lds_ld<v4u>(bb + kb * 64 + 64), d1);
    }
    d0 += d1;
    const v2u o = {canon(pk(d0[0], d0[1])), canon(pk(d0[2], d0[3]))};
    const unsigned rowo = role == 0 ? 12288 : 8192;
    if (n < 8) st_dev2(o, rws, n * rowo + g * 8, (role == 0 ? O_MOD + wg * 128 : O_Q + (wg - 96) * 128) + cw * 32);
    lds_signal(F_HEAD, lane);
  }
  STAMP(4);
  if (role != 2) {
    if (cw) __builtin_amdgcn_s_sleep(12);
    if (cw > 1) __builtin_amdgcn_s_sleep(12);
    if (cw > 2) __builtin_amdgcn_s_sleep(12);
  }
#pragma nounroll
  for (int it = 0; it < POLL_MAX; ++it) {
    cfence();
    if (lds_flag(F_Q)) break;
    v4u a = ld_dev(rws, lane * 16, O_Q + ab * 8192 + kvh * 2048), b = ld_dev(rws, lane * 16, O_Q + ab * 8192 + kvh * 2048 + 1024);
    if (!__any(umax(max4(a), max4(b)) == SENT)) {
      lds_st<v4u>(L_QB + g * 288 + n * 16, a);
      lds_st<v4u>(L_QB + (4 + g) * 288 + n * 16, b);
      lds_signal(F_Q, lane);
      break;
    }
    __builtin_amdgcn_s_sleep(4);
  }
  STAMP(5);
  if (role != 2) lds_wait<1>(F_HEAD, 4);
  {
    const rsrc_t ro = mk(pwo, 4096 * 2048), rd = mk(pwd, 3584 * 2048);
    const unsigned c0 = ((n ^ (g << 1)) << 4), c1 = c0 ^ 128;
#pragma unroll
    for (int i = 0; i < 8; ++i)
      dma16(ro, g * 8 * 2048 + ((i & 2) ? c1 : c0), (oh * 128 + cw * 32 + i) * 2048 + oj * 256, L_W + (cw * 8 + i) * 1024);
    if (ffn) {
#pragma unroll
      for (int i = 0; i < 8; ++i)
        dma16(rd, g * 2048 + ((i & 2) ? c1 : c0), (dft * 128 + cw * 32 + i * 4) * 2048 + dj * 256, L_DN + (cw * 8 + i) * 1024);
    }
  }
  v2u xu, gau, gbu;
  {
    const rsrc_t rx = mk(px, 16384);
    xu = __builtin_amdgcn_raw_buffer_load_b64(rx, (lane & 7) * 8, ub * 2048 + uu * 64, 0);
    v4u sb, cb;
    const unsigned vo = (lane >> 5) * 12288 + (lane & 31) * 16;
    const int ks = ffn ? gks : 0;
#pragma nounroll
    for (int it = 0; it < POLL_MAX; ++it) {
      cfence();
      sb = ld_dev(rws, vo, O_MOD + 2 * cw * 12288 + 3 * 2048 + ks * 512);
      cb = ld_dev(rws, vo, O_MOD + 2 * cw * 12288 + 4 * 2048 + ks * 512);
      gau = ld_dev2(rws, (lane & 7) * 8, O_MOD + ub * 12288 + 2 * 2048 + uu * 64);
      gbu = ld_dev2(rws, (lane & 7) * 8, O_MOD + ub * 12288 + 5 * 2048 + uu * 64);
      unsigned m = umax(umax(max4(sb), max4(cb)), umax(umax(gau.x, gau.y), umax(gbu.x, gbu.y)));
      if (!__any(m == SENT)) break;
      __builtin_amdgcn_s_sleep(8);
    }
    lds_st<v4u>(L_SB + cw * 1024 + lane * 16, sb);
    lds_st<v4u>(L_CB + cw * 1024 + lane * 16, cb);
    lds_signal(F_SIDE, lane);
  }
  VMWAIT(0);
  lds_signal(F_CBAR, lane); cb_n += 4; lds_wait<1>(F_CBAR, cb_n);
  v4u wof[4][2], wdf[4][2];
  {
    const unsigned sw = (tq | ((g & 1) << 2)) << 1;
#pragma unroll
    for (int e = 0; e < 2; ++e) {
      const unsigned base = (8 * g + tq) * 256 + (((2 * (2 * cw + e) + (tp >> 1)) ^ sw) << 4) + 8 * (tp & 1);
#pragma unroll
      for (int kb = 0; kb < 4; ++kb) {
        wof[kb][e] = trfrag(L_W + base + kb * 8192, 1024);
        wdf[kb][e] = trfrag(L_DN + base + kb * 8192, 1024);
      }
    }
    asm volatile("s_waitcnt lgkmcnt(0)" ::: "memory");
    lds_signal(F_WFREE, lane);
  }
  STAMP(6);
  lds_wait<4>(F_KML, 8);
  if (cw == 3) {
    float m = -INFINITY, l = 0.f;
#pragma unroll
    for (int w = 0; w < 8; ++w) { m = fmaxf(m, lds_ld<float>(L_MX + w * 32 + (lane & 7) * 4)); l += lds_ld<float>(L_LS + w * 32 + (lane & 7) * 4); }
    if (lane < 8) st_dev2((v2u){canon(fbits(m)), canon(fbits(l))}, rws, lane * 512, O_ML + kvh * 4096 + ab * 64 + sp * 8);
  }
  float wgt[8];
  {
    v4u ml[4];
#pragma nounroll
    for (int it = 0; it < POLL_MAX; ++it) {
      __builtin_amdgcn_s_sleep(16);
      cfence();
      unsigned m = 0;
#pragma unroll
      for (int k = 0; k < 4; ++k) { ml[k] = ld_dev(rws, (n & 7) * 64 + k * 16, O_ML + oh * 512); }
#pragma unroll
      for (int k = 0; k < 4; ++k) m = umax(m, max4(ml[k]));
      if (!__any(m == SENT)) break;
    }
    float M = -INFINITY;
#pragma unroll
    for (int k = 0; k < 4; ++k) M = fmaxf(M, fmaxf(bitsf(ml[k].x), bitsf(ml[k].z)));
    float L = 0.f;
#pragma unroll
    for (int k = 0; k < 4; ++k) {
      wgt[2 * k] = __expf(bitsf(ml[k].x) - M); wgt[2 * k + 1] = __expf(bitsf(ml[k].z) - M);
      L = fmaf(wgt[2 * k], bitsf(ml[k].y), L); L = fmaf(wgt[2 * k + 1], bitsf(ml[k].w), L);
    }
    const float rl = 1.f / L;
#pragma unroll
    for (int k = 0; k < 8; ++k) wgt[k] *= rl;
  }
  STAMP(7);
  const bool hi = n >= 8;
  lds_wait<1>(F_ATT, 8);
  if (D1) __builtin_amdgcn_s_sleep(D1);
  {
    v4u a[8];
    const unsigned vo = (n & 7) * 512 + (cw * 8 + (n >> 3) * 4 + g) * 16;
#pragma nounroll
    for (int it = 0; it < POLL_MAX; ++it) {
      cfence();
      unsigned m = 0;
#pragma unroll
      for (int s = 0; s < 8; ++s) a[s] = ld_dev(rws, vo, O_AO + oh * 32768 + s * 4096);
#pragma unroll
      for (int s = 0; s < 8; ++s) m = umax(m, max4(a[s]));
      if (!__any(m == SENT)) break;
    }
    STAMP(8);
    v4f o = asf4(a[0]) * wgt[0];
#pragma unroll
    for (int s = 1; s < 8; ++s) o += asf4(a[s]) * wgt[s];
    lds_st<v2u>(L_A1 + (n & 7) * 288 + cw * 64 + ((n >> 3) * 4 + g) * 8, (v2u){pk(o[0], o[1]), pk(o[2], o[3])});
    lds_signal(F_CBAR, lane); cb_n += 4; lds_wait<0>(F_CBAR, cb_n);
    v4f d0 = {0.f, 0.f, 0.f, 0.f}, d1 = d0;
#pragma unroll
    for (int kb = 0; kb < 4; ++kb) {
      const v4u b = lds_ld<v4u>(L_A1 + (n & 7) * 288 + kb * 64 + g * 16);
      d0 = mfma(wof[kb][0], b, d0);
      d1 = mfma(wof[kb][1], b, d1);
    }
    st_dev(canon4(sel4(hi, d1, d0)), rws, (n & 7) * 131072 + (n >> 3) * 64 + g * 16, O_WP + (oj * 4 + cw) * 4096 + oh * 128);
  }
  STAMP(9);
  v2u x1u = {0u, 0u};
  if (cw == 0) {
    if (D2) __builtin_amdgcn_s_sleep(D2);
    v4u a[4];
#pragma nounroll
    for (int it = 0; it < POLL_MAX; ++it) {
      cfence();
      unsigned m = 0;
#pragma unroll
      for (int k = 0; k < 4; ++k) a[k] = ld_dev(rws, lane * 16, O_WP + wg * 4096 + k * 1024);
#pragma unroll
      for (int k = 0; k < 4; ++k) m = umax(m, max4(a[k]));
      if (!__any(m == SENT)) break;
    }
    STAMP(10);
    const v4f s = unit_reduce((asf4(a[0]) + asf4(a[1])) + (asf4(a[2]) + asf4(a[3])));
    x1u.x = canon(res_pair(xu.x, gau.x, s[0], s[1]));
    x1u.y = canon(res_pair(xu.y, gau.y, s[2], s[3]));
    if (lane < 8) st_dev2(x1u, rws, lane * 8, O_X1 + ub * 2048 + uu * 64);
    lds_signal(F_T2, lane);
    STAMP(11);
  }
  if (ffn) {
    lds_wait<1>(F_GU, 8);
    if (D4) __builtin_amdgcn_s_sleep(D4);
    v4u a[8];
    const unsigned vo = (n >> 3) * 512 + (n & 7) * 64 + g * 16;
#pragma nounroll
    for (int it = 0; it < POLL_MAX; ++it) {
      cfence();
      unsigned m = 0;
#pragma unroll
      for (int s = 0; s < 8; ++s) a[s] = ld_dev(rws, vo, O_GP + dft * 32768 + cw * 8192 + s * 1024);
#pragma unroll
      for (int s = 0; s < 8; ++s) m = umax(m, max4(a[s]));
      if (!__any(m == SENT)) break;
    }
    STAMP(12);
    const v4f gt = (asf4(a[0]) + asf4(a[2])) + (asf4(a[4]) + asf4(a[6]));
    const v4f up = (asf4(a[1]) + asf4(a[3])) + (asf4(a[5]) + asf4(a[7]));
    const unsigned g0 = pk(gt[0], gt[1]), g1 = pk(gt[2], gt[3]), u0 = pk(up[0], up[1]), u1 = pk(up[2], up[3]);
    auto relu = [](float v) { return v > 0.f ? v : v * 0.f; };
    const v2u hv = {pk(relu(bflo(g0)) * bflo(u0), relu(bfhi(g0)) * bfhi(u0)), pk(relu(bflo(g1)) * bflo(u1), relu(bfhi(g1)) * bfhi(u1))};
    lds_st<v2u>(L_A4 + (n & 7) * 288 + cw * 64 + ((n >> 3) * 4 + g) * 8, hv);
    lds_signal(F_CBAR, lane); cb_n += 4; lds_wait<0>(F_CBAR, cb_n);
    v4f d0 = {0.f, 0.f, 0.f, 0.f}, d1 = d0;
#pragma unroll
    for (int kb = 0; kb < 4; ++kb) {
      const v4u b = lds_ld<v4u>(L_A4 + (n & 7) * 288 + kb * 64 + g * 16);
      d0 = mfma(wdf[kb][0], b, d0);
      d1 = mfma(wdf[kb][1], b, d1);
    }
    st_dev(canon4(sel4(hi, d1, d0)), rws, (n & 7) * (32 * 3584) + (n >> 3) * 64 + g * 16, O_DP + (dj * 4 + cw) * 3584 + dft * 128);
    STAMP(13);
  }
  if (cw == 0) {
    if (D5) __builtin_amdgcn_s_sleep(D5);
    v4u a[4];
#pragma nounroll
    for (int it = 0; it < POLL_MAX; ++it) {
      cfence();
      unsigned m = 0;
#pragma unroll
      for (int k = 0; k < 3; ++k) a[k] = ld_dev(rws, lane * 16, O_DP + wg * 3584 + k * 1024);
      a[3] = ld_dev(rws, (lane & 31) * 16, O_DP + wg * 3584 + 3072);
#pragma unroll
      for (int k = 0; k < 4; ++k) m = umax(m, max4(a[k]));
      if (!__any(m == SENT)) break;
    }
    STAMP(14);
    v4f t = asf4(a[3]);
    if (lane >= 32) t = (v4f){0.f, 0.f, 0.f, 0.f};
    const v4f s = unit_reduce((asf4(a[0]) + asf4(a[1])) + (asf4(a[2]) + t));
    const v2u o = {res_pair(x1u.x, gbu.x, s[0], s[1]), res_pair(x1u.y, gbu.y, s[2], s[3])};
    if (lane < 8) __builtin_amdgcn_raw_buffer_store_b64(o, mk(pout, 16384), lane * 8, ub * 2048 + uu * 64, 0);
    STAMP(15);
  }
  STAMP_DUMP();
}

extern "C" void kernel_launch(void* const* d_in, const int* in_sizes, int n_in, void* d_out, int out_size, void* d_ws, size_t ws_size,
                              hipStream_t stream) {
  (void)in_sizes; (void)n_in; (void)out_size; (void)ws_size;
  (void)hipFuncSetAttribute((const void*)fused_kernel, hipFuncAttributeMaxDynamicSharedMemorySize, L_END);
  fused_kernel<<<NWG, NTHR, L_END, stream>>>(d_in[0], d_in[1], d_in[2], d_in[3], d_in[4], d_in[6], d_in[7], d_in[8], d_in[9], d_in[10],
                                             d_out, d_ws);
}
```

```cpp
#include <hip/hip_runtime.h>
#include <stdint.h>

typedef unsigned v4u __attribute__((ext_vector_type(4)));
typedef unsigned v2u __attribute__((ext_vector_type(2)));
typedef float v4f __attribute__((ext_vector_type(4)));
typedef float v2f __attribute__((ext_vector_type(2)));
typedef __bf16 v2bf __attribute__((ext_vector_type(2)));
typedef __bf16 v8bf __attribute__((ext_vector_type(8)));
typedef short v4s __attribute__((ext_vector_type(4)));
#define LAS __attribute__((address_space(3)))
typedef __amdgpu_buffer_rsrc_t rsrc_t;
#define DEV __device__ __forceinline__

#define KWIN 2
#define DQ0 100
#define DQS 36
#define D1 8
#define D2 8
#define D3 8
#define D4 8
#define D5 8
#define PS 1

constexpr int NWG = 256, NTHR = 768;
constexpr unsigned SENT = 0xFFFFFFFFu;
constexpr unsigned WS_EPOCH = 0;
constexpr unsigned O_MOD = 65536;
constexpr unsigned O_Q = O_MOD + 196608;
constexpr unsigned O_ML = O_Q + 131072;
constexpr unsigned O_X1 = O_ML + 16384;
constexpr unsigned O_AO = O_X1 + 16384;
constexpr unsigned O_WP = O_AO + 1048576;
constexpr unsigned O_GP = O_WP + 1048576;
constexpr unsigned O_DP = O_GP + 28 * 32768;
constexpr unsigned O_STAMP = 65536 + 256 * 16384 + 1048576;
static_assert(O_DP + 256 * 28 * 128 <= O_STAMP, "ws map");
constexpr unsigned WS_BYTES = O_STAMP + NWG * 12 * 16 * 8;
constexpr unsigned L_G = 0, L_W = 65536, L_DN = 98304;
constexpr unsigned L_ACT = 131072;
constexpr unsigned L_SB = L_ACT, L_CB = L_ACT + 4096;
constexpr unsigned L_A1 = L_ACT + 8192;
constexpr unsigned L_A4 = L_A1 + 2304;
constexpr unsigned L_QB = L_ACT + 16640;
constexpr unsigned L_A3 = L_QB + 2304;
constexpr unsigned L_MX = L_A3 + 4352;
constexpr unsigned L_LS = L_MX + 256;
constexpr unsigned L_WT = L_LS + 256;
constexpr unsigned L_FL = L_WT + 256;
constexpr unsigned L_ST = L_FL + 128;
constexpr unsigned L_END = L_ST + 12 * 16 * 8;
enum { F_H, F_WQ, F_CBAR, F_HEAD, F_Q, F_KMAX, F_KML, F_SIDE, F_WFREE, F_MERGE, F_ATT, F_T2, F_A3, F_GU, F_WT };

DEV float bflo(unsigned u) { return __builtin_bit_cast(float, u << 16); }
DEV float bfhi(unsigned u) { return __builtin_bit_cast(float, u & 0xffff0000u); }
DEV unsigned pk(float a, float b) {
  v2f f = {a, b};
  unsigned u = __builtin_bit_cast(unsigned, __builtin_convertvector(f, v2bf));
  asm("" : "+v"(u));
  return u;
}
DEV float rbf(float a) { return bflo(pk(a, a)); }
DEV unsigned fbits(float a) { return __builtin_bit_cast(unsigned, a); }
DEV float bitsf(unsigned a) { return __builtin_bit_cast(float, a); }
DEV unsigned umax(unsigned a, unsigned b) { return a > b ? a : b; }
DEV unsigned canon(unsigned a) { return a < 0xFFFFFFFEu ? a : 0xFFFFFFFEu; }
DEV v4f sel4(bool c, v4f a, v4f b) { return (v4f){c ? a.x : b.x, c ? a.y : b.y, c ? a.z : b.z, c ? a.w : b.w}; }
DEV v4f asf4(v4u v) { return __builtin_bit_cast(v4f, v); }
DEV v4u tagbytes(unsigned T) { return (v4u){T & 255u, (T >> 8) & 255u, (T >> 16) & 255u, T >> 24}; }
DEV v4u tag4(v4f v, v4u tb) {
  return (v4u){(fbits(v.x) & ~255u) | tb.x, (fbits(v.y) & ~255u) | tb.y, (fbits(v.z) & ~255u) | tb.z, (fbits(v.w) & ~255u) | tb.w};
}

template <class T> DEV T lds_ld(unsigned a) { return *(LAS T*)(size_t)a; }
template <class T> DEV void lds_st(unsigned a, T v) { *(LAS T*)(size_t)a = v; }
DEV void cfence() { asm volatile("" ::: "memory"); }
DEV void lds_signal(int f, int lane) {
  cfence();
  if (lane == 0) __hip_atomic_fetch_add((LAS unsigned*)(size_t)(L_FL + 4 * f), 1u, __ATOMIC_RELAXED, __HIP_MEMORY_SCOPE_WORKGROUP);
  cfence();
}
DEV unsigned lds_flag(int f) { return __builtin_amdgcn_readfirstlane(*(volatile LAS unsigned*)(size_t)(L_FL + 4 * f)); }
template <int SLEEP> DEV void lds_wait(int f, unsigned target) {
  cfence();
#pragma nounroll
  for (int i = 0; i < 200000; ++i) {
    if (lds_flag(f) >= target) break;
    if (SLEEP) __builtin_amdgcn_s_sleep(SLEEP);
  }
  cfence();
}
constexpr int POLL_MAX = 8000;

DEV rsrc_t mk(const void* p, unsigned bytes) { return __builtin_amdgcn_make_buffer_rsrc((void*)p, 0, bytes, 0x00020000); }
DEV v4u ld_nt(rsrc_t r, unsigned v, unsigned s) { return __builtin_amdgcn_raw_buffer_load_b128(r, v, s, 2); }
DEV v4u ld_dev(rsrc_t r, unsigned v, unsigned s) { return __builtin_amdgcn_raw_buffer_load_b128(r, v, s, 16); }
DEV v2u ld_dev2(rsrc_t r, unsigned v, unsigned s) { return __builtin_amdgcn_raw_buffer_load_b64(r, v, s, 16); }
DEV void st_dev(v4u d, rsrc_t r, unsigned v, unsigned s) {
  asm volatile("buffer_store_dwordx4 %0, %1, %2, %3 offen sc1\n\ts_nop 1" : : "v"(d), "v"(v), "s"(r), "s"(s) : "memory");
}
DEV void st_dev2(v2u d, rsrc_t r, unsigned v, unsigned s) {
  asm volatile("buffer_store_dwordx2 %0, %1, %2, %3 offen sc1" : : "v"(d), "v"(v), "s"(r), "s"(s) : "memory");
}
DEV void dma16(rsrc_t r, unsigned v, unsigned s, unsigned lds) {
  unsigned keep;
  asm volatile("s_mov_b32 %0, m0\n\ts_mov_b32 m0, %4\n\ts_nop 0\n\tbuffer_load_dwordx4 %1, %2, %3 offen sc1 lds\n\ts_mov_b32 m0, %0"
               : "=&s"(keep) : "v"(v), "s"(r), "s"(s), "s"(lds) : "memory");
}
#define VMWAIT(n) asm volatile("s_waitcnt vmcnt(" #n ")" ::: "memory")
template <int N> DEV void vmwait() { asm volatile("s_waitcnt vmcnt(%0)" ::"n"(N) : "memory"); }
DEV v8bf asbf(v4u v) { return __builtin_bit_cast(v8bf, v); }
DEV v4f mfma(v4u a, v4u b, v4f c) { return __builtin_amdgcn_mfma_f32_16x16x32_bf16(asbf(a), asbf(b), c, 0, 0, 0); }
DEV v2u trrd(unsigned a) { return __builtin_bit_cast(v2u, __builtin_amdgcn_ds_read_tr16_b64_v4i16((LAS v4s*)(size_t)a)); }
DEV v4u trfrag(unsigned a, unsigned step) { v2u x = trrd(a), y = trrd(a + step); return (v4u){x.x, x.y, y.x, y.y}; }

typedef unsigned long long u64;
#define GS(x) #x
#define GX(x) GS(x)
#define G_LD4(k, a, d) "s_mov_b64 exec, %[m" #k "]\n\ts_cbranch_execz 1" #k "f\n\tbuffer_load_dwordx4 v[" #a ":" #d "], %[vo], %[rs], %[s" #k "] offen sc1\n1" #k ":\n\t"
#define G_LD2(k, a, b) "s_mov_b64 exec, %[m" #k "]\n\ts_cbranch_execz 1" #k "f\n\tbuffer_load_dwordx2 v[" #a ":" #b "], %[vo], %[rs], %[s" #k "] offen sc1\n1" #k ":\n\t"
#define G_CK0(k, a, b, c, d) "s_mov_b64 exec, %[m" #k "]\n\ts_cbranch_execz 2" #k "f\n\t" \
  "v_xor_b32 v" #a ", %[t0], v" #a "\n\tv_xor_b32 v" #b ", %[t1], v" #b "\n\tv_xor_b32 v" #c ", %[t2], v" #c "\n\tv_xor_b32 v" #d ", %[t3], v" #d "\n\t" \
  "v_or3_b32 %[tm], v" #a ", v" #b ", v" #c "\n\tv_bitop3_b32 %[tm], %[tm], v" #d ", %[ff] bitop3:0xa8\n\tv_cmp_ne_u32 %[m" #k "], 0, %[tm]\n2" #k ":\n\t"
#define G_CK1(k, a, b, c, d) "s_mov_b64 exec, %[m" #k "]\n\t" \
  "v_bitop3_b32 %[tm], v" #b ", v" #d ", %[t0] bitop3:0x7e\n\tv_cmp_ne_u32 %[m" #k "], 0, %[tm]\n\t"
#define G_CK2(k, a, b) "s_mov_b64 exec, %[m" #k "]\n\tv_max_u32 %[tm], v" #a ", v" #b "\n\tv_cmp_eq_u32 %[m" #k "], -1, %[tm]\n\t"
#define G_OR(k) "s_or_b64 %[ac], %[ac], %[m" #k "]\n\t"
#define G_TAIL(sl) "s_cbranch_scc0 9f\n\ts_sub_u32 %[cn], %[cn], 1\n\ts_cbranch_scc1 9f\n\ts_sleep " GX(sl) "\n\ts_branch 0b\n9:\n\ts_mov_b64 exec, -1\n\ts_nop 1"
#define G_M4 [m0] "+&s"(m[0]), [m1] "+&s"(m[1]), [m2] "+&s"(m[2]), [m3] "+&s"(m[3])
#define G_M8 G_M4, [m4] "+&s"(m[4]), [m5] "+&s"(m[5]), [m6] "+&s"(m[6]), [m7] "+&s"(m[7])
#define G_S4 [s0] "s"(so), [s1] "s"(so + step), [s2] "s"(so + 2 * step), [s3] "s"(so + 3 * step)
#define G_S8 G_S4, [s4] "s"(so + 4 * step), [s5] "s"(so + 5 * step), [s6] "s"(so + 6 * step), [s7] "s"(so + 7 * step)
#define G_A4 "=&{v[128:131]}"(a[0]), "=&{v[132:135]}"(a[1]), "=&{v[136:139]}"(a[2]), "=&{v[140:143]}"(a[3])
#define G_A8 G_A4, "=&{v[144:147]}"(a[4]), "=&{v[148:151]}"(a[5]), "=&{v[152:155]}"(a[6]), "=&{v[156:159]}"(a[7])
#define G_TMP [tm] "=&v"(tm), [ac] "=&s"(ac), [cn] "+&s"(cn)
#define G_L4 G_LD4(0, 128, 131) G_LD4(1, 132, 135) G_LD4(2, 136, 139) G_LD4(3, 140, 143)
#define G_L8 G_L4 G_LD4(4, 144, 147) G_LD4(5, 148, 151) G_LD4(6, 152, 155) G_LD4(7, 156, 159)
#define G_C4(C) C(0, 128, 129, 130, 131) C(1, 132, 133, 134, 135) C(2, 136, 137, 138, 139) C(3, 140, 141, 142, 143)
#define G_C8(C) G_C4(C) C(4, 144, 145, 146, 147) C(5, 148, 149, 150, 151) C(6, 152, 153, 154, 155) C(7, 156, 157, 158, 159)
#define G_O4 "s_or_b64 %[ac], %[m0], %[m1]\n\t" G_OR(2) G_OR(3)
#define G_O8 G_O4 G_OR(4) G_OR(5) G_OR(6) G_OR(7)
#define G_DECL(N) u64 m[N]; for (int i = 0; i < N; ++i) m[i] = ~0ull; unsigned tm, cn = POLL_MAX; u64 ac
#define G_T4 [t0] "s"(tb.x), [t1] "s"(tb.y), [t2] "s"(tb.z), [t3] "s"(tb.w), [ff] "s"(255u)
DEV void gather8f(v4u (&a)[8], rsrc_t r, unsigned vo, unsigned so, unsigned step, v4u tb) {
  G_DECL(8);
  asm volatile("0:\n\t" G_L8 "s_waitcnt vmcnt(0)\n\t" G_C8(G_CK0) G_O8 G_TAIL(PS)
               : G_A8, G_M8, G_TMP : [vo] "v"(vo), [rs] "s"(r), G_S8, G_T4 : "memory", "scc");
}
DEV void gather4f(v4u (&a)[4], rsrc_t r, unsigned vo, unsigned so, unsigned step, v4u tb) {
  G_DECL(4);
  asm volatile("0:\n\t" G_L4 "s_waitcnt vmcnt(0)\n\t" G_C4(G_CK0) G_O4 G_TAIL(PS)
               : G_A4, G_M4, G_TMP : [vo] "v"(vo), [rs] "s"(r), G_S4, G_T4 : "memory", "scc");
}
DEV void gather8p(v4u (&a)[8], rsrc_t r, unsigned vo, unsigned so, unsigned step, unsigned tag) {
  G_DECL(8);
  asm volatile("0:\n\t" G_L8 "s_waitcnt vmcnt(0)\n\t" G_C8(G_CK1) G_O8 G_TAIL(4)
               : G_A8, G_M8, G_TMP : [vo] "v"(vo), [rs] "s"(r), G_S8, [t0] "s"(tag) : "memory", "scc");
}
DEV void gather4p(v4u (&a)[4], rsrc_t r, unsigned vo, unsigned s0, unsigned s1, unsigned s2, unsigned s3, unsigned tag) {
  G_DECL(4);
  asm volatile("0:\n\t" G_L4 "s_waitcnt vmcnt(0)\n\t" G_C4(G_CK1) G_O4 G_TAIL(16)
               : G_A4, G_M4, G_TMP : [vo] "v"(vo), [rs] "s"(r), [s0] "s"(s0), [s1] "s"(s1), [s2] "s"(s2), [s3] "s"(s3), [t0] "s"(tag) : "memory", "scc");
}
DEV void gather4s(v2u (&a)[4], rsrc_t r, unsigned vo, unsigned s0, unsigned s1, unsigned s2, unsigned s3) {
  G_DECL(4);
  asm volatile("0:\n\t" G_LD2(0, 128, 129) G_LD2(1, 130, 131) G_LD2(2, 132, 133) G_LD2(3, 134, 135) "s_waitcnt vmcnt(0)\n\t"
               G_CK2(0, 128, 129) G_CK2(1, 130, 131) G_CK2(2, 132, 133) G_CK2(3, 134, 135) G_O4 G_TAIL(PS)
               : "=&{v[128:129]}"(a[0]), "=&{v[130:131]}"(a[1]), "=&{v[132:133]}"(a[2]), "=&{v[134:135]}"(a[3]), G_M4, G_TMP
               : [vo] "v"(vo), [rs] "s"(r), [s0] "s"(s0), [s1] "s"(s1), [s2] "s"(s2), [s3] "s"(s3) : "memory", "scc");
}

template <int CTRL> DEV float dpp(float x) { return __builtin_bit_cast(float, __builtin_amdgcn_mov_dpp(__builtin_bit_cast(int, x), CTRL, 0xf, 0xf, true)); }
DEV float x16sum(float x) { auto s = __builtin_amdgcn_permlane16_swap(fbits(x), fbits(x), false, false); return bitsf(s[0]) + bitsf(s[1]); }
DEV float x32sum(float x) { auto s = __builtin_amdgcn_permlane32_swap(fbits(x), fbits(x), false, false); return bitsf(s[0]) + bitsf(s[1]); }
DEV float x16max(float x) { auto s = __builtin_amdgcn_permlane16_swap(fbits(x), fbits(x), false, false); return fmaxf(bitsf(s[0]), bitsf(s[1])); }
DEV float x32max(float x) { auto s = __builtin_amdgcn_permlane32_swap(fbits(x), fbits(x), false, false); return fmaxf(bitsf(s[0]), bitsf(s[1])); }
DEV float wave_sum(float x) {
  x += dpp<0xB1>(x); x += dpp<0x4E>(x); x += dpp<0x141>(x); x += dpp<0x140>(x);
  return x32sum(x16sum(x));
}
DEV float fdiv(float a, float d, float rd) { float q = a * rd; return fmaf(fmaf(-q, d, a), rd, q); }
struct Stat { float mb, sd, rd; };
template <int N> DEV Stat ln_stat(const unsigned (&w)[N]) {
  float s = 0.f;
#pragma unroll
  for (int i = 0; i < N; ++i) s += bflo(w[i]) + bfhi(w[i]);
  const float m = wave_sum(s) * (1.f / 1024.f);
  float q = 0.f;
#pragma unroll
  for (int i = 0; i < N; ++i) { float a = bflo(w[i]) - m, b = bfhi(w[i]) - m; q = fmaf(a, a, q); q = fmaf(b, b, q); }
  const float var = rbf(wave_sum(q) * (1.f / 1024.f));
  Stat st;
  st.mb = rbf(m);
  st.sd = rbf(__builtin_amdgcn_sqrtf(rbf(var + 1e-5f)));
  st.rd = __builtin_amdgcn_rcpf(st.sd);
  return st;
}
DEV unsigned ln_pair(unsigned x, const Stat& st) {
  unsigned d = pk(bflo(x) - st.mb, bfhi(x) - st.mb);
  return pk(fdiv(bflo(d), st.sd, st.rd), fdiv(bfhi(d), st.sd, st.rd));
}
DEV unsigned onep(unsigned c) { return pk(1.f + bflo(c), 1.f + bfhi(c)); }
DEV unsigned mod_pair(unsigned ln, unsigned c1, unsigned s) {
  unsigned p = pk(bflo(ln) * bflo(c1), bfhi(ln) * bfhi(c1));
  return pk(bflo(p) + bflo(s), bfhi(p) + bfhi(s));
}
DEV unsigned res_pair(unsigned x, unsigned g, float a, float b) {
  unsigned y = pk(a, b);
  unsigned p = pk(bflo(g) * bflo(y), bfhi(g) * bfhi(y));
  return pk(bflo(x) + bflo(p), bfhi(x) + bfhi(p));
}
DEV float silu1(float v) { return v / (1.f + __expf(-v)); }

#define STAMP(k)
#define STAMP_DUMP()

DEV v4f unit_reduce(v4f a) {
#pragma unroll
  for (int i = 0; i < 4; ++i) { float t = a[i]; t += dpp<0x128>(t); a[i] = x32sum(x16sum(t)); }
  return a;
}
DEV void wg_barrier() { asm volatile("s_waitcnt lgkmcnt(0)\n\ts_barrier" ::: "memory"); }

__global__ void __launch_bounds__(NTHR, 3)
fused_kernel(const void* __restrict__ px, const void* __restrict__ pk_, const void* __restrict__ pv, const void* __restrict__ pwq,
             const void* __restrict__ pwo, const void* __restrict__ pwg, const void* __restrict__ pwu, const void* __restrict__ pwd,
             const void* __restrict__ pc, const void* __restrict__ pwm, void* __restrict__ pout, void* __restrict__ pws) {
  const int lane = threadIdx.x & 63;
  const int wave = __builtin_amdgcn_readfirstlane(threadIdx.x >> 6);
  const int wg = blockIdx.x;
  const int n = lane & 15, g = lane >> 4;
  const rsrc_t rws = mk(pws, WS_BYTES);
  const int bk = wg & 31, sp = wg >> 5;
  const int ab = bk >> 2, kvh = bk & 3;
  const int role = wg < 96 ? 0 : wg < 160 ? 1 : 2;
  const bool ffn = wg >= 32;
  const int fi = wg - 32;
  const int gks = fi & 3, gct = fi >> 2;
  const int dj = fi & 7, dft = fi >> 3;
  const int oh = wg >> 3, oj = wg & 7;
  const int ub = wg >> 5, uu = wg & 31;

  STAMP(0);
  unsigned ep;
  {
    const unsigned z = 0;
    asm volatile("buffer_load_dword %0, %1, %2, 0 offen sc1" : "=v"(ep) : "v"(z), "s"(rws) : "memory");
  }
  const int tq = (lane >> 2) & 3, tp = lane & 3;

  if (wave < 8) {
    const rsrc_t rk = mk((const char*)pk_ + (size_t)bk * 2097152 + sp * 262144 + wave * 32768, 32768);
    const rsrc_t rv = mk((const char*)pv + (size_t)bk * 2097152 + sp * 262144 + wave * 32768, 32768);
    const bool hold = wg < 32 || role == 1;
    if (hold) {
      unsigned xr[8];
      {
        const rsrc_t rx = mk(px, role == 1 ? 16384 : 0);
#pragma unroll
        for (int k = 0; k < 4; ++k) {
          v2u v = __builtin_amdgcn_raw_buffer_load_b64(rx, lane * 8, wave * 2048 + k * 512, 0);
          xr[2 * k] = v.x; xr[2 * k + 1] = v.y;
        }
      }
      wg_barrier();
      if (role == 1) {
        const Stat st = ln_stat<8>(xr);
#pragma unroll
        for (int k = 0; k < 8; ++k) xr[k] = ln_pair(xr[k], st);
        VMWAIT(0);
        asm volatile("" : "+v"(ep));
        const unsigned T1 = __builtin_amdgcn_readfirstlane(ep) + 0x01010101u;
        lds_wait<2>(F_WQ, 4);
        STAMP(10);
        v4u a[8];
        gather8p(a, rws, lane * 16, O_MOD + wave * 24576, 1024, T1);
        STAMP(11);
#pragma unroll
        for (int k = 0; k < 4; ++k)
          lds_st<v2u>(L_ACT + wave * 2080 + k * 512 + lane * 8,
                      (v2u){mod_pair(xr[2 * k], onep(a[4 + k].x), a[k].x), mod_pair(xr[2 * k + 1], onep(a[4 + k].z), a[k].z)});
        lds_signal(F_H, lane);
        STAMP(12);
      }
    }
    v4u kf[8][4];
    const unsigned kvo = n * 256 + g * 16;
#pragma unroll
    for (int t = 0; t < 8; ++t) {
      if (t == KWIN && !hold) wg_barrier();
      if (t >= KWIN) vmwait<4 * (KWIN - 1)>();
#pragma unroll
      for (int s = 0; s < 4; ++s) kf[t][s] = ld_nt(rk, kvo + s * 64, t * 4096);
      cfence();
    }
    asm volatile("" : "+v"(ep));
    const v4u tb = tagbytes(__builtin_amdgcn_readfirstlane(ep) + 0x01010101u);
    STAMP(1);
    if (ffn) {
      if (role != 2) lds_wait<2>(F_HEAD, 4);
      const rsrc_t rg = mk(pwg, 1024 * 3584 * 2), ru = mk(pwu, 1024 * 3584 * 2);
      const unsigned v0 = (lane >> 3) * 7168 + (((lane & 7) ^ (((lane >> 4) & 1) << 1)) << 4), v1 = v0 ^ 64;
#pragma unroll
      for (int i = 0; i < 4; ++i) {
        if (i == 0 || i == 2) vmwait<4 * (KWIN - 1)>();
        const unsigned so = (gks * 256 + wave * 32 + i * 8) * 7168 + gct * 128;
        dma16(rg, (i & 1) ? v1 : v0, so, L_G + (wave * 4 + i) * 1024);
        dma16(ru, (i & 1) ? v1 : v0, so, L_G + 32768 + (wave * 4 + i) * 1024);
      }
    }
    lds_wait<2>(F_Q, 1);
    STAMP(2);
    v4u qf[4];
#pragma unroll
    for (int s = 0; s < 4; ++s) qf[s] = lds_ld<v4u>(L_QB + (n & 7) * 288 + s * 64 + g * 16);
    const unsigned vvo = g * 1024 + n * 16;
    v4u vf[4][8];
    unsigned sc[8][2];
    float mx = -INFINITY;
    v4f prev;
#pragma unroll
    for (int t = 0; t < 9; ++t) {
      v4f a = {0.f, 0.f, 0.f, 0.f};
      if (t < 8) {
#pragma unroll
        for (int s = 0; s < 4; ++s) a = mfma(kf[t][s], qf[s], a);
      }
      if (t >= 1) {
#pragma unroll
        for (int r = 0; r < 2; ++r) {
          unsigned u = pk(prev[2 * r], prev[2 * r + 1]);
          u = pk(bflo(u) * 0.08838834764831845f, bfhi(u) * 0.08838834764831845f);
          sc[t - 1][r] = u;
          mx = fmaxf(mx, fmaxf(bflo(u), bfhi(u)));
        }
      }
      prev = a;
      if (t >= 2 && t <= 7) {
        __builtin_amdgcn_sched_barrier(0);
#pragma unroll
        for (int j = 0; j < 4; ++j) vf[(t - 2) >> 1][((t - 2) & 1) * 4 + j] = ld_nt(rv, vvo + j * 256, (t - 2) * 4096);
        __builtin_amdgcn_sched_barrier(0);
      }
    }
    STAMP(3);
    mx = x32max(x16max(mx));
    if (lane < 8) lds_st<float>(L_MX + wave * 32 + lane * 4, mx);
    lds_signal(F_KMAX, lane);
    lds_wait<1>(F_KMAX, 8);
#pragma unroll
    for (int w = 0; w < 8; ++w) mx = fmaxf(mx, lds_ld<float>(L_MX + w * 32 + (n & 7) * 4));
    float ls = 0.f;
    v4u pf[4];
#pragma unroll
    for (int t = 0; t < 8; ++t) {
      float p0 = __expf(bflo(sc[t][0]) - mx), p1 = __expf(bfhi(sc[t][0]) - mx), p2 = __expf(bflo(sc[t][1]) - mx), p3 = __expf(bfhi(sc[t][1]) - mx);
      ls += (p0 + p1) + (p2 + p3);
      pf[t >> 1][(t & 1) * 2] = pk(p0, p1);
      pf[t >> 1][(t & 1) * 2 + 1] = pk(p2, p3);
    }
    ls = x32sum(x16sum(ls));
    if (lane < 8) lds_st<float>(L_LS + wave * 32 + lane * 4, ls);
    lds_signal(F_KML, lane);
    STAMP(4);
    v4f acc[8];
#pragma unroll
    for (int i = 0; i < 8; ++i) acc[i] = (v4f){0.f, 0.f, 0.f, 0.f};
#pragma unroll
    for (int p = 0; p < 4; ++p) {
#pragma unroll
      for (int i = 0; i < 8; ++i) {
        v4u a;
#pragma unroll
        for (int e = 0; e < 4; ++e) a[e] = __builtin_amdgcn_perm(vf[p][2 * e + 1][i >> 1], vf[p][2 * e][i >> 1], (i & 1) ? 0x07060302u : 0x05040100u);
        acc[i] = mfma(a, pf[p], acc[i]);
      }
      if (p == 0) {
        __builtin_amdgcn_sched_barrier(0);
#pragma unroll
        for (int j = 0; j < 8; ++j) vf[3][j] = ld_nt(rv, vvo + (j & 3) * 256, (6 + (j >> 2)) * 4096);
        __builtin_amdgcn_sched_barrier(0);
      }
      if (p == 2) lds_wait<1>(F_WFREE, 4);
    }
    STAMP(5);
    {
      const int hd = n & 7, hi = n >> 3;
#pragma unroll
      for (int i = 0; i < 4; ++i) {
        const v4f v = sel4(hi, acc[i + 4], acc[i]);
        lds_st<v4f>(L_W + wave * 4096 + hd * 512 + (g * 8 + ((i + 4 * hi) ^ hd)) * 16, v);
      }
      lds_signal(F_MERGE, lane);
      lds_wait<0>(F_MERGE, 8);
      const int ci = lane & 31;
      const unsigned a0 = L_W + (lane >> 5) * 16384 + wave * 512 + (ci ^ wave) * 16;
      v4f s0 = lds_ld<v4f>(a0), s1 = lds_ld<v4f>(a0 + 4096), s2 = lds_ld<v4f>(a0 + 8192), s3 = lds_ld<v4f>(a0 + 12288);
      v4f s = (s0 + s1) + (s2 + s3);
#pragma unroll
      for (int i = 0; i < 4; ++i) s[i] = x32sum(s[i]);
      if (lane < 32) st_dev(tag4(s, tb), rws, ci * 16, O_AO + (kvh * 8 + wave) * 32768 + sp * 4096 + ab * 512);
      lds_signal(F_ATT, lane);
    }
    STAMP(6);
    if (ffn) {
      v4u wf[8];
      {
        const int nt = wave & 3;
        const unsigned sw = ((tq >> 1) & 1) | ((g & 1) << 1);
        const unsigned base = L_G + (wave >> 2) * 32768 + (8 * g + tq) * 128 + (((2 * nt + (tp >> 1)) ^ (sw << 1)) << 4) + 8 * (tp & 1);
#pragma unroll
        for (int kb = 0; kb < 8; ++kb) wf[kb] = trfrag(base + kb * 4096, 512);
      }
      lds_wait<1>(F_SIDE, 4);
      const v2u sbv = lds_ld<v2u>(L_SB + wave * 512 + lane * 8);
      v2u cbv = lds_ld<v2u>(L_CB + wave * 512 + lane * 8);
      cbv.x = onep(cbv.x); cbv.y = onep(cbv.y);
      lds_wait<1>(F_T2, 1);
      if (D3) __builtin_amdgcn_s_sleep(D3);
      STAMP(7);
      v2u xv[4];
      {
        const unsigned xo = O_X1 + wave * 2048;
        gather4s(xv, rws, lane * 8, xo + gks * 512, xo + ((gks + 1) & 3) * 512, xo + ((gks + 2) & 3) * 512, xo + ((gks + 3) & 3) * 512);
      }
      const unsigned xr[8] = {xv[0].x, xv[0].y, xv[1].x, xv[1].y, xv[2].x, xv[2].y, xv[3].x, xv[3].y};
      STAMP(8);
      const Stat st = ln_stat<8>(xr);
      v2u h2;
      h2.x = mod_pair(ln_pair(xr[0], st), cbv.x, sbv.x);
      h2.y = mod_pair(ln_pair(xr[1], st), cbv.y, sbv.y);
      lds_st<v2u>(L_A3 + wave * 544 + lane * 8, h2);
      lds_signal(F_A3, lane);
      lds_wait<0>(F_A3, 8);
      v4f d0 = {0.f, 0.f, 0.f, 0.f}, d1 = d0;
#pragma unroll
      for (int kb = 0; kb < 8; kb += 2) {
        d0 = mfma(wf[kb], lds_ld<v4u>(L_A3 + (n & 7) * 544 + kb * 64 + g * 16), d0);
        d1 = mfma(wf[kb + 1], lds_ld<v4u>(L_A3 + (n & 7) * 544 + kb * 64 + 64 + g * 16), d1);
      }
      d0 += d1;
      if (n < 8)
        st_dev(tag4(d0, tb), rws, n * 64 + g * 16,
               O_GP + (gct >> 1) * 32768 + ((gct & 1) * 2 + ((wave & 3) >> 1)) * 8192 + gks * 2048 + (wave >> 2) * 1024 + (wave & 1) * 512);
      lds_signal(F_GU, lane);
      STAMP(9);
    }
    STAMP_DUMP();
    return;
  }

  const int cw = wave - 8;
  __builtin_amdgcn_s_setprio(2);
  unsigned cb_n = 0;
  v4u in[4];
  {
    const rsrc_t ri = mk(pc, role == 0 ? 16384 : 0);
    const unsigned vo = cw * 4096 + lane * 16;
    asm volatile("buffer_load_dwordx4 %0, %4, %5, 0 offen\n\tbuffer_load_dwordx4 %1, %4, %5, 0 offen offset:1024\n\t"
                 "buffer_load_dwordx4 %2, %4, %5, 0 offen offset:2048\n\tbuffer_load_dwordx4 %3, %4, %5, 0 offen offset:3072"
                 : "=&v"(in[0]), "=&v"(in[1]), "=&v"(in[2]), "=&v"(in[3]) : "v"(vo), "s"(ri) : "memory");
  }
  if (cw == 0) {
    if (lane < 32) lds_st<unsigned>(L_FL + 4 * lane, 0u);
    if (lane < 4) st_dev((v4u){SENT, SENT, SENT, SENT}, rws, lane * 16, O_X1 + wg * 64);
  }
  cfence();
  if (role != 2) {
    const unsigned rowb = role == 0 ? 12288 : 8192;
    const rsrc_t rh = mk(role == 0 ? pwm : pwq, 1024 * rowb);
    const unsigned col = (role == 0 ? wg : wg - 96) * 128;
    const unsigned v0 = (lane >> 3) * rowb + (((lane & 7) ^ (((lane >> 4) & 1) << 1)) << 4), v1 = v0 ^ 64;
#pragma unroll
    for (int k = 0; k < 32; ++k) dma16(rh, (k & 1) ? v1 : v0, (cw * 256 + k * 8) * rowb + col, (cw * 32 + k) * 1024);
    wg_barrier();
    VMWAIT(32);
  } else {
    wg_barrier();
    VMWAIT(0);
  }
  asm volatile("" : "+v"(ep), "+v"(in[0]), "+v"(in[1]), "+v"(in[2]), "+v"(in[3]) : : "memory");
  const unsigned TAG = __builtin_amdgcn_readfirstlane(ep) + 0x01010101u;
  const v4u tb = tagbytes(TAG);
  STAMP(1);
  if (role == 0) {
#pragma unroll
    for (int k = 0; k < 4; ++k) {
      v4u o;
#pragma unroll
      for (int e = 0; e < 4; ++e) o[e] = pk(silu1(bflo(in[k][e])), silu1(bfhi(in[k][e])));
      lds_st<v4u>(L_ACT + (2 * cw + (k >> 1)) * 2080 + (k & 1) * 1024 + lane * 16, o);
    }
    lds_signal(F_H, lane);
    lds_signal(F_H, lane);
  }
  STAMP(2);
  if (role != 2) {
    const unsigned sw = ((tq >> 1) & 1) | ((g & 1) << 1);
    const unsigned base = cw * 32768 + (8 * g + tq) * 128 + (((tp >> 1) ^ (sw << 1)) << 4) + 8 * (tp & 1);
    v4u wf[8][4];
#pragma unroll
    for (int kb = 0; kb < 8; ++kb) {
      if (kb == 0) VMWAIT(28); if (kb == 1) VMWAIT(24); if (kb == 2) VMWAIT(20); if (kb == 3) VMWAIT(16);
      if (kb == 4) VMWAIT(12); if (kb == 5) VMWAIT(8); if (kb == 6) VMWAIT(4); if (kb == 7) VMWAIT(0);
      if (kb == 6) lds_signal(F_WQ, lane);
#pragma unroll
      for (int t = 0; t < 4; ++t) wf[kb][t] = trfrag((base ^ (t << 5)) + kb * 4096, 512);
    }
    STAMP(3);
    lds_wait<0>(F_H, 8);
    const unsigned bb = L_ACT + (n & 7) * 2080 + cw * 512 + g * 16;
    v4f d[4];
#pragma unroll
    for (int t = 0; t < 4; ++t) d[t] = (v4f){0.f, 0.f, 0.f, 0.f};
#pragma unroll
    for (int kb = 0; kb < 8; ++kb) {
      const v4u b = lds_ld<v4u>(bb + kb * 64);
#pragma unroll
      for (int t = 0; t < 4; ++t) d[t] = mfma(wf[kb][t], b, d[t]);
    }
    const bool hi = n >= 8;
    const unsigned ra = cw * 32768 + (n >> 3) * 1024 + (n & 7) * 64 + g * 16;
    lds_st<v4f>(ra, sel4(hi, d[2], d[0]));
    lds_st<v4f>(ra + 512, sel4(hi, d[3], d[1]));
    lds_signal(F_CBAR, lane); cb_n += 4; lds_wait<0>(F_CBAR, cb_n);
    const unsigned rb = (lane >> 5) * 65536 + cw * 512 + (lane & 31) * 16;
    v4f s = lds_ld<v4f>(rb) + lds_ld<v4f>(rb + 32768);
#pragma unroll
    for (int i = 0; i < 4; ++i) s[i] = x32sum(s[i]);
    const unsigned rowo = role == 0 ? 24576 : 16384;
    if (lane < 32)
      st_dev((v4u){pk(s[0], s[1]), TAG, pk(s[2], s[3]), TAG}, rws, (lane >> 2) * rowo + (lane & 3) * 16,
             (role == 0 ? O_MOD + wg * 256 : O_Q + (wg - 96) * 256) + cw * 64);
    lds_signal(F_HEAD, lane);
  } else {
    __builtin_amdgcn_s_sleep(DQ0);
    __builtin_amdgcn_s_sleep(DQ0);
  }
  STAMP(4);
  if (role == 0) __builtin_amdgcn_s_sleep(DQS);
  if (cw) __builtin_amdgcn_s_sleep(DQS);
  if (cw > 1) __builtin_amdgcn_s_sleep(DQS);
  if (cw > 2) __builtin_amdgcn_s_sleep(DQS);
#pragma nounroll
  for (int it = 0; it < POLL_MAX; ++it) {
    cfence();
    if (lds_flag(F_Q)) break;
    v4u a[4];
    bool bad = false;
#pragma unroll
    for (int k = 0; k < 4; ++k) a[k] = ld_dev(rws, lane * 16, O_Q + ab * 16384 + kvh * 4096 + k * 1024);
#pragma unroll
    for (int k = 0; k < 4; ++k) bad = bad || a[k].y != TAG || a[k].w != TAG;
    if (!__any(bad)) {
#pragma unroll
      for (int k = 0; k < 4; ++k) lds_st<v2u>(L_QB + (2 * k + (lane >> 5)) * 288 + (lane & 31) * 8, (v2u){a[k].x, a[k].z});
      lds_signal(F_Q, lane);
      break;
    }
    __builtin_amdgcn_s_sleep(DQS);
  }
  STAMP(5);
  if (role != 2) lds_wait<1>(F_HEAD, 4);
  {
    const rsrc_t ro = mk(pwo, 4096 * 2048), rd = mk(pwd, 3584 * 2048);
    const unsigned c0 = ((n ^ (g << 1)) << 4), c1 = c0 ^ 128;
#pragma unroll
    for (int i = 0; i < 8; ++i)
      dma16(ro, g * 8 * 2048 + ((i & 2) ? c1 : c0), (oh * 128 + cw * 32 + i) * 2048 + oj * 256, L_W + (cw * 8 + i) * 1024);
    if (ffn) {
#pragma unroll
      for (int i = 0; i < 8; ++i)
        dma16(rd, g * 2048 + ((i & 2) ? c1 : c0), (dft * 128 + cw * 32 + i * 4) * 2048 + dj * 256, L_DN + (cw * 8 + i) * 1024);
    }
  }
  v2u xu, gau, gbu;
  {
    const rsrc_t rx = mk(px, 16384);
    xu = __builtin_amdgcn_raw_buffer_load_b64(rx, (lane & 7) * 8, ub * 2048 + uu * 64, 0);
    const int ks = ffn ? gks : 0;
    v4u a[4];
    const unsigned mo = O_MOD + 2 * cw * 24576 + 3 * 4096 + ks * 1024;
    gather4p(a, rws, lane * 16, mo, mo + 4096, mo + 24576, mo + 24576 + 4096, TAG);
    lds_st<v2u>(L_SB + cw * 1024 + lane * 8, (v2u){a[0].x, a[0].z});
    lds_st<v2u>(L_CB + cw * 1024 + lane * 8, (v2u){a[1].x, a[1].z});
    lds_st<v2u>(L_SB + cw * 1024 + 512 + lane * 8, (v2u){a[2].x, a[2].z});
    lds_st<v2u>(L_CB + cw * 1024 + 512 + lane * 8, (v2u){a[3].x, a[3].z});
    gau = gbu = (v2u){0u, 0u};
    if (cw == 0) {
      const unsigned uo = O_MOD + ub * 24576 + 2 * 4096 + uu * 128;
      gather4p(a, rws, (lane & 7) * 16, uo, uo + 3 * 4096, uo, uo + 3 * 4096, TAG);
      gau = (v2u){a[0].x, a[0].z}; gbu = (v2u){a[1].x, a[1].z};
    }
    lds_signal(F_SIDE, lane);
  }
  VMWAIT(0);
  lds_signal(F_CBAR, lane); cb_n += 4; lds_wait<1>(F_CBAR, cb_n);
  v4u wof[4][2], wdf[4][2];
  {
    const unsigned sw = (tq | ((g & 1) << 2)) << 1;
#pragma unroll
    for (int e = 0; e < 2; ++e) {
      const unsigned base = (8 * g + tq) * 256 + (((2 * (2 * cw + e) + (tp >> 1)) ^ sw) << 4) + 8 * (tp & 1);
#pragma unroll
      for (int kb = 0; kb < 4; ++kb) {
        wof[kb][e] = trfrag(L_W + base + kb * 8192, 1024);
        wdf[kb][e] = trfrag(L_DN + base + kb * 8192, 1024);
      }
    }
    asm volatile("s_waitcnt lgkmcnt(0)" ::: "memory");
    lds_signal(F_WFREE, lane);
  }
  STAMP(6);
  if (cw == 3) {
    lds_wait<4>(F_KML, 8);
    float m = -INFINITY, l = 0.f;
#pragma unroll
    for (int w = 0; w < 8; ++w) { m = fmaxf(m, lds_ld<float>(L_MX + w * 32 + (lane & 7) * 4)); l += lds_ld<float>(L_LS + w * 32 + (lane & 7) * 4); }
    const unsigned t0 = TAG & 0xffffu, t1 = TAG >> 24;
    if (lane < 8) st_dev2((v2u){(fbits(m) & 0xffff0000u) | t0, (fbits(l) & ~255u) | t1}, rws, lane * 512, O_ML + kvh * 4096 + ab * 64 + sp * 8);
    v2u ml;
#pragma nounroll
    for (int it = 0; it < POLL_MAX; ++it) {
      __builtin_amdgcn_s_sleep(16);
      cfence();
      ml = ld_dev2(rws, lane * 8, O_ML + oh * 512);
      ml.x ^= t0; ml.y ^= t1;
      if (!__any(((ml.x & 0xffffu) | (ml.y & 255u)) != 0)) break;
    }
    float M = bitsf(ml.x);
    M = fmaxf(M, dpp<0xB1>(M)); M = fmaxf(M, dpp<0x4E>(M)); M = fmaxf(M, dpp<0x141>(M));
    const float w = __expf(bitsf(ml.x) - M);
    float L = w * bitsf(ml.y);
    L += dpp<0xB1>(L); L += dpp<0x4E>(L); L += dpp<0x141>(L);
    lds_st<float>(L_WT + lane * 4, w / L);
    lds_signal(F_WT, lane);
  }
  lds_wait<4>(F_WT, 1);
  const v4f w0 = lds_ld<v4f>(L_WT + (2 * cw + (lane >> 5)) * 32), w1 = lds_ld<v4f>(L_WT + (2 * cw + (lane >> 5)) * 32 + 16);
  STAMP(7);
  const bool hi = n >= 8;
  lds_wait<1>(F_ATT, 8);
  if (D1) __builtin_amdgcn_s_sleep(D1);
  {
    v4u a[8];
    gather8f(a, rws, lane * 16, O_AO + oh * 32768 + cw * 1024, 4096, tb);
    STAMP(8);
    v4f o = asf4(a[0]) * w0[0], o2 = asf4(a[4]) * w1[0];
#pragma unroll
    for (int s = 1; s < 4; ++s) { o += asf4(a[s]) * w0[s]; o2 += asf4(a[4 + s]) * w1[s]; }
    o += o2;
    lds_st<v2u>(L_A1 + (2 * cw + (lane >> 5)) * 288 + (lane & 31) * 8, (v2u){pk(o[0], o[1]), pk(o[2], o[3])});
    lds_signal(F_CBAR, lane); cb_n += 4; lds_wait<0>(F_CBAR, cb_n);
    v4f d0 = {0.f, 0.f, 0.f, 0.f}, d1 = d0;
#pragma unroll
    for (int kb = 0; kb < 4; ++kb) {
      const v4u b = lds_ld<v4u>(L_A1 + (n & 7) * 288 + kb * 64 + g * 16);
      d0 = mfma(wof[kb][0], b, d0);
      d1 = mfma(wof[kb][1], b, d1);
    }
    st_dev(tag4(sel4(hi, d1, d0), tb), rws, (n & 7) * 131072 + (n >> 3) * 64 + g * 16, O_WP + (oj * 4 + cw) * 4096 + oh * 128);
  }
  STAMP(9);
  v2u x1u = {0u, 0u};
  if (cw == 0) {
    if (D2) __builtin_amdgcn_s_sleep(D2);
    v4u a[4];
    gather4f(a, rws, lane * 16, O_WP + wg * 4096, 1024, tb);
    STAMP(10);
    const v4f s = unit_reduce((asf4(a[0]) + asf4(a[1])) + (asf4(a[2]) + asf4(a[3])));
    x1u.x = canon(res_pair(xu.x, gau.x, s[0], s[1]));
    x1u.y = canon(res_pair(xu.y, gau.y, s[2], s[3]));
    if (lane < 8) st_dev2(x1u, rws, lane * 8, O_X1 + wg * 64);
    lds_signal(F_T2, lane);
    STAMP(11);
  }
  if (ffn) {
    lds_wait<1>(F_GU, 8);
    if (D4) __builtin_amdgcn_s_sleep(D4);
    v4u a[8];
    gather8f(a, rws, (n >> 3) * 512 + (n & 7) * 64 + g * 16, O_GP + dft * 32768 + cw * 8192, 1024, tb);
    STAMP(12);
    const v4f gt = (asf4(a[0]) + asf4(a[2])) + (asf4(a[4]) + asf4(a[6]));
    const v4f up = (asf4(a[1]) + asf4(a[3])) + (asf4(a[5]) + asf4(a[7]));
    const unsigned g0 = pk(gt[0], gt[1]), g1 = pk(gt[2], gt[3]), u0 = pk(up[0], up[1]), u1 = pk(up[2], up[3]);
    auto relu = [](float v) { return v > 0.f ? v : v * 0.f; };
    const v2u hv = {pk(relu(bflo(g0)) * bflo(u0), relu(bfhi(g0)) * bfhi(u0)), pk(relu(bflo(g1)) * bflo(u1), relu(bfhi(g1)) * bfhi(u1))};
    lds_st<v2u>(L_A4 + (n & 7) * 288 + cw * 64 + ((n >> 3) * 4 + g) * 8, hv);
    lds_signal(F_CBAR, lane); cb_n += 4; lds_wait<0>(F_CBAR, cb_n);
    v4f d0 = {0.f, 0.f, 0.f, 0.f}, d1 = d0;
#pragma unroll
    for (int kb = 0; kb < 4; ++kb) {
      const v4u b = lds_ld<v4u>(L_A4 + (n & 7) * 288 + kb * 64 + g * 16);
      d0 = mfma(wdf[kb][0], b, d0);
      d1 = mfma(wdf[kb][1], b, d1);
    }
    st_dev(tag4(sel4(hi, d1, d0), tb), rws, (n & 7) * (32 * 3584) + (n >> 3) * 64 + g * 16, O_DP + (dj * 4 + cw) * 3584 + dft * 128);
    STAMP(13);
  } else {
    __builtin_amdgcn_s_sleep(64);
  }
  if (cw == 0) {
    if (D5) __builtin_amdgcn_s_sleep(D5);
    v4u a[4];
    gather4f(a, rws, (lane < 56 ? lane : lane - 8) * 16, O_DP + wg * 3584, 896, tb);
    STAMP(14);
    v4f t = (asf4(a[0]) + asf4(a[1])) + (asf4(a[2]) + asf4(a[3]));
    if (lane >= 56) t = (v4f){0.f, 0.f, 0.f, 0.f};
    const v4f s = unit_reduce(t);
    const v2u o = {res_pair(x1u.x, gbu.x, s[0], s[1]), res_pair(x1u.y, gbu.y, s[2], s[3])};
    if (lane < 8) __builtin_amdgcn_raw_buffer_store_b64(o, mk(pout, 16384), lane * 8, wg * 64, 0);
    if (wg == 0 && lane == 0) __builtin_amdgcn_raw_buffer_store_b32(TAG, rws, 0, WS_EPOCH, 16);
    STAMP(15);
  }
  STAMP_DUMP();
}

extern "C" void kernel_launch(void* const* d_in, const int* in_sizes, int n_in, void* d_out, int out_size, void* d_ws, size_t ws_size,
                              hipStream_t stream) {
  (void)in_sizes; (void)n_in; (void)out_size; (void)ws_size;
  (void)hipFuncSetAttribute((const void*)fused_kernel, hipFuncAttributeMaxDynamicSharedMemorySize, L_END);
  fused_kernel<<<NWG, NTHR, L_END, stream>>>(d_in[0], d_in[1], d_in[2], d_in[3], d_in[4], d_in[6], d_in[7], d_in[8], d_in[9], d_in[10],
                                             d_out, d_ws);
}
```

```cpp
#include <hip/hip_runtime.h>
#include <stdint.h>

typedef unsigned v4u __attribute__((ext_vector_type(4)));
typedef unsigned v2u __attribute__((ext_vector_type(2)));
typedef float v4f __attribute__((ext_vector_type(4)));
typedef float v2f __attribute__((ext_vector_type(2)));
typedef __bf16 v2bf __attribute__((ext_vector_type(2)));
typedef __bf16 v8bf __attribute__((ext_vector_type(8)));
typedef short v4s __attribute__((ext_vector_type(4)));
#define LAS __attribute__((address_space(3)))
typedef __amdgpu_buffer_rsrc_t rsrc_t;
#define DEV __device__ __forceinline__

#define WK 4
#define WV 4
#define WH1 12
#define WH0 4
#define WF 3
#define DK0 40
#define DQ0 90
#define DQS 16
#define DMS 6
#define D1 8
#define D2 8
#define D2S 5
#define D3 40
#define D3B 8
#define D4 8
#define D5 8
#define PS 1

constexpr int NWG = 256, NTHR = 768;
constexpr unsigned SENT = 0xFFFFFFFFu;
constexpr unsigned WS_EPOCH = 0;
constexpr unsigned O_SC = 65536;
constexpr unsigned O_MOD = O_SC + 65536;
constexpr unsigned O_Q = O_MOD + 131072;
constexpr unsigned O_ML = O_Q + 131072;
constexpr unsigned O_X1 = O_ML + 32768;
constexpr unsigned O_AO = O_X1 + 16384;
constexpr unsigned O_WP = O_AO + 1048576;
constexpr unsigned O_GP = O_WP + 1048576;
constexpr unsigned O_DP = O_GP + 28 * 32768;
constexpr unsigned O_STAMP = 65536 + 256 * 16384 + 1048576;
static_assert(O_DP + 256 * 28 * 128 <= O_STAMP, "ws map");
constexpr unsigned WS_BYTES = O_STAMP + NWG * 12 * 16 * 8;
constexpr unsigned L_G = 0, L_W = 65536, L_DN = 98304;
constexpr unsigned L_M = 131072;
constexpr unsigned L_RED = L_M;
constexpr unsigned L_SB = L_RED + 8192, L_CB = L_SB + 4096;
constexpr unsigned L_A1 = L_CB + 4096;
constexpr unsigned L_A4 = L_A1 + 2304;
constexpr unsigned L_QB = L_A4 + 2304;
constexpr unsigned L_A3 = L_QB + 2304;
constexpr unsigned L_MX = L_A3 + 4352;
constexpr unsigned L_LS = L_MX + 256;
constexpr unsigned L_WT = L_LS + 256;
constexpr unsigned L_UN = L_WT + 256;
constexpr unsigned L_SX = L_UN + 256;
constexpr unsigned L_FL = L_SX + 256;
constexpr unsigned L_ST = L_FL + 128;
constexpr unsigned L_END = L_ST + 12 * 16 * 8;
static_assert(L_END <= 163840, "lds");
enum { F_CBAR, F_Q, F_KML, F_T2, F_T5 };

DEV float bflo(unsigned u) { return __builtin_bit_cast(float, u << 16); }
DEV float bfhi(unsigned u) { return __builtin_bit_cast(float, u & 0xffff0000u); }
DEV unsigned pk(float a, float b) {
  v2f f = {a, b};
  unsigned u = __builtin_bit_cast(unsigned, __builtin_convertvector(f, v2bf));
  asm("" : "+v"(u));
  return u;
}
DEV float rbf(float a) { return bflo(pk(a, a)); }
DEV unsigned fbits(float a) { return __builtin_bit_cast(unsigned, a); }
DEV float bitsf(unsigned a) { return __builtin_bit_cast(float, a); }
DEV unsigned canon(unsigned a) { return a < 0xFFFFFFFEu ? a : 0xFFFFFFFEu; }
DEV v4f sel4(bool c, v4f a, v4f b) { return (v4f){c ? a.x : b.x, c ? a.y : b.y, c ? a.z : b.z, c ? a.w : b.w}; }
DEV v4f asf4(v4u v) { return __builtin_bit_cast(v4f, v); }
DEV unsigned opq(unsigned a) { asm("" : "+v"(a)); return a; }
DEV v4u tagbytes(unsigned T) { return (v4u){T & 255u, (T >> 8) & 255u, (T >> 16) & 255u, T >> 24}; }
DEV v4u tag4(v4f v, v4u tb) {
  return (v4u){(fbits(v.x) & ~255u) | tb.x, (fbits(v.y) & ~255u) | tb.y, (fbits(v.z) & ~255u) | tb.z, (fbits(v.w) & ~255u) | tb.w};
}

template <class T> DEV T lds_ld(unsigned a) { return *(LAS T*)(size_t)a; }
template <class T> DEV void lds_st(unsigned a, T v) { *(LAS T*)(size_t)a = v; }
DEV void cfence() { asm volatile("" ::: "memory"); }
#define SCHED() __builtin_amdgcn_sched_barrier(0)
DEV void lds_signal(int f, int lane) {
  cfence();
  if (lane == 0) __hip_atomic_fetch_add((LAS unsigned*)(size_t)(L_FL + 4 * f), 1u, __ATOMIC_RELAXED, __HIP_MEMORY_SCOPE_WORKGROUP);
  cfence();
}
DEV unsigned lds_flag(int f) { return __builtin_amdgcn_readfirstlane(*(volatile LAS unsigned*)(size_t)(L_FL + 4 * f)); }
template <int SLEEP> DEV void lds_wait(int f, unsigned target) {
  cfence();
#pragma nounroll
  for (int i = 0; i < 200000; ++i) {
    if (lds_flag(f) >= target) break;
    if (SLEEP) __builtin_amdgcn_s_sleep(SLEEP);
  }
  cfence();
}
constexpr int POLL_MAX = 8000;

DEV rsrc_t mk(const void* p, unsigned bytes) { return __builtin_amdgcn_make_buffer_rsrc((void*)p, 0, bytes, 0x00020000); }
DEV v4u ld_nt(rsrc_t r, unsigned v, unsigned s) { return __builtin_amdgcn_raw_buffer_load_b128(r, v, s, 2); }
DEV v4u ld_dev(rsrc_t r, unsigned v, unsigned s) { return __builtin_amdgcn_raw_buffer_load_b128(r, v, s, 16); }
DEV void st_dev(v4u d, rsrc_t r, unsigned v, unsigned s) {
  asm volatile("s_nop 0\n\tbuffer_store_dwordx4 %0, %1, %2, %3 offen sc1\n\ts_nop 1" : : "v"(d), "v"(v), "s"(r), "s"(s) : "memory");
}
DEV void st_dev2(v2u d, rsrc_t r, unsigned v, unsigned s) {
  asm volatile("s_nop 0\n\tbuffer_store_dwordx2 %0, %1, %2, %3 offen sc1" : : "v"(d), "v"(v), "s"(r), "s"(s) : "memory");
}
DEV void dma16(rsrc_t r, unsigned v, unsigned s, unsigned lds) {
  unsigned keep;
  asm volatile("s_mov_b32 %0, m0\n\ts_mov_b32 m0, %4\n\ts_nop 0\n\tbuffer_load_dwordx4 %1, %2, %3 offen nt lds\n\ts_mov_b32 m0, %0"
               : "=&s"(keep) : "v"(v), "s"(r), "s"(s), "s"(lds) : "memory");
}
template <int N> DEV void vmwait() { asm volatile("s_waitcnt vmcnt(%0)" ::"n"(N) : "memory"); }
DEV v8bf asbf(v4u v) { return __builtin_bit_cast(v8bf, v); }
DEV v4f mfma(v4u a, v4u b, v4f c) { return __builtin_amdgcn_mfma_f32_16x16x32_bf16(asbf(a), asbf(b), c, 0, 0, 0); }
DEV v2u trrd(unsigned a) { return __builtin_bit_cast(v2u, __builtin_amdgcn_ds_read_tr16_b64_v4i16((LAS v4s*)(size_t)a)); }
DEV v4u trfrag(unsigned a, unsigned step) { v2u x = trrd(a), y = trrd(a + step); return (v4u){x.x, x.y, y.x, y.y}; }

typedef unsigned long long u64;
#define GS(x) #x
#define GX(x) GS(x)
#define G_LD4(k, a, d) "s_mov_b64 exec, %[m" #k "]\n\ts_cbranch_execz 1" #k "f\n\tbuffer_load_dwordx4 v[" #a ":" #d "], %[vo], %[rs], %[s" #k "] offen sc1\n1" #k ":\n\t"
#define G_LD2(k, a, b) "s_mov_b64 exec, %[m" #k "]\n\ts_cbranch_execz 1" #k "f\n\tbuffer_load_dwordx2 v[" #a ":" #b "], %[vo], %[rs], %[s" #k "] offen sc1\n1" #k ":\n\t"
#define G_CK0(k, a, b, c, d) "s_mov_b64 exec, %[m" #k "]\n\t" \
  "v_xor_b32 v" #a ", %[t0], v" #a "\n\tv_xor_b32 v" #b ", %[t1], v" #b "\n\tv_xor_b32 v" #c ", %[t2], v" #c "\n\tv_xor_b32 v" #d ", %[t3], v" #d "\n\t" \
  "v_or3_b32 %[tm], v" #a ", v" #b ", v" #c "\n\tv_bitop3_b32 %[tm], %[tm], v" #d ", %[ff] bitop3:0xa8\n\tv_cmp_ne_u32 %[m" #k "], 0, %[tm]\n\t"
#define G_CK2(k, a, b) "s_mov_b64 exec, %[m" #k "]\n\tv_max_u32 %[tm], v" #a ", v" #b "\n\tv_cmp_eq_u32 %[m" #k "], -1, %[tm]\n\t"
#define G_OR(k) "s_or_b64 %[ac], %[ac], %[m" #k "]\n\t"
#define G_TAIL(sl) "s_cbranch_scc0 9f\n\ts_sub_u32 %[cn], %[cn], 1\n\ts_cbranch_scc1 9f\n\ts_sleep " GX(sl) "\n\ts_branch 0b\n9:\n\ts_mov_b64 exec, -1\n\ts_nop 1"
#define G_M4 [m0] "+&s"(m[0]), [m1] "+&s"(m[1]), [m2] "+&s"(m[2]), [m3] "+&s"(m[3])
#define G_S4 [s0] "s"(so), [s1] "s"(so + step), [s2] "s"(so + 2 * step), [s3] "s"(so + 3 * step)
#define G_A4 "=&{v[128:131]}"(a[0]), "=&{v[132:135]}"(a[1]), "=&{v[136:139]}"(a[2]), "=&{v[140:143]}"(a[3])
#define G_TMP [tm] "=&v"(tm), [ac] "=&s"(ac), [cn] "+&s"(cn)
#define G_L4 G_LD4(0, 128, 131) G_LD4(1, 132, 135) G_LD4(2, 136, 139) G_LD4(3, 140, 143)
#define G_C4(C) C(0, 128, 129, 130, 131) C(1, 132, 133, 134, 135) C(2, 136, 137, 138, 139) C(3, 140, 141, 142, 143)
#define G_O4 "s_nop 3\n\ts_or_b64 %[ac], %[m0], %[m1]\n\t" G_OR(2) G_OR(3)
#define G_DECL(N) u64 m[N]; for (int i = 0; i < N; ++i) m[i] = ~0ull; unsigned tm, cn = POLL_MAX; u64 ac
#define G_T4 [t0] "s"(tb.x), [t1] "s"(tb.y), [t2] "s"(tb.z), [t3] "s"(tb.w), [ff] "s"(255u)
DEV void gather4f(v4u (&a)[4], rsrc_t r, unsigned vo, unsigned so, unsigned step, v4u tb) {
  G_DECL(4);
  asm volatile("0:\n\t" G_L4 "s_waitcnt vmcnt(0)\n\t" G_C4(G_CK0) G_O4 G_TAIL(PS)
               : G_A4, G_M4, G_TMP : [vo] "v"(vo), [rs] "s"(r), G_S4, G_T4 : "memory", "scc");
}
DEV void gather4s(v2u (&a)[4], rsrc_t r, unsigned vo, unsigned s0, unsigned s1, unsigned s2, unsigned s3) {
  G_DECL(4);
  asm volatile("0:\n\t" G_LD2(0, 128, 129) G_LD2(1, 130, 131) G_LD2(2, 132, 133) G_LD2(3, 134, 135) "s_waitcnt vmcnt(0)\n\t"
               G_CK2(0, 128, 129) G_CK2(1, 130, 131) G_CK2(2, 132, 133) G_CK2(3, 134, 135) G_O4 G_TAIL(PS)
               : "=&{v[128:129]}"(a[0]), "=&{v[130:131]}"(a[1]), "=&{v[132:133]}"(a[2]), "=&{v[134:135]}"(a[3]), G_M4, G_TMP
               : [vo] "v"(vo), [rs] "s"(r), [s0] "s"(s0), [s1] "s"(s1), [s2] "s"(s2), [s3] "s"(s3) : "memory", "scc");
}
struct Watch { unsigned r[4]; };
#define W_STEP(k, sl) "s_waitcnt vmcnt(3)\n\tv_cmp_ne_u32 vcc, %[tg], %[r" #k "]\n\ts_nop 0\n\ts_cbranch_vccz 9f\n\t" \
  "buffer_load_dword %[r" #k "], %[vo], %[rs], %[so] offen sc1\n\ts_sleep " GX(sl) "\n\t"
#define W_INIT(k, sl) "buffer_load_dword %[r" #k "], %[vo], %[rs], %[so] offen sc1\n\ts_sleep " GX(sl) "\n\t"
#define WATCH(name, sl) \
DEV Watch name(rsrc_t r, unsigned vo, unsigned so, unsigned tag) { \
  Watch w; unsigned cn = POLL_MAX; \
  asm volatile("s_mov_b64 exec, 0xff\n\t" W_INIT(0, sl) W_INIT(1, sl) W_INIT(2, sl) W_INIT(3, sl) \
               "0:\n\t" W_STEP(0, sl) W_STEP(1, sl) W_STEP(2, sl) W_STEP(3, sl) \
               "s_sub_u32 %[cn], %[cn], 1\n\ts_cbranch_scc0 0b\n9:\n\ts_mov_b64 exec, -1\n\ts_nop 1" \
               : [r0] "=&v"(w.r[0]), [r1] "=&v"(w.r[1]), [r2] "=&v"(w.r[2]), [r3] "=&v"(w.r[3]), [cn] "+&s"(cn) \
               : [vo] "v"(vo), [rs] "s"(r), [so] "s"(so), [tg] "s"(tag) : "memory", "scc", "vcc"); \
  return w; \
}
WATCH(watch_q, DQS)
WATCH(watch_m, DMS)
DEV void release_watch(Watch& w) { asm volatile("s_waitcnt vmcnt(0)" : : "v"(w.r[0]), "v"(w.r[1]), "v"(w.r[2]), "v"(w.r[3]) : "memory"); }
template <int N, int SL> DEV void gatherp(v4u (&a)[N], rsrc_t r, unsigned vo, unsigned so, unsigned step, unsigned tag) {
#pragma nounroll
  for (int it = 0; it < POLL_MAX; ++it) {
    cfence();
    unsigned bad = 0;
#pragma unroll
    for (int k = 0; k < N; ++k) a[k] = ld_dev(r, vo, so + k * step);
#pragma unroll
    for (int k = 0; k < N; ++k) bad |= (a[k].y ^ tag) | (a[k].w ^ tag);
    if (!__any(bad != 0)) break;
    __builtin_amdgcn_s_sleep(SL);
  }
  cfence();
}

template <int CTRL> DEV float dpp(float x) { return __builtin_bit_cast(float, __builtin_amdgcn_mov_dpp(__builtin_bit_cast(int, x), CTRL, 0xf, 0xf, true)); }
DEV float x16sum(float x) { auto s = __builtin_amdgcn_permlane16_swap(fbits(x), fbits(x), false, false); return bitsf(s[0]) + bitsf(s[1]); }
DEV float x32sum(float x) { auto s = __builtin_amdgcn_permlane32_swap(fbits(x), fbits(x), false, false); return bitsf(s[0]) + bitsf(s[1]); }
DEV float x16max(float x) { auto s = __builtin_amdgcn_permlane16_swap(fbits(x), fbits(x), false, false); return fmaxf(bitsf(s[0]), bitsf(s[1])); }
DEV float x32max(float x) { auto s = __builtin_amdgcn_permlane32_swap(fbits(x), fbits(x), false, false); return fmaxf(bitsf(s[0]), bitsf(s[1])); }
DEV float wave_sum(float x) {
  x += dpp<0xB1>(x); x += dpp<0x4E>(x); x += dpp<0x141>(x); x += dpp<0x140>(x);
  return x32sum(x16sum(x));
}
DEV float sum8up(float x) { x += dpp<0x128>(x); return x32sum(x16sum(x)); }
DEV float max8up(float x) { x = fmaxf(x, dpp<0x128>(x)); return x32max(x16max(x)); }
DEV v4u dup_lo(v4u v) { v4u o; for (int i = 0; i < 4; ++i) o[i] = __builtin_amdgcn_update_dpp(v[i], v[i], 0x128, 0xf, 0xc, false); return o; }
DEV v4u dup_hi(v4u v) { v4u o; for (int i = 0; i < 4; ++i) o[i] = __builtin_amdgcn_update_dpp(v[i], v[i], 0x128, 0xf, 0x3, false); return o; }
DEV float fdiv(float a, float d, float rd) { float q = a * rd; return fmaf(fmaf(-q, d, a), rd, q); }
DEV float dot2(unsigned a, unsigned b, float c) { return __builtin_amdgcn_fdot2_f32_bf16(__builtin_bit_cast(v2bf, a), __builtin_bit_cast(v2bf, b), c, false); }
struct Stat { float mb, sd, rd; };
DEV Stat mk_stat(float m, float var) {
  Stat st;
  st.mb = rbf(m);
  st.sd = rbf(__builtin_amdgcn_sqrtf(rbf(rbf(var) + 1e-5f)));
  st.rd = __builtin_amdgcn_rcpf(st.sd);
  return st;
}
DEV Stat ln_stat8(const unsigned (&w)[8]) {
  float s = 0.f, s2 = 0.f, q = 0.f, q2 = 0.f;
#pragma unroll
  for (int i = 0; i < 8; i += 2) {
    s = dot2(w[i], 0x3f803f80u, s); q = dot2(w[i], w[i], q);
    s2 = dot2(w[i + 1], 0x3f803f80u, s2); q2 = dot2(w[i + 1], w[i + 1], q2);
  }
  const float m = wave_sum(s + s2) * (1.f / 1024.f);
  float var = fmaf(-m, m, wave_sum(q + q2) * (1.f / 1024.f));
  if (__builtin_amdgcn_readfirstlane(!(m * m <= 16.f * var))) {
    q = 0.f;
#pragma unroll
    for (int i = 0; i < 8; ++i) { float a = bflo(w[i]) - m, b = bfhi(w[i]) - m; q = fmaf(a, a, q); q = fmaf(b, b, q); }
    var = wave_sum(q) * (1.f / 1024.f);
  }
  return mk_stat(m, var);
}
DEV unsigned ln_pair(unsigned x, const Stat& st) {
  unsigned d = pk(bflo(x) - st.mb, bfhi(x) - st.mb);
  return pk(fdiv(bflo(d), st.sd, st.rd), fdiv(bfhi(d), st.sd, st.rd));
}
DEV unsigned onep(unsigned c) { return pk(1.f + bflo(c), 1.f + bfhi(c)); }
DEV unsigned mod_pair(unsigned ln, unsigned c1, unsigned s) {
  unsigned p = pk(bflo(ln) * bflo(c1), bfhi(ln) * bfhi(c1));
  return pk(bflo(p) + bflo(s), bfhi(p) + bfhi(s));
}
DEV unsigned res_pair(unsigned x, unsigned g, float a, float b) {
  unsigned y = pk(a, b);
  unsigned p = pk(bflo(g) * bflo(y), bfhi(g) * bfhi(y));
  return pk(bflo(x) + bflo(p), bfhi(x) + bfhi(p));
}
DEV float silu1(float v) {
  const float d = 1.f + __expf(-v);
  return __builtin_amdgcn_div_fixupf(fdiv(v, d, __builtin_amdgcn_rcpf(d)), d, v);
}

#define STAMP(k)
#define STAMP_DUMP()

DEV v4f unit_reduce(v4f a) {
#pragma unroll
  for (int i = 0; i < 4; ++i) a[i] = sum8up(a[i]);
  return a;
}
DEV void wg_barrier() { asm volatile("s_waitcnt lgkmcnt(0)\n\ts_barrier" ::: "memory"); }
DEV v4f mm4(const v4u (&w)[4], unsigned bb) {
  v4u b[4];
#pragma unroll
  for (int kb = 0; kb < 4; ++kb) b[kb] = lds_ld<v4u>(bb + kb * 64);
  const v4f z = {0.f, 0.f, 0.f, 0.f};
  v4f d0 = mfma(w[0], b[0], z), d1 = mfma(w[1], b[1], z);
  d0 = mfma(w[2], b[2], d0); d1 = mfma(w[3], b[3], d1);
  return d0 + d1;
}

__global__ void __launch_bounds__(NTHR, 3)
fused_kernel(const void* __restrict__ px, const void* __restrict__ pk_, const void* __restrict__ pv, const void* __restrict__ pwq,
             const void* __restrict__ pwo, const void* __restrict__ pwg, const void* __restrict__ pwu, const void* __restrict__ pwd,
             const void* __restrict__ pc, const void* __restrict__ pwm, void* __restrict__ pout, void* __restrict__ pws) {
  const int lane = threadIdx.x & 63;
  const int wave = __builtin_amdgcn_readfirstlane(threadIdx.x >> 6);
  const int wg = blockIdx.x;
  const int n = lane & 15, g = lane >> 4;
  asm volatile("" : : "s"(px), "s"(pk_), "s"(pv), "s"(pwq), "s"(pwo), "s"(pwg), "s"(pwu), "s"(pwd), "s"(pc), "s"(pwm), "s"(pout), "s"(pws));
  const rsrc_t rws = mk(pws, WS_BYTES);
  const int bk = wg & 31, sp = wg >> 5;
  const int ab = bk >> 2, kvh = bk & 3;
  const int role = wg < 96 ? 0 : wg < 160 ? 1 : 2;
  const bool ffn = wg >= 32;
  const int fi = wg - 32;
  const int gks = fi & 3, gct = fi >> 2;
  const int dj = fi & 7, dft = fi >> 3;
  const int oh = wg >> 3, oj = wg & 7;
  const int ub = wg >> 5, uu = wg & 31;

  STAMP(0);
  const int tq = (lane >> 2) & 3, tp = lane & 3;
  const bool hi = n >= 8;
  unsigned ep;

  if (wave < 8) {
    const rsrc_t rk = mk((const char*)pk_ + (size_t)bk * 2097152 + sp * 262144 + wave * 32768, 32768);
    const rsrc_t rv = mk((const char*)pv + (size_t)bk * 2097152 + sp * 262144 + wave * 32768, 32768);
    const unsigned rot = wave + wg;
    if (DK0 && wg < 32) __builtin_amdgcn_s_sleep(DK0);
    v4u kf[8][4];
    const unsigned kva = ((n & 3) * 4 + ((n >> 2) & 1)) * 256 + g * 16 + (n >> 3) * 64, kvb = kva ^ 64;
#pragma unroll
    for (int i = 0; i < 32; ++i) {
      const int o = i >> 2, t = (o & 4) | ((o & 1) << 1) | ((o >> 1) & 1);
      if (i == WK) {
        asm volatile("s_barrier\n\ts_load_dword %0, %1, 0x0" : "=s"(ep) : "s"(pws) : "memory");
      }
      if (i >= WK) vmwait<WK - 1>();
      kf[t][i & 3] = ld_nt(rk, ((t & 2) ? kvb : kva) + ((i >> 1) & 1) * 512 + (i & 1) * 128, ((t + rot) & 7) * 4096);
      cfence();
    }
    STAMP(1);
    lds_wait<2>(F_Q, 1);
    asm volatile("s_waitcnt lgkmcnt(0)" : "+s"(ep) : : "memory");
    const v4u tb = tagbytes(ep + 0x01010101u);
    STAMP(2);
    v4u qf[2];
    {
      const unsigned qa = opq(L_QB + (n & 7) * 288 + (n >> 3) * 64 + g * 16);
      qf[0] = lds_ld<v4u>(qa); qf[1] = lds_ld<v4u>(qa + 128);
    }
    const unsigned vvo = lane * 16;
    v4u vf[4][8];
    auto vload = [&](int i) {
      SCHED();
      vmwait<WV - 1>();
      vf[i >> 3][i & 7] = ld_nt(rv, vvo + (i & 3) * 1024, (((i >> 2) + rot) & 7) * 4096);
      cfence();
      SCHED();
    };
    unsigned sc[4][2];
    float mx = -INFINITY;
    const bool va = (g >> 1) == (n >> 3);
#pragma unroll
    for (int q = 0; q < 4; ++q) {
      const int ta = (q >> 1) * 4 + (q & 1), tbb = ta + 2;
      const v4f z = {0.f, 0.f, 0.f, 0.f};
      v4f a0 = mfma(kf[ta][0], qf[0], z), b0 = mfma(kf[tbb][0], qf[0], z), a1 = mfma(kf[ta][2], qf[0], z), b1 = mfma(kf[tbb][2], qf[0], z);
      a0 = mfma(kf[ta][1], qf[1], a0); b0 = mfma(kf[tbb][1], qf[1], b0); a1 = mfma(kf[ta][3], qf[1], a1); b1 = mfma(kf[tbb][3], qf[1], b1);
      if (q < 3) { vload(2 * q); vload(2 * q + 1); }
      const v4f e0 = sel4(va, a0, b0), e1 = sel4(va, a1, b1);
      float s[4];
#pragma unroll
      for (int r = 0; r < 4; ++r) {
        auto w = __builtin_amdgcn_permlane32_swap(fbits(e0[r]), fbits(e1[r]), false, false);
        s[r] = bitsf(w[0]) + dpp<0x128>(bitsf(w[1]));
      }
#pragma unroll
      for (int r = 0; r < 2; ++r) {
        unsigned u = pk(s[2 * r], s[2 * r + 1]);
        u = pk(bflo(u) * 0.08838834764831845f, bfhi(u) * 0.08838834764831845f);
        sc[q][r] = u;
        mx = fmaxf(mx, fmaxf(bflo(u), bfhi(u)));
      }
    }
    vload(6);
    STAMP(3);
    mx = max8up(mx);
    float ls = 0.f;
    v4u pf[4];
#pragma unroll
    for (int b = 0; b < 2; ++b) {
      v4u p;
#pragma unroll
      for (int e = 0; e < 4; ++e) {
        const unsigned u = sc[2 * b + (e >> 1)][e & 1];
        const float p0 = __expf(bflo(u) - mx), p1 = __expf(bfhi(u) - mx);
        ls += p0 + p1;
        p[e] = pk(p0, p1);
      }
      pf[2 * b] = dup_lo(p); pf[2 * b + 1] = dup_hi(p);
      vload(7 + b);
    }
    ls = sum8up(ls);
    if (lane < 8) { lds_st<float>(L_MX + wave * 32 + lane * 4, mx); lds_st<float>(L_LS + wave * 32 + lane * 4, ls); }
    lds_signal(F_KML, lane);
    STAMP(4);
    v4f acc[8];
    auto piece = [&](int k) {
      const int p = k >> 3, i = k & 7;
      v4u a;
#pragma unroll
      for (int e = 0; e < 4; ++e) a[e] = __builtin_amdgcn_perm(vf[p][2 * e + 1][i >> 1], vf[p][2 * e][i >> 1], (i & 1) ? 0x07060302u : 0x05040100u);
      acc[i] = mfma(a, pf[p], p ? acc[i] : (v4f){0.f, 0.f, 0.f, 0.f});
    };
#pragma unroll
    for (int i = 9; i < 32; ++i) {
      vload(i);
      if (i >= 7 + WV) piece(i - 7 - WV);
    }
#pragma unroll
    for (int k = 25 - WV; k < 32; ++k) piece(k);
    STAMP(5);
    {
      const int hd = n & 7;
      const unsigned wa = opq(L_DN + wave * 4096 + hd * 512 + g * 128);
#pragma unroll
      for (int i = 0; i < 4; ++i) lds_st<v4f>(wa + (((i + 4 * (n >> 3)) ^ hd) << 4), sel4(hi, acc[i + 4], acc[i]));
      const int ci = lane & 31;
      const unsigned a0 = opq(L_DN + (lane >> 5) * 16384 + wave * 512 + (ci ^ wave) * 16);
      const unsigned fa = opq(L_WT + wave * 32 + (lane >> 5) * 16);
      wg_barrier();
      const v4f f = lds_ld<v4f>(fa);
      v4f s0 = lds_ld<v4f>(a0), s1 = lds_ld<v4f>(a0 + 4096), s2 = lds_ld<v4f>(a0 + 8192), s3 = lds_ld<v4f>(a0 + 12288);
      v4f s = (s0 * f[0] + s1 * f[1]) + (s2 * f[2] + s3 * f[3]);
#pragma unroll
      for (int i = 0; i < 4; ++i) s[i] = x32sum(s[i]);
      if (lane < 32) st_dev(tag4(s, tb), rws, ci * 16, O_AO + (kvh * 8 + wave) * 32768 + ab * 4096 + sp * 512);
    }
    STAMP(6);
    v4u wof[4], wgf[8];
    const unsigned tw = (8 * g + tq) * 256 + (((2 * wave + (tp >> 1)) ^ ((tq | ((g & 1) << 2)) << 1)) << 4) + 8 * (tp & 1);
    {
      const unsigned b1 = opq(L_W + tw);
#pragma unroll
      for (int kb = 0; kb < 4; ++kb) wof[kb] = trfrag(b1 + kb * 8192, 1024);
      const unsigned sw = ((tq >> 1) & 1) | ((g & 1) << 1);
      const unsigned b2 = opq(L_G + (wave >> 2) * 32768 + (8 * g + tq) * 128 + (((2 * (wave & 3) + (tp >> 1)) ^ (sw << 1)) << 4) + 8 * (tp & 1));
#pragma unroll
      for (int kb = 0; kb < 8; ++kb) wgf[kb] = trfrag(b2 + kb * 4096, 512);
    }
    const unsigned ua = opq(L_UN + (lane & 7) * 8);
    const v2u xu = lds_ld<v2u>(ua), gau = lds_ld<v2u>(ua + 64), gbu = lds_ld<v2u>(ua + 128);
    const unsigned sa_ = opq(L_SB + wave * 512 + lane * 8);
    const v2u sbv = lds_ld<v2u>(sa_), cbv = lds_ld<v2u>(sa_ + 4096);
    const unsigned bb = opq(L_A1 + (n & 7) * 288 + g * 16);
    {
      const unsigned wa = opq(L_A1 + wave * 288 + (lane & 31) * 8);
      const unsigned po = (n & 7) * 131072 + g * 16;
      if (D1) __builtin_amdgcn_s_sleep(D1);
      v4u a[4];
      gather4f(a, rws, lane * 16, O_AO + oh * 32768 + wave * 4096, 1024, tb);
      STAMP(7);
      v4f o = (asf4(a[0]) + asf4(a[1])) + (asf4(a[2]) + asf4(a[3]));
#pragma unroll
      for (int i = 0; i < 4; ++i) o[i] = x32sum(o[i]);
      if (lane < 32) lds_st<v2u>(wa, (v2u){pk(o[0], o[1]), pk(o[2], o[3])});
      wg_barrier();
      const v4f d = mm4(wof, bb);
      if (n < 8) st_dev(tag4(d, tb), rws, po, O_WP + (oj * 4 + (wave >> 1)) * 4096 + oh * 128 + (wave & 1) * 64);
    }
    STAMP(8);
    v2u x1u = {0u, 0u};
    if (wave < 4) {
      if (D2) __builtin_amdgcn_s_sleep(D2);
      if (wave & 1) __builtin_amdgcn_s_sleep(D2S);
      if (wave & 2) __builtin_amdgcn_s_sleep(2 * D2S);
      v4u a[4];
      gather4f(a, rws, lane * 16, O_WP + wg * 4096, 1024, tb);
      STAMP(9);
      const v4f s = unit_reduce((asf4(a[0]) + asf4(a[1])) + (asf4(a[2]) + asf4(a[3])));
      x1u.x = canon(res_pair(xu.x, gau.x, s[0], s[1]));
      x1u.y = canon(res_pair(xu.y, gau.y, s[2], s[3]));
      if (lane < 8) st_dev2(x1u, rws, lane * 8, O_X1 + wg * 64);
      STAMP(10);
      if (D3B) __builtin_amdgcn_s_sleep(D3B);
    } else if (ffn) {
      const rsrc_t rd = mk(pwd, 3584 * 2048);
      const unsigned c0 = g * 2048 + ((n ^ (g << 1)) << 4), c1 = c0 ^ 128;
#pragma unroll
      for (int i = 0; i < 8; ++i)
        dma16(rd, (i & 2) ? c1 : c0, (dft * 128 + (wave - 4) * 32 + i * 4) * 2048 + dj * 256, L_DN + ((wave - 4) * 8 + i) * 1024);
      if (D3) __builtin_amdgcn_s_sleep(D3);
    }
    if (ffn) {
      {
        const unsigned wa = opq(L_A3 + wave * 544 + lane * 8), b3 = opq(L_A3 + (n & 7) * 544 + g * 16);
        const unsigned po = n * 64 + g * 16;
        v2u xv[4];
        const unsigned xo = O_X1 + wave * 2048;
        gather4s(xv, rws, lane * 8, xo + gks * 512, xo + ((gks + 1) & 3) * 512, xo + ((gks + 2) & 3) * 512, xo + ((gks + 3) & 3) * 512);
        const unsigned xr[8] = {xv[0].x, xv[0].y, xv[1].x, xv[1].y, xv[2].x, xv[2].y, xv[3].x, xv[3].y};
        STAMP(11);
        const Stat st = ln_stat8(xr);
        v2u h2;
        h2.x = mod_pair(ln_pair(xr[0], st), cbv.x, sbv.x);
        h2.y = mod_pair(ln_pair(xr[1], st), cbv.y, sbv.y);
        lds_st<v2u>(wa, h2);
        wg_barrier();
        v4u b[8];
#pragma unroll
        for (int kb = 0; kb < 8; ++kb) b[kb] = lds_ld<v4u>(b3 + kb * 64);
        const v4f z = {0.f, 0.f, 0.f, 0.f};
        v4f d0 = mfma(wgf[0], b[0], z), d1 = mfma(wgf[1], b[1], z);
#pragma unroll
        for (int kb = 2; kb < 8; kb += 2) { d0 = mfma(wgf[kb], b[kb], d0); d1 = mfma(wgf[kb + 1], b[kb + 1], d1); }
        d0 += d1;
        if (n < 8) st_dev(tag4(d0, tb), rws, po, O_GP + (gct >> 1) * 32768 + ((gct & 1) * 4 + (wave & 3)) * 4096 + gks * 1024 + (wave >> 2) * 512);
      }
      STAMP(12);
      {
        v4u wdf[4];
        const unsigned b1 = opq(L_DN + tw);
#pragma unroll
        for (int kb = 0; kb < 4; ++kb) wdf[kb] = trfrag(b1 + kb * 8192, 1024);
        const unsigned wa = opq(L_A4 + ((lane >> 2) & 7) * 288 + wave * 32 + (lane & 3) * 8);
        const unsigned po = (n & 7) * (32 * 3584) + g * 16;
        if (D4) __builtin_amdgcn_s_sleep(D4);
        v4u a[4];
        gather4f(a, rws, lane * 16, O_GP + dft * 32768 + wave * 4096, 1024, tb);
        STAMP(13);
        const v4f t = (asf4(a[0]) + asf4(a[1])) + (asf4(a[2]) + asf4(a[3]));
        const unsigned t0 = pk(t[0], t[1]), t1 = pk(t[2], t[3]);
        auto w0 = __builtin_amdgcn_permlane32_swap(t0, t0, false, false), w1 = __builtin_amdgcn_permlane32_swap(t1, t1, false, false);
        auto relu = [](float v) { return v > 0.f ? v : v * 0.f; };
        const v2u hv = {pk(relu(bflo(w0[0])) * bflo(w0[1]), relu(bfhi(w0[0])) * bfhi(w0[1])),
                        pk(relu(bflo(w1[0])) * bflo(w1[1]), relu(bfhi(w1[0])) * bfhi(w1[1]))};
        if (lane < 32) lds_st<v2u>(wa, hv);
        wg_barrier();
        const v4f d = mm4(wdf, bb + (L_A4 - L_A1));
        if (n < 8) st_dev(tag4(d, tb), rws, po, O_DP + (dj * 4 + (wave >> 1)) * 3584 + dft * 128 + (wave & 1) * 64);
      }
      STAMP(14);
    } else {
      __builtin_amdgcn_s_sleep(90);
    }
    if (wave < 4) {
      if (D5) __builtin_amdgcn_s_sleep(D5);
      if (wave & 1) __builtin_amdgcn_s_sleep(D2S);
      if (wave & 2) __builtin_amdgcn_s_sleep(2 * D2S);
      v4u a[4];
      gather4f(a, rws, (lane < 56 ? lane : lane - 8) * 16, O_DP + wg * 3584, 896, tb);
      v4f t = (asf4(a[0]) + asf4(a[1])) + (asf4(a[2]) + asf4(a[3]));
      if (lane >= 56) t = (v4f){0.f, 0.f, 0.f, 0.f};
      const v4f s = unit_reduce(t);
      const v2u o = {res_pair(x1u.x, gbu.x, s[0], s[1]), res_pair(x1u.y, gbu.y, s[2], s[3])};
      if (lane < 8) __builtin_amdgcn_raw_buffer_store_b64(o, mk(pout, 16384), lane * 8, wg * 64, 0);
      if (wg == 0 && wave == 0 && lane == 0) __builtin_amdgcn_raw_buffer_store_b32(ep + 0x01010101u, rws, 0, WS_EPOCH, 16);
      STAMP(15);
    }
    STAMP_DUMP();
    return;
  }

  const int cw = wave - 8;
  __builtin_amdgcn_s_setprio(2);
  unsigned cb_n = 0;
  const unsigned sw64 = (((lane & 7) ^ (((lane >> 4) & 1) << 1)) << 4);
  if (role != 2) {
    v4u in[4];
    {
      const rsrc_t ri = mk(role == 0 ? pc : px, 16384);
      const unsigned vo = (n & 7) * 2048 + cw * 512 + (n >> 3) * 64 + g * 16;
      asm volatile("buffer_load_dwordx4 %0, %4, %5, 0 offen\n\tbuffer_load_dwordx4 %1, %4, %5, 0 offen offset:128\n\t"
                   "buffer_load_dwordx4 %2, %4, %5, 0 offen offset:256\n\tbuffer_load_dwordx4 %3, %4, %5, 0 offen offset:384"
                   : "=&v"(in[0]), "=&v"(in[1]), "=&v"(in[2]), "=&v"(in[3]) : "v"(vo), "s"(ri) : "memory");
    }
    const unsigned rowb = role == 0 ? 12288 : 8192;
    const rsrc_t rh = mk(role == 0 ? pwm : pwq, 1024 * rowb);
    const unsigned col = (role == 0 ? wg : wg - 96) * 128 + cw * 256 * rowb;
    const unsigned v0 = (lane >> 3) * rowb + sw64, v1 = v0 ^ 64;
    if (!ffn) {
      if (cw == 0 && lane < 32) st_dev((v4u){SENT, SENT, SENT, SENT}, rws, lane * 16, O_X1 + wg * 512);
#pragma unroll
      for (int k = 0; k < 24; ++k) dma16(rh, (k & 1) ? v1 : v0, k * 8 * rowb + col, (cw * 32 + k) * 1024);
    }
    const unsigned k0 = ffn ? 0 : 24;
#pragma unroll
    for (int k = 0; k < 8; ++k) dma16(rh, (k & 1) ? v1 : v0, (k0 + k) * 8 * rowb + col, (cw * 32 + k0 + k) * 1024);
    if (cw == 0 && lane < 32) lds_st<unsigned>(L_FL + 4 * lane, 0u);
    asm volatile("s_waitcnt lgkmcnt(0)\n\ts_barrier\n\ts_load_dword %0, %1, 0x0" : "=s"(ep) : "s"(pws) : "memory");
    if (ffn) vmwait<8>(); else vmwait<32>();
    asm volatile("" : "+v"(in[0]), "+v"(in[1]), "+v"(in[2]), "+v"(in[3]) : : "memory");
    STAMP(1);
    if (role == 0) {
#pragma unroll
      for (int p = 0; p < 4; ++p)
#pragma unroll
        for (int e = 0; e < 4; ++e) in[p][e] = pk(silu1(bflo(in[p][e])), silu1(bfhi(in[p][e])));
    } else {
      float s = 0.f;
#pragma unroll
      for (int p = 0; p < 4; ++p)
#pragma unroll
        for (int e = 0; e < 4; ++e) s += bflo(in[p][e]) + bfhi(in[p][e]);
      s = sum8up(s);
      if (lane < 8) lds_st<float>(L_SX + lane * 16 + cw * 4, s);
      lds_signal(F_CBAR, lane); cb_n += 4; lds_wait<1>(F_CBAR, cb_n);
      v4f t = lds_ld<v4f>(L_SX + (n & 7) * 16);
      const float m = ((t[0] + t[1]) + (t[2] + t[3])) * (1.f / 1024.f);
      s = 0.f;
#pragma unroll
      for (int p = 0; p < 4; ++p)
#pragma unroll
        for (int e = 0; e < 4; ++e) { float a = bflo(in[p][e]) - m, b = bfhi(in[p][e]) - m; s = fmaf(a, a, s); s = fmaf(b, b, s); }
      s = sum8up(s);
      if (lane < 8) lds_st<float>(L_SX + 128 + lane * 16 + cw * 4, s);
      lds_signal(F_CBAR, lane); cb_n += 4; lds_wait<1>(F_CBAR, cb_n);
      t = lds_ld<v4f>(L_SX + 128 + (n & 7) * 16);
      const Stat st = mk_stat(m, ((t[0] + t[1]) + (t[2] + t[3])) * (1.f / 1024.f));
#pragma unroll
      for (int p = 0; p < 4; ++p)
#pragma unroll
        for (int e = 0; e < 4; ++e) in[p][e] = ln_pair(in[p][e], st);
    }
    STAMP(2);
    if (ffn) {
#pragma unroll
      for (int k = 8; k < 32; ++k) {
        if (role == 1) vmwait<WH1 - 1>(); else vmwait<WH0 - 1>();
        dma16(rh, (k & 1) ? v1 : v0, k * 8 * rowb + col, (cw * 32 + k) * 1024);
      }
    }
    asm volatile("s_waitcnt lgkmcnt(0)" : "+s"(ep) : : "memory");
    const unsigned TAG = ep + 0x01010101u;
    const unsigned sw = ((tq >> 1) & 1) | ((g & 1) << 1);
    const unsigned tb0 = cw * 32768 + (8 * g + tq) * 128 + (((tp >> 1) ^ (sw << 1)) << 4) + 8 * (tp & 1);
    unsigned tba[4];
#pragma unroll
    for (int t = 0; t < 4; ++t) tba[t] = opq(tb0 ^ (t << 5));
    v4u wf[4][4];
#pragma unroll
    for (int kb = 0; kb < 4; ++kb) {
      if (kb == 0) vmwait<28>(); if (kb == 1) vmwait<24>(); if (kb == 2) vmwait<20>(); if (kb == 3) vmwait<16>();
#pragma unroll
      for (int t = 0; t < 4; ++t) wf[kb][t] = trfrag(tba[t] + kb * 4096, 512);
    }
    STAMP(3);
    if (role == 1) {
      Watch w = watch_m(rws, ((lane & 4) ? 32768 : 0) + (lane & 3) * 2048 + 4, O_SC + cw * 8192, TAG);
      STAMP(4);
      v4u a[16];
#pragma nounroll
      for (int it = 0; it < POLL_MAX; ++it) {
        cfence();
        unsigned bad = 0;
#pragma unroll
        for (int k = 0; k < 16; ++k) a[k] = ld_dev(rws, lane * 16 + (k & 3) * 1024, O_SC + cw * 8192 + ((k >> 2) & 1) * 4096 + (k >> 3) * 32768);
#pragma unroll
        for (int k = 0; k < 16; ++k) bad |= (a[k].y ^ TAG) | (a[k].w ^ TAG);
        if (!__any(bad != 0)) break;
        __builtin_amdgcn_s_sleep(DMS);
      }
      release_watch(w);
      STAMP(5);
#pragma unroll
      for (int p = 0; p < 4; ++p)
#pragma unroll
        for (int e = 0; e < 4; ++e) in[p][e] = mod_pair(in[p][e], onep(a[8 + 2 * p + (e >> 1)][(e & 1) * 2]), a[2 * p + (e >> 1)][(e & 1) * 2]);
    }
    v4f d[4];
#pragma unroll
    for (int t = 0; t < 4; ++t) d[t] = (v4f){0.f, 0.f, 0.f, 0.f};
#pragma unroll
    for (int kb = 0; kb < 8; ++kb) {
      const v4u b = (kb & 1) ? dup_hi(in[kb >> 1]) : dup_lo(in[kb >> 1]);
#pragma unroll
      for (int t = 0; t < 4; ++t) d[t] = mfma(wf[kb & 3][t], b, d[t]);
      if (kb < 4) {
        SCHED();
        if (kb == 0) vmwait<12>(); if (kb == 1) vmwait<8>(); if (kb == 2) vmwait<4>(); if (kb == 3) vmwait<0>();
#pragma unroll
        for (int t = 0; t < 4; ++t) wf[kb][t] = trfrag(tba[t] + (kb + 4) * 4096, 512);
        SCHED();
      }
    }
    const unsigned ra = L_RED + cw * 2048 + (n >> 3) * 1024 + (n & 7) * 64 + g * 16;
    lds_st<v4f>(ra, sel4(hi, d[2], d[0]));
    lds_st<v4f>(ra + 512, sel4(hi, d[3], d[1]));
    lds_signal(F_CBAR, lane); cb_n += 4; lds_wait<0>(F_CBAR, cb_n);
    const unsigned rb = L_RED + (lane >> 5) * 4096 + cw * 512 + (lane & 31) * 16;
    v4f s = lds_ld<v4f>(rb) + lds_ld<v4f>(rb + 2048);
#pragma unroll
    for (int i = 0; i < 4; ++i) s[i] = x32sum(s[i]);
    const unsigned vo = ffn ? (lane >> 2) * 16384 + (lane & 3) * 16 : (lane & 1) * 1024 + ((lane >> 1) & 1) * 256 + (lane >> 2) * 16;
    const unsigned so = ffn ? (role == 0 ? O_MOD + (wg - 32) * 256 : O_Q + (wg - 96) * 256) + cw * 64 : O_SC + wg * 2048 + (cw & 1) * 512 + (cw >> 1) * 128;
    if (lane < 32) st_dev((v4u){pk(s[0], s[1]), TAG, pk(s[2], s[3]), TAG}, rws, vo, so);
    STAMP(6);
  } else {
    if (cw == 0 && lane < 32) lds_st<unsigned>(L_FL + 4 * lane, 0u);
    asm volatile("s_waitcnt lgkmcnt(0)\n\ts_barrier\n\ts_load_dword %0, %1, 0x0\n\ts_waitcnt lgkmcnt(0)" : "=s"(ep) : "s"(pws) : "memory");
  }
  const unsigned TAG = ep + 0x01010101u;
  if (cw == 3) {
    if (role == 2) { __builtin_amdgcn_s_sleep(DQ0); __builtin_amdgcn_s_sleep(DQ0); }
    const unsigned qo = O_Q + ab * 16384 + kvh * 4096;
    Watch w = watch_q(rws, (lane & 7) * 256 + ((wg >> 5) & 1) * 2048 + 4, qo, TAG);
    STAMP(7);
    v4u a[4];
    gatherp<4, 8>(a, rws, lane * 16, qo, 1024, TAG);
    release_watch(w);
#pragma unroll
    for (int k = 0; k < 4; ++k) lds_st<v2u>(L_QB + (2 * k + (lane >> 5)) * 288 + (lane & 31) * 8, (v2u){a[k].x, a[k].z});
    lds_signal(F_Q, lane);
    STAMP(8);
    lds_wait<8>(F_KML, 8);
    const float mw = lds_ld<float>(L_MX + lane * 4), lw = lds_ld<float>(L_LS + lane * 4);
    const float ms = max8up(mw);
    const float lsum = sum8up(lw * __expf(mw - ms));
    if (lane < 8) st_dev((v4u){fbits(ms), TAG, fbits(lsum), TAG}, rws, lane * 16, O_ML + bk * 1024 + sp * 128);
    STAMP(9);
    __builtin_amdgcn_s_sleep(32);
    v4u ml[1];
    gatherp<1, 24>(ml, rws, lane * 16, O_ML + bk * 1024, 0, TAG);
    const float M = max8up(bitsf(ml[0].x));
    const float L = sum8up(bitsf(ml[0].z) * __expf(bitsf(ml[0].x) - M));
    lds_st<float>(L_WT + (lane & 7) * 32 + (lane >> 3) * 4, __expf(mw - M) / L);
    STAMP(10);
  } else if (cw == 2) {
    asm volatile("s_waitcnt lgkmcnt(0)" ::: "memory");
    const rsrc_t ro = mk(pwo, 4096 * 2048);
    const unsigned c0 = g * 8 * 2048 + ((n ^ (g << 1)) << 4), c1 = c0 ^ 128;
#pragma unroll
    for (int i = 0; i < 32; ++i) {
      if (i >= WF) vmwait<WF - 1>();
      dma16(ro, (i & 2) ? c1 : c0, (oh * 128 + (i >> 3) * 32 + (i & 7)) * 2048 + oj * 256, L_W + i * 1024);
    }
    STAMP(7);
    const v2u xu = __builtin_amdgcn_raw_buffer_load_b64(mk(px, 16384), (lane & 7) * 8, ub * 2048 + uu * 64, 0);
    v4u a[1];
    gatherp<1, 32>(a, rws, (lane & 7) * 16 + ((lane >> 3) & 1) * 12288, O_MOD + ub * 16384 + uu * 128, 0, TAG);
    if (lane < 8) lds_st<v2u>(L_UN + lane * 8, xu);
    if (lane < 16) lds_st<v2u>(L_UN + 64 + lane * 8, (v2u){a[0].x, a[0].z});
    STAMP(8);
  } else if (ffn) {
    asm volatile("s_waitcnt lgkmcnt(0)" ::: "memory");
    const rsrc_t rg = mk(cw ? pwu : pwg, 1024 * 3584 * 2);
    const unsigned v0 = (lane >> 3) * 7168 + sw64, v1 = v0 ^ 64;
#pragma unroll
    for (int i = 0; i < 32; ++i) {
      if (i >= WF) vmwait<WF - 1>();
      dma16(rg, (i & 1) ? v1 : v0, (gks * 256 + i * 8) * 7168 + gct * 128, L_G + cw * 32768 + i * 1024);
    }
    STAMP(7);
    v4u a[8];
    gatherp<8, 32>(a, rws, lane * 16, O_MOD + 4096 + cw * 4096 + gks * 1024, 16384, TAG);
#pragma unroll
    for (int k = 0; k < 8; ++k)
      lds_st<v2u>(L_SB + cw * 4096 + k * 512 + lane * 8, cw ? (v2u){onep(a[k].x), onep(a[k].z)} : (v2u){a[k].x, a[k].z});
    STAMP(8);
  }
  STAMP_DUMP();
  asm volatile("s_waitcnt vmcnt(0) lgkmcnt(0)" ::: "memory");
}

extern "C" void kernel_launch(void* const* d_in, const int* in_sizes, int n_in, void* d_out, int out_size, void* d_ws, size_t ws_size,
                              hipStream_t stream) {
  (void)in_sizes; (void)n_in; (void)out_size; (void)ws_size;
  (void)hipFuncSetAttribute((const void*)fused_kernel, hipFuncAttributeMaxDynamicSharedMemorySize, L_END);
  fused_kernel<<<NWG, NTHR, L_END, stream>>>(d_in[0], d_in[1], d_in[2], d_in[3], d_in[4], d_in[6], d_in[7], d_in[8], d_in[9], d_in[10],
                                             d_out, d_ws);
}
```

```cpp
#include <hip/hip_runtime.h>
#include <stdint.h>

typedef unsigned v4u __attribute__((ext_vector_type(4)));
typedef unsigned v2u __attribute__((ext_vector_type(2)));
typedef float v4f __attribute__((ext_vector_type(4)));
typedef float v2f __attribute__((ext_vector_type(2)));
typedef __bf16 v2bf __attribute__((ext_vector_type(2)));
typedef __bf16 v8bf __attribute__((ext_vector_type(8)));
typedef short v4s __attribute__((ext_vector_type(4)));
#define LAS __attribute__((address_space(3)))
typedef __amdgpu_buffer_rsrc_t rsrc_t;
#define DEV __device__ __forceinline__

#define WK 4
#define WV 4
#define WH1 12
#define WH0 4
#define WF 3
#define DK0 40
#define DQ0 90
#define DQS 16
#define DMS 6
#define D1 8
#define D2 8
#define D2S 5
#define D3 40
#define D3B 8
#define D4 8
#define D5 8
#define NODN 0
#define PRET 0
#define WF1 WF
#define CPF 0
#define PS 1

constexpr int NWG = 256, NTHR = 768;
constexpr unsigned SENT = 0xFFFFFFFFu;
constexpr unsigned WS_EPOCH = 0;
constexpr unsigned O_SC = 65536;
constexpr unsigned O_MOD = O_SC + 65536;
constexpr unsigned O_Q = O_MOD + 131072;
constexpr unsigned O_ML = O_Q + 131072;
constexpr unsigned O_X1 = O_ML + 32768;
constexpr unsigned O_AO = O_X1 + 16384;
constexpr unsigned O_WP = O_AO + 1048576;
constexpr unsigned O_GP = O_WP + 1048576;
constexpr unsigned O_DP = O_GP + 28 * 32768;
constexpr unsigned O_STAMP = 65536 + 256 * 16384 + 1048576;
static_assert(O_DP + 256 * 28 * 128 <= O_STAMP, "ws map");
constexpr unsigned WS_BYTES = O_STAMP + NWG * 12 * 16 * 8;
constexpr unsigned L_G = 0, L_W = 65536, L_DN = 98304;
constexpr unsigned L_M = 131072;
constexpr unsigned L_RED = L_M;
constexpr unsigned L_SB = L_RED + 8192, L_CB = L_SB + 4096;
constexpr unsigned L_A1 = L_CB + 4096;
constexpr unsigned L_A4 = L_A1 + 2304;
constexpr unsigned L_QB = L_A4 + 2304;
constexpr unsigned L_A3 = L_QB + 2304;
constexpr unsigned L_MX = L_A3 + 4352;
constexpr unsigned L_LS = L_MX + 256;
constexpr unsigned L_WT = L_LS + 256;
constexpr unsigned L_UN = L_WT + 256;
constexpr unsigned L_SX = L_UN + 256;
constexpr unsigned L_FL = L_SX + 256;
constexpr unsigned L_ST = L_FL + 128;
constexpr unsigned L_END = L_ST + 12 * 16 * 8;
static_assert(L_END <= 163840, "lds");
enum { F_CBAR, F_Q, F_KML, F_T2, F_T5 };

DEV float bflo(unsigned u) { return __builtin_bit_cast(float, u << 16); }
DEV float bfhi(unsigned u) { return __builtin_bit_cast(float, u & 0xffff0000u); }
DEV unsigned pk(float a, float b) {
  v2f f = {a, b};
  unsigned u = __builtin_bit_cast(unsigned, __builtin_convertvector(f, v2bf));
  asm("" : "+v"(u));
  return u;
}
DEV float rbf(float a) { return bflo(pk(a, a)); }
DEV unsigned fbits(float a) { return __builtin_bit_cast(unsigned, a); }
DEV float bitsf(unsigned a) { return __builtin_bit_cast(float, a); }
DEV unsigned canon(unsigned a) { return a < 0xFFFFFFFEu ? a : 0xFFFFFFFEu; }
DEV v4f sel4(bool c, v4f a, v4f b) { return (v4f){c ? a.x : b.x, c ? a.y : b.y, c ? a.z : b.z, c ? a.w : b.w}; }
DEV v4f asf4(v4u v) { return __builtin_bit_cast(v4f, v); }
DEV unsigned opq(unsigned a) { asm("" : "+v"(a)); return a; }
DEV v4u tagbytes(unsigned T) { return (v4u){T & 255u, (T >> 8) & 255u, (T >> 16) & 255u, T >> 24}; }
DEV v4u tag4(v4f v, v4u tb) {
  return (v4u){(fbits(v.x) & ~255u) | tb.x, (fbits(v.y) & ~255u) | tb.y, (fbits(v.z) & ~255u) | tb.z, (fbits(v.w) & ~255u) | tb.w};
}

template <class T> DEV T lds_ld(unsigned a) { return *(LAS T*)(size_t)a; }
template <class T> DEV void lds_st(unsigned a, T v) { *(LAS T*)(size_t)a = v; }
DEV void cfence() { asm volatile("" ::: "memory"); }
#define SCHED() __builtin_amdgcn_sched_barrier(0)
DEV void lds_signal(int f, int lane) {
  cfence();
  if (lane == 0) __hip_atomic_fetch_add((LAS unsigned*)(size_t)(L_FL + 4 * f), 1u, __ATOMIC_RELAXED, __HIP_MEMORY_SCOPE_WORKGROUP);
  cfence();
}
DEV unsigned lds_flag(int f) { return __builtin_amdgcn_readfirstlane(*(volatile LAS unsigned*)(size_t)(L_FL + 4 * f)); }
template <int SLEEP> DEV void lds_wait(int f, unsigned target) {
  cfence();
#pragma nounroll
  for (int i = 0; i < 200000; ++i) {
    if (lds_flag(f) >= target) break;
    if (SLEEP) __builtin_amdgcn_s_sleep(SLEEP);
  }
  cfence();
}
constexpr int POLL_MAX = 8000;

DEV rsrc_t mk(const void* p, unsigned bytes) { return __builtin_amdgcn_make_buffer_rsrc((void*)p, 0, bytes, 0x00020000); }
DEV v4u ld_nt(rsrc_t r, unsigned v, unsigned s) { return __builtin_amdgcn_raw_buffer_load_b128(r, v, s, 2); }
DEV v4u ld_dev(rsrc_t r, unsigned v, unsigned s) { return __builtin_amdgcn_raw_buffer_load_b128(r, v, s, 16); }
DEV void st_dev(v4u d, rsrc_t r, unsigned v, unsigned s) {
  asm volatile("s_nop 0\n\tbuffer_store_dwordx4 %0, %1, %2, %3 offen sc1\n\ts_nop 1" : : "v"(d), "v"(v), "s"(r), "s"(s) : "memory");
}
DEV void st_dev2(v2u d, rsrc_t r, unsigned v, unsigned s) {
  asm volatile("s_nop 0\n\tbuffer_store_dwordx2 %0, %1, %2, %3 offen sc1" : : "v"(d), "v"(v), "s"(r), "s"(s) : "memory");
}
DEV void dma16(rsrc_t r, unsigned v, unsigned s, unsigned lds) {
  unsigned keep;
  asm volatile("s_mov_b32 %0, m0\n\ts_mov_b32 m0, %4\n\ts_nop 0\n\tbuffer_load_dwordx4 %1, %2, %3 offen nt lds\n\ts_mov_b32 m0, %0"
               : "=&s"(keep) : "v"(v), "s"(r), "s"(s), "s"(lds) : "memory");
}
template <int N> DEV void vmwait() { asm volatile("s_waitcnt vmcnt(%0)" ::"n"(N) : "memory"); }
DEV v8bf asbf(v4u v) { return __builtin_bit_cast(v8bf, v); }
DEV v4f mfma(v4u a, v4u b, v4f c) { return __builtin_amdgcn_mfma_f32_16x16x32_bf16(asbf(a), asbf(b), c, 0, 0, 0); }
DEV v2u trrd(unsigned a) { return __builtin_bit_cast(v2u, __builtin_amdgcn_ds_read_tr16_b64_v4i16((LAS v4s*)(size_t)a)); }
DEV v4u trfrag(unsigned a, unsigned step) { v2u x = trrd(a), y = trrd(a + step); return (v4u){x.x, x.y, y.x, y.y}; }

typedef unsigned long long u64;
#define GS(x) #x
#define GX(x) GS(x)
#define G_LD4(k, a, d) "s_mov_b64 exec, %[m" #k "]\n\ts_cbranch_execz 1" #k "f\n\tbuffer_load_dwordx4 v[" #a ":" #d "], %[vo], %[rs], %[s" #k "] offen sc1\n1" #k ":\n\t"
#define G_LD2(k, a, b) "s_mov_b64 exec, %[m" #k "]\n\ts_cbranch_execz 1" #k "f\n\tbuffer_load_dwordx2 v[" #a ":" #b "], %[vo], %[rs], %[s" #k "] offen sc1\n1" #k ":\n\t"
#define G_CK0(k, a, b, c, d) "s_mov_b64 exec, %[m" #k "]\n\t" \
  "v_xor_b32 v" #a ", %[t0], v" #a "\n\tv_xor_b32 v" #b ", %[t1], v" #b "\n\tv_xor_b32 v" #c ", %[t2], v" #c "\n\tv_xor_b32 v" #d ", %[t3], v" #d "\n\t" \
  "v_or3_b32 %[tm], v" #a ", v" #b ", v" #c "\n\tv_bitop3_b32 %[tm], %[tm], v" #d ", %[ff] bitop3:0xa8\n\tv_cmp_ne_u32 %[m" #k "], 0, %[tm]\n\t"
#define G_CK2(k, a, b) "s_mov_b64 exec, %[m" #k "]\n\tv_max_u32 %[tm], v" #a ", v" #b "\n\tv_cmp_eq_u32 %[m" #k "], -1, %[tm]\n\t"
#define G_OR(k) "s_or_b64 %[ac], %[ac], %[m" #k "]\n\t"
#define G_TAIL(sl) "s_cbranch_scc0 9f\n\t" G_WQ "s_sub_u32 %[cn], %[cn], 1\n\ts_cbranch_scc1 9f\n\ts_sleep " GX(sl) "\n\ts_branch 0b\n9:\n\ts_mov_b64 exec, -1\n\ts_nop 1"
#define G_M4 [m0] "+&s"(m[0]), [m1] "+&s"(m[1]), [m2] "+&s"(m[2]), [m3] "+&s"(m[3])
#define G_S4 [s0] "s"(so), [s1] "s"(so + step), [s2] "s"(so + 2 * step), [s3] "s"(so + 3 * step)
#define G_A4 "=&{v[128:131]}"(a[0]), "=&{v[132:135]}"(a[1]), "=&{v[136:139]}"(a[2]), "=&{v[140:143]}"(a[3])
#define G_TMP [tm] "=&v"(tm), [ac] "=&s"(ac), [cn] "+&s"(cn)
#define G_L4 G_LD4(0, 128, 131) G_LD4(1, 132, 135) G_LD4(2, 136, 139) G_LD4(3, 140, 143)
#define G_C4(C) C(0, 128, 129, 130, 131) C(1, 132, 133, 134, 135) C(2, 136, 137, 138, 139) C(3, 140, 141, 142, 143)
#define G_O4 "s_nop 3\n\ts_or_b64 %[ac], %[m0], %[m1]\n\t" G_OR(2) G_OR(3)
#define G_WQ "s_wqm_b64 %[m0], %[m0]\n\ts_wqm_b64 %[m1], %[m1]\n\ts_wqm_b64 %[m2], %[m2]\n\ts_wqm_b64 %[m3], %[m3]\n\t"
#define G_DECL(N) u64 m[N]; for (int i = 0; i < N; ++i) m[i] = ~0ull; unsigned tm, cn = POLL_MAX; u64 ac
#define G_T4 [t0] "s"(tb.x), [t1] "s"(tb.y), [t2] "s"(tb.z), [t3] "s"(tb.w), [ff] "s"(255u)
DEV void gather4f(v4u (&a)[4], rsrc_t r, unsigned vo, unsigned so, unsigned step, v4u tb) {
  G_DECL(4);
  asm volatile("0:\n\t" G_L4 "s_waitcnt vmcnt(0)\n\t" G_C4(G_CK0) G_O4 G_TAIL(PS)
               : G_A4, G_M4, G_TMP : [vo] "v"(vo), [rs] "s"(r), G_S4, G_T4 : "memory", "scc");
}
DEV void gather4s(v2u (&a)[4], rsrc_t r, unsigned vo, unsigned s0, unsigned s1, unsigned s2, unsigned s3) {
  G_DECL(4);
  asm volatile("0:\n\t" G_LD2(0, 128, 129) G_LD2(1, 130, 131) G_LD2(2, 132, 133) G_LD2(3, 134, 135) "s_waitcnt vmcnt(0)\n\t"
               G_CK2(0, 128, 129) G_CK2(1, 130, 131) G_CK2(2, 132, 133) G_CK2(3, 134, 135) G_O4 G_TAIL(PS)
               : "=&{v[128:129]}"(a[0]), "=&{v[130:131]}"(a[1]), "=&{v[132:133]}"(a[2]), "=&{v[134:135]}"(a[3]), G_M4, G_TMP
               : [vo] "v"(vo), [rs] "s"(r), [s0] "s"(s0), [s1] "s"(s1), [s2] "s"(s2), [s3] "s"(s3) : "memory", "scc");
}
struct Watch { unsigned r[4]; };
#define W_STEP(k, sl) "s_waitcnt vmcnt(3)\n\tv_cmp_ne_u32 vcc, %[tg], %[r" #k "]\n\ts_nop 0\n\ts_cbranch_vccz 9f\n\t" \
  "buffer_load_dword %[r" #k "], %[vo], %[rs], %[so] offen sc1\n\ts_sleep " GX(sl) "\n\t"
#define W_INIT(k, sl) "buffer_load_dword %[r" #k "], %[vo], %[rs], %[so] offen sc1\n\ts_sleep " GX(sl) "\n\t"
#define WATCH(name, sl) \
DEV Watch name(rsrc_t r, unsigned vo, unsigned so, unsigned tag) { \
  Watch w; unsigned cn = POLL_MAX; \
  asm volatile("s_mov_b64 exec, 0xff\n\t" W_INIT(0, sl) W_INIT(1, sl) W_INIT(2, sl) W_INIT(3, sl) \
               "0:\n\t" W_STEP(0, sl) W_STEP(1, sl) W_STEP(2, sl) W_STEP(3, sl) \
               "s_sub_u32 %[cn], %[cn], 1\n\ts_cbranch_scc0 0b\n9:\n\ts_mov_b64 exec, -1\n\ts_nop 1" \
               : [r0] "=&v"(w.r[0]), [r1] "=&v"(w.r[1]), [r2] "=&v"(w.r[2]), [r3] "=&v"(w.r[3]), [cn] "+&s"(cn) \
               : [vo] "v"(vo), [rs] "s"(r), [so] "s"(so), [tg] "s"(tag) : "memory", "scc", "vcc"); \
  return w; \
}
WATCH(watch_q, DQS)
WATCH(watch_m, DMS)
DEV void release_watch(Watch& w) { asm volatile("s_waitcnt vmcnt(0)" : : "v"(w.r[0]), "v"(w.r[1]), "v"(w.r[2]), "v"(w.r[3]) : "memory"); }
template <int N, int SL> DEV void gatherp(v4u (&a)[N], rsrc_t r, unsigned vo, unsigned so, unsigned step, unsigned tag) {
#pragma nounroll
  for (int it = 0; it < POLL_MAX; ++it) {
    cfence();
    unsigned bad = 0;
#pragma unroll
    for (int k = 0; k < N; ++k) a[k] = ld_dev(r, vo, so + k * step);
#pragma unroll
    for (int k = 0; k < N; ++k) bad |= (a[k].y ^ tag) | (a[k].w ^ tag);
    if (!__any(bad != 0)) break;
    __builtin_amdgcn_s_sleep(SL);
  }
  cfence();
}

template <int CTRL> DEV float dpp(float x) { return __builtin_bit_cast(float, __builtin_amdgcn_mov_dpp(__builtin_bit_cast(int, x), CTRL, 0xf, 0xf, true)); }
DEV float x16sum(float x) { auto s = __builtin_amdgcn_permlane16_swap(fbits(x), fbits(x), false, false); return bitsf(s[0]) + bitsf(s[1]); }
DEV float x32sum(float x) { auto s = __builtin_amdgcn_permlane32_swap(fbits(x), fbits(x), false, false); return bitsf(s[0]) + bitsf(s[1]); }
DEV float x16max(float x) { auto s = __builtin_amdgcn_permlane16_swap(fbits(x), fbits(x), false, false); return fmaxf(bitsf(s[0]), bitsf(s[1])); }
DEV float x32max(float x) { auto s = __builtin_amdgcn_permlane32_swap(fbits(x), fbits(x), false, false); return fmaxf(bitsf(s[0]), bitsf(s[1])); }
DEV float wave_sum(float x) {
  x += dpp<0xB1>(x); x += dpp<0x4E>(x); x += dpp<0x141>(x); x += dpp<0x140>(x);
  return x32sum(x16sum(x));
}
DEV float sum8up(float x) { x += dpp<0x128>(x); return x32sum(x16sum(x)); }
DEV float max8up(float x) { x = fmaxf(x, dpp<0x128>(x)); return x32max(x16max(x)); }
DEV v4u dup_lo(v4u v) { v4u o; for (int i = 0; i < 4; ++i) o[i] = __builtin_amdgcn_update_dpp(v[i], v[i], 0x128, 0xf, 0xc, false); return o; }
DEV v4u dup_hi(v4u v) { v4u o; for (int i = 0; i < 4; ++i) o[i] = __builtin_amdgcn_update_dpp(v[i], v[i], 0x128, 0xf, 0x3, false); return o; }
DEV float fdiv(float a, float d, float rd) { float q = a * rd; return fmaf(fmaf(-q, d, a), rd, q); }
DEV float dot2(unsigned a, unsigned b, float c) { return __builtin_amdgcn_fdot2_f32_bf16(__builtin_bit_cast(v2bf, a), __builtin_bit_cast(v2bf, b), c, false); }
struct Stat { float mb, sd, rd; };
DEV Stat mk_stat(float m, float var) {
  Stat st;
  st.mb = rbf(m);
  st.sd = rbf(__builtin_amdgcn_sqrtf(rbf(rbf(var) + 1e-5f)));
  st.rd = __builtin_amdgcn_rcpf(st.sd);
  return st;
}
DEV Stat ln_stat8(const unsigned (&w)[8]) {
  float s = 0.f, s2 = 0.f, q = 0.f, q2 = 0.f;
#pragma unroll
  for (int i = 0; i < 8; i += 2) {
    s = dot2(w[i], 0x3f803f80u, s); q = dot2(w[i], w[i], q);
    s2 = dot2(w[i + 1], 0x3f803f80u, s2); q2 = dot2(w[i + 1], w[i + 1], q2);
  }
  const float m = wave_sum(s + s2) * (1.f / 1024.f);
  float var = fmaf(-m, m, wave_sum(q + q2) * (1.f / 1024.f));
  if (__builtin_amdgcn_readfirstlane(!(m * m <= 16.f * var))) {
    q = 0.f;
#pragma unroll
    for (int i = 0; i < 8; ++i) { float a = bflo(w[i]) - m, b = bfhi(w[i]) - m; q = fmaf(a, a, q); q = fmaf(b, b, q); }
    var = wave_sum(q) * (1.f / 1024.f);
  }
  return mk_stat(m, var);
}
DEV unsigned ln_pair(unsigned x, const Stat& st) {
  unsigned d = pk(bflo(x) - st.mb, bfhi(x) - st.mb);
  return pk(fdiv(bflo(d), st.sd, st.rd), fdiv(bfhi(d), st.sd, st.rd));
}
DEV unsigned onep(unsigned c) { return pk(1.f + bflo(c), 1.f + bfhi(c)); }
DEV unsigned mod_pair(unsigned ln, unsigned c1, unsigned s) {
  unsigned p = pk(bflo(ln) * bflo(c1), bfhi(ln) * bfhi(c1));
  return pk(bflo(p) + bflo(s), bfhi(p) + bfhi(s));
}
DEV unsigned res_pair(unsigned x, unsigned g, float a, float b) {
  unsigned y = pk(a, b);
  unsigned p = pk(bflo(g) * bflo(y), bfhi(g) * bfhi(y));
  return pk(bflo(x) + bflo(p), bfhi(x) + bfhi(p));
}
DEV float silu1(float v) {
  const float d = 1.f + __expf(-v);
  return __builtin_amdgcn_div_fixupf(fdiv(v, d, __builtin_amdgcn_rcpf(d)), d, v);
}

#define STAMP(k)
#define STAMP_DUMP()

DEV v4f unit_reduce(v4f a) {
#pragma unroll
  for (int i = 0; i < 4; ++i) a[i] = sum8up(a[i]);
  return a;
}
DEV void wg_barrier() { asm volatile("s_waitcnt lgkmcnt(0)\n\ts_barrier" ::: "memory"); }
DEV v4f mm4(const v4u (&w)[4], unsigned bb) {
  v4u b[4];
#pragma unroll
  for (int kb = 0; kb < 4; ++kb) b[kb] = lds_ld<v4u>(bb + kb * 64);
  const v4f z = {0.f, 0.f, 0.f, 0.f};
  v4f d0 = mfma(w[0], b[0], z), d1 = mfma(w[1], b[1], z);
  d0 = mfma(w[2], b[2], d0); d1 = mfma(w[3], b[3], d1);
  return d0 + d1;
}

__global__ void __launch_bounds__(NTHR, 3)
fused_kernel(const void* __restrict__ px, const void* __restrict__ pk_, const void* __restrict__ pv, const void* __restrict__ pwq,
             const void* __restrict__ pwo, const void* __restrict__ pwg, const void* __restrict__ pwu, const void* __restrict__ pwd,
             const void* __restrict__ pc, const void* __restrict__ pwm, void* __restrict__ pout, void* __restrict__ pws) {
  const int lane = threadIdx.x & 63;
  const int wave = __builtin_amdgcn_readfirstlane(threadIdx.x >> 6);
  const int wg = blockIdx.x;
  const int n = lane & 15, g = lane >> 4;
  asm volatile("" : : "s"(px), "s"(pk_), "s"(pv), "s"(pwq), "s"(pwo), "s"(pwg), "s"(pwu), "s"(pwd), "s"(pc), "s"(pwm), "s"(pout), "s"(pws));
  const rsrc_t rws = mk(pws, WS_BYTES);
  const int bk = wg & 31, sp = wg >> 5;
  const int ab = bk >> 2, kvh = bk & 3;
  const int role = wg < 96 ? 0 : wg < 160 ? 1 : 2;
  const bool ffn = wg >= 32;
  const int fi = wg - 32;
  const int gks = fi & 3, gct = fi >> 2;
  const int dj = fi & 7, dft = fi >> 3;
  const int oh = wg >> 3, oj = wg & 7;
  const int ub = wg >> 5, uu = wg & 31;

  STAMP(0);
  const int tq = (lane >> 2) & 3, tp = lane & 3;
  const bool hi = n >= 8;
  unsigned ep;

  if (wave < 8) {
    const rsrc_t rk = mk((const char*)pk_ + (size_t)bk * 2097152 + sp * 262144 + wave * 32768, 32768);
    const rsrc_t rv = mk((const char*)pv + (size_t)bk * 2097152 + sp * 262144 + wave * 32768, 32768);
    const unsigned rot = wave + wg;
    if (DK0 && wg < 32) __builtin_amdgcn_s_sleep(DK0);
    v4u kf[8][4];
    const unsigned kva = ((n & 3) * 4 + ((n >> 2) & 1)) * 256 + g * 16 + (n >> 3) * 64, kvb = kva ^ 64;
#pragma unroll
    for (int i = 0; i < 32; ++i) {
      const int o = i >> 2, t = (o & 4) | ((o & 1) << 1) | ((o >> 1) & 1);
      if (i == WK) {
        asm volatile("s_barrier\n\ts_load_dword %0, %1, 0x0" : "=s"(ep) : "s"(pws) : "memory");
      }
      if (i >= WK) vmwait<WK - 1>();
      kf[t][i & 3] = ld_nt(rk, ((t & 2) ? kvb : kva) + ((i >> 1) & 1) * 512 + (i & 1) * 128, ((t + rot) & 7) * 4096);
      cfence();
    }
    STAMP(1);
    lds_wait<2>(F_Q, 1);
    asm volatile("s_waitcnt lgkmcnt(0)" : "+s"(ep) : : "memory");
    const v4u tb = tagbytes(ep + 0x01010101u);
    STAMP(2);
    v4u qf[2];
    {
      const unsigned qa = opq(L_QB + (n & 7) * 288 + (n >> 3) * 64 + g * 16);
      qf[0] = lds_ld<v4u>(qa); qf[1] = lds_ld<v4u>(qa + 128);
    }
    const unsigned vvo = lane * 16;
    v4u vf[4][8];
    auto vload = [&](int i) {
      SCHED();
      vmwait<WV - 1>();
      vf[i >> 3][i & 7] = ld_nt(rv, vvo + (i & 3) * 1024, (((i >> 2) + rot) & 7) * 4096);
      cfence();
      SCHED();
    };
    unsigned sc[4][2];
    float mx = -INFINITY;
    const bool va = (g >> 1) == (n >> 3);
#pragma unroll
    for (int q = 0; q < 4; ++q) {
      const int ta = (q >> 1) * 4 + (q & 1), tbb = ta + 2;
      const v4f z = {0.f, 0.f, 0.f, 0.f};
      v4f a0 = mfma(kf[ta][0], qf[0], z), b0 = mfma(kf[tbb][0], qf[0], z), a1 = mfma(kf[ta][2], qf[0], z), b1 = mfma(kf[tbb][2], qf[0], z);
      a0 = mfma(kf[ta][1], qf[1], a0); b0 = mfma(kf[tbb][1], qf[1], b0); a1 = mfma(kf[ta][3], qf[1], a1); b1 = mfma(kf[tbb][3], qf[1], b1);
      if (q < 3) { vload(2 * q); vload(2 * q + 1); }
      const v4f e0 = sel4(va, a0, b0), e1 = sel4(va, a1, b1);
      float s[4];
#pragma unroll
      for (int r = 0; r < 4; ++r) {
        auto w = __builtin_amdgcn_permlane32_swap(fbits(e0[r]), fbits(e1[r]), false, false);
        s[r] = bitsf(w[0]) + dpp<0x128>(bitsf(w[1]));
      }
#pragma unroll
      for (int r = 0; r < 2; ++r) {
        unsigned u = pk(s[2 * r], s[2 * r + 1]);
        u = pk(bflo(u) * 0.08838834764831845f, bfhi(u) * 0.08838834764831845f);
        sc[q][r] = u;
        mx = fmaxf(mx, fmaxf(bflo(u), bfhi(u)));
      }
    }
    vload(6);
    STAMP(3);
    mx = max8up(mx);
    float ls = 0.f;
    v4u pf[4];
#pragma unroll
    for (int b = 0; b < 2; ++b) {
      v4u p;
#pragma unroll
      for (int e = 0; e < 4; ++e) {
        const unsigned u = sc[2 * b + (e >> 1)][e & 1];
        const float p0 = __expf(bflo(u) - mx), p1 = __expf(bfhi(u) - mx);
        ls += p0 + p1;
        p[e] = pk(p0, p1);
      }
      pf[2 * b] = dup_lo(p); pf[2 * b + 1] = dup_hi(p);
      vload(7 + b);
    }
    ls = sum8up(ls);
    if (lane < 8) { lds_st<float>(L_MX + wave * 32 + lane * 4, mx); lds_st<float>(L_LS + wave * 32 + lane * 4, ls); }
    lds_signal(F_KML, lane);
    STAMP(4);
    v4f acc[8];
    auto piece = [&](int k) {
      const int p = k >> 3, i = k & 7;
      v4u a;
#pragma unroll
      for (int e = 0; e < 4; ++e) a[e] = __builtin_amdgcn_perm(vf[p][2 * e + 1][i >> 1], vf[p][2 * e][i >> 1], (i & 1) ? 0x07060302u : 0x05040100u);
      acc[i] = mfma(a, pf[p], p ? acc[i] : (v4f){0.f, 0.f, 0.f, 0.f});
    };
#pragma unroll
    for (int i = 9; i < 32; ++i) {
      vload(i);
      if (i >= 7 + WV) piece(i - 7 - WV);
    }
#pragma unroll
    for (int k = 25 - WV; k < 32; ++k) piece(k);
    STAMP(5);
    {
      const int hd = n & 7;
      const unsigned wa = opq(L_DN + wave * 4096 + hd * 512 + g * 128);
#pragma unroll
      for (int i = 0; i < 4; ++i) lds_st<v4f>(wa + (((i + 4 * (n >> 3)) ^ hd) << 4), sel4(hi, acc[i + 4], acc[i]));
      const int ci = lane & 31;
      const unsigned a0 = opq(L_DN + (lane >> 5) * 16384 + wave * 512 + (ci ^ wave) * 16);
      const unsigned fa = opq(L_WT + wave * 32 + (lane >> 5) * 16);
      wg_barrier();
      const v4f f = lds_ld<v4f>(fa);
      v4f s0 = lds_ld<v4f>(a0), s1 = lds_ld<v4f>(a0 + 4096), s2 = lds_ld<v4f>(a0 + 8192), s3 = lds_ld<v4f>(a0 + 12288);
      v4f s = (s0 * f[0] + s1 * f[1]) + (s2 * f[2] + s3 * f[3]);
#pragma unroll
      for (int i = 0; i < 4; ++i) s[i] = x32sum(s[i]);
      if (lane < 32) st_dev(tag4(s, tb), rws, ci * 16, O_AO + (kvh * 8 + wave) * 32768 + ab * 4096 + sp * 512);
    }
    STAMP(6);
    v4u wof[4], wgf[8];
    const unsigned tw = (8 * g + tq) * 256 + (((2 * wave + (tp >> 1)) ^ ((tq | ((g & 1) << 2)) << 1)) << 4) + 8 * (tp & 1);
    {
      const unsigned b1 = opq(L_W + tw);
#pragma unroll
      for (int kb = 0; kb < 4; ++kb) wof[kb] = trfrag(b1 + kb * 8192, 1024);
      const unsigned sw = ((tq >> 1) & 1) | ((g & 1) << 1);
      const unsigned b2 = opq(L_G + (wave >> 2) * 32768 + (8 * g + tq) * 128 + (((2 * (wave & 3) + (tp >> 1)) ^ (sw << 1)) << 4) + 8 * (tp & 1));
#pragma unroll
      for (int kb = 0; kb < 8; ++kb) wgf[kb] = trfrag(b2 + kb * 4096, 512);
    }
    const unsigned ua = opq(L_UN + (lane & 7) * 8);
    const v2u xu = lds_ld<v2u>(ua), gau = lds_ld<v2u>(ua + 64), gbu = lds_ld<v2u>(ua + 128);
    const unsigned sa_ = opq(L_SB + wave * 512 + lane * 8);
    const v2u sbv = lds_ld<v2u>(sa_), cbv = lds_ld<v2u>(sa_ + 4096);
    const unsigned bb = opq(L_A1 + (n & 7) * 288 + g * 16);
    {
      const unsigned wa = opq(L_A1 + wave * 288 + (lane & 31) * 8);
      const unsigned po = (n & 7) * 131072 + g * 16;
      if (D1) __builtin_amdgcn_s_sleep(D1);
      v4u a[4];
      gather4f(a, rws, lane * 16, O_AO + oh * 32768 + wave * 4096, 1024, tb);
      STAMP(7);
      v4f o = (asf4(a[0]) + asf4(a[1])) + (asf4(a[2]) + asf4(a[3]));
#pragma unroll
      for (int i = 0; i < 4; ++i) o[i] = x32sum(o[i]);
      if (lane < 32) lds_st<v2u>(wa, (v2u){pk(o[0], o[1]), pk(o[2], o[3])});
      wg_barrier();
      const v4f d = mm4(wof, bb);
      if (n < 8) st_dev(tag4(d, tb), rws, po, O_WP + (oj * 4 + (wave >> 1)) * 4096 + oh * 128 + (wave & 1) * 64);
    }
    STAMP(8);
    v2u x1u = {0u, 0u};
    if (wave < 4) {
      if (D2) __builtin_amdgcn_s_sleep(D2);
      if (wave & 1) __builtin_amdgcn_s_sleep(D2S);
      if (wave & 2) __builtin_amdgcn_s_sleep(2 * D2S);
      v4u a[4];
      gather4f(a, rws, lane * 16, O_WP + wg * 4096, 1024, tb);
      STAMP(9);
      const v4f s = unit_reduce((asf4(a[0]) + asf4(a[1])) + (asf4(a[2]) + asf4(a[3])));
      x1u.x = canon(res_pair(xu.x, gau.x, s[0], s[1]));
      x1u.y = canon(res_pair(xu.y, gau.y, s[2], s[3]));
      if (lane < 8) st_dev2(x1u, rws, lane * 8, O_X1 + wg * 64);
      STAMP(10);
      if (D3B) __builtin_amdgcn_s_sleep(D3B);
    } else if (ffn) {
      const rsrc_t rd = mk(pwd, NODN ? 0 : 3584 * 2048);
      const unsigned c0 = g * 2048 + ((n ^ (g << 1)) << 4), c1 = c0 ^ 128;
#pragma unroll
      for (int i = 0; i < 8; ++i)
        dma16(rd, (i & 2) ? c1 : c0, (dft * 128 + (wave - 4) * 32 + i * 4) * 2048 + dj * 256, L_DN + ((wave - 4) * 8 + i) * 1024);
      if (D3) __builtin_amdgcn_s_sleep(D3);
    }
    if (ffn) {
      {
        const unsigned wa = opq(L_A3 + wave * 544 + lane * 8), b3 = opq(L_A3 + (n & 7) * 544 + g * 16);
        const unsigned po = n * 64 + g * 16;
        v2u xv[4];
        const unsigned xo = O_X1 + wave * 2048;
        gather4s(xv, rws, lane * 8, xo + gks * 512, xo + ((gks + 1) & 3) * 512, xo + ((gks + 2) & 3) * 512, xo + ((gks + 3) & 3) * 512);
        const unsigned xr[8] = {xv[0].x, xv[0].y, xv[1].x, xv[1].y, xv[2].x, xv[2].y, xv[3].x, xv[3].y};
        STAMP(11);
        const Stat st = ln_stat8(xr);
        v2u h2;
        h2.x = mod_pair(ln_pair(xr[0], st), cbv.x, sbv.x);
        h2.y = mod_pair(ln_pair(xr[1], st), cbv.y, sbv.y);
        lds_st<v2u>(wa, h2);
        wg_barrier();
        v4u b[8];
#pragma unroll
        for (int kb = 0; kb < 8; ++kb) b[kb] = lds_ld<v4u>(b3 + kb * 64);
        const v4f z = {0.f, 0.f, 0.f, 0.f};
        v4f d0 = mfma(wgf[0], b[0], z), d1 = mfma(wgf[1], b[1], z);
#pragma unroll
        for (int kb = 2; kb < 8; kb += 2) { d0 = mfma(wgf[kb], b[kb], d0); d1 = mfma(wgf[kb + 1], b[kb + 1], d1); }
        d0 += d1;
        if (n < 8) st_dev(tag4(d0, tb), rws, po, O_GP + (gct >> 1) * 32768 + ((gct & 1) * 4 + (wave & 3)) * 4096 + gks * 1024 + (wave >> 2) * 512);
      }
      STAMP(12);
      {
        v4u wdf[4];
        const unsigned b1 = opq(L_DN + tw);
#pragma unroll
        for (int kb = 0; kb < 4; ++kb) wdf[kb] = trfrag(b1 + kb * 8192, 1024);
        const unsigned wa = opq(L_A4 + ((lane >> 2) & 7) * 288 + wave * 32 + (lane & 3) * 8);
        const unsigned po = (n & 7) * (32 * 3584) + g * 16;
        if (D4) __builtin_amdgcn_s_sleep(D4);
        v4u a[4];
        gather4f(a, rws, lane * 16, O_GP + dft * 32768 + wave * 4096, 1024, tb);
        STAMP(13);
        const v4f t = (asf4(a[0]) + asf4(a[1])) + (asf4(a[2]) + asf4(a[3]));
        const unsigned t0 = pk(t[0], t[1]), t1 = pk(t[2], t[3]);
        auto w0 = __builtin_amdgcn_permlane32_swap(t0, t0, false, false), w1 = __builtin_amdgcn_permlane32_swap(t1, t1, false, false);
        auto relu = [](float v) { return v > 0.f ? v : v * 0.f; };
        const v2u hv = {pk(relu(bflo(w0[0])) * bflo(w0[1]), relu(bfhi(w0[0])) * bfhi(w0[1])),
                        pk(relu(bflo(w1[0])) * bflo(w1[1]), relu(bfhi(w1[0])) * bfhi(w1[1]))};
        if (lane < 32) lds_st<v2u>(wa, hv);
        wg_barrier();
        const v4f d = mm4(wdf, bb + (L_A4 - L_A1));
        if (n < 8) st_dev(tag4(d, tb), rws, po, O_DP + (dj * 4 + (wave >> 1)) * 3584 + dft * 128 + (wave & 1) * 64);
      }
      STAMP(14);
    } else {
      __builtin_amdgcn_s_sleep(90);
    }
    if (wave < 4) {
      if (D5) __builtin_amdgcn_s_sleep(D5);
      if (wave & 1) __builtin_amdgcn_s_sleep(D2S);
      if (wave & 2) __builtin_amdgcn_s_sleep(2 * D2S);
      v4u a[4];
      gather4f(a, rws, (lane < 56 ? lane : lane - 8) * 16, O_DP + wg * 3584, 896, tb);
      v4f t = (asf4(a[0]) + asf4(a[1])) + (asf4(a[2]) + asf4(a[3]));
      if (lane >= 56) t = (v4f){0.f, 0.f, 0.f, 0.f};
      const v4f s = unit_reduce(t);
      const v2u o = {res_pair(x1u.x, gbu.x, s[0], s[1]), res_pair(x1u.y, gbu.y, s[2], s[3])};
      if (lane < 8) __builtin_amdgcn_raw_buffer_store_b64(o, mk(pout, 16384), lane * 8, wg * 64, 0);
      if (wg == 0 && wave == 0 && lane == 0) __builtin_amdgcn_raw_buffer_store_b32(ep + 0x01010101u, rws, 0, WS_EPOCH, 16);
      STAMP(15);
    }
    STAMP_DUMP();
    return;
  }

  const int cw = wave - 8;
  __builtin_amdgcn_s_setprio(2);
  unsigned cpf[2] = {0u, 0u};
  if (CPF) {
    unsigned long long pc; unsigned len;
    unsigned back;
    asm volatile("s_getpc_b64 %0\n.Lcpf_here:\n\ts_mov_b32 %2, .Lcpf_here-_Z12fused_kernelPKvS0_S0_S0_S0_S0_S0_S0_S0_S0_PvS1_\n\t"
                 "s_mov_b32 %1, .Lfunc_end0-_Z12fused_kernelPKvS0_S0_S0_S0_S0_S0_S0_S0_S0_PvS1_" : "=s"(pc), "=s"(len), "=s"(back));
    pc -= back;
    const rsrc_t rc = mk((const void*)pc, len);
    const unsigned vo = lane * 128 + cw * 16384;
    asm volatile("buffer_load_dword %0, %2, %3, 0 offen\n\tbuffer_load_dword %1, %4, %3, 0 offen" : "=&v"(cpf[0]), "=&v"(cpf[1]) : "v"(vo), "s"(rc), "v"(vo + 8192) : "memory");
  }
  unsigned cb_n = 0;
  const unsigned sw64 = (((lane & 7) ^ (((lane >> 4) & 1) << 1)) << 4);
  if (role != 2) {
    v4u in[4];
    {
      const rsrc_t ri = mk(role == 0 ? pc : px, 16384);
      const unsigned vo = (n & 7) * 2048 + cw * 512 + (n >> 3) * 64 + g * 16;
      asm volatile("buffer_load_dwordx4 %0, %4, %5, 0 offen\n\tbuffer_load_dwordx4 %1, %4, %5, 0 offen offset:128\n\t"
                   "buffer_load_dwordx4 %2, %4, %5, 0 offen offset:256\n\tbuffer_load_dwordx4 %3, %4, %5, 0 offen offset:384"
                   : "=&v"(in[0]), "=&v"(in[1]), "=&v"(in[2]), "=&v"(in[3]) : "v"(vo), "s"(ri) : "memory");
    }
    const unsigned rowb = role == 0 ? 12288 : 8192;
    const rsrc_t rh = mk(role == 0 ? pwm : pwq, 1024 * rowb);
    const unsigned col = (role == 0 ? wg : wg - 96) * 128 + cw * 256 * rowb;
    const unsigned v0 = (lane >> 3) * rowb + sw64, v1 = v0 ^ 64;
    if (!ffn) {
      if (cw == 0 && lane < 32) st_dev((v4u){SENT, SENT, SENT, SENT}, rws, lane * 16, O_X1 + wg * 512);
#pragma unroll
      for (int k = 0; k < 24; ++k) dma16(rh, (k & 1) ? v1 : v0, k * 8 * rowb + col, (cw * 32 + k) * 1024);
    }
    const unsigned k0 = ffn ? 0 : 24;
#pragma unroll
    for (int k = 0; k < 8; ++k) dma16(rh, (k & 1) ? v1 : v0, (k0 + k) * 8 * rowb + col, (cw * 32 + k0 + k) * 1024);
    if (cw == 0 && lane < 32) lds_st<unsigned>(L_FL + 4 * lane, 0u);
    asm volatile("s_waitcnt lgkmcnt(0)\n\ts_barrier\n\ts_load_dword %0, %1, 0x0" : "=s"(ep) : "s"(pws) : "memory");
    if (ffn) vmwait<8>(); else vmwait<32>();
    asm volatile("" : "+v"(in[0]), "+v"(in[1]), "+v"(in[2]), "+v"(in[3]) : "v"(cpf[0]), "v"(cpf[1]) : "memory");
    STAMP(1);
    if (role == 0) {
#pragma unroll
      for (int p = 0; p < 4; ++p)
#pragma unroll
        for (int e = 0; e < 4; ++e) in[p][e] = pk(silu1(bflo(in[p][e])), silu1(bfhi(in[p][e])));
    } else {
      float s = 0.f;
#pragma unroll
      for (int p = 0; p < 4; ++p)
#pragma unroll
        for (int e = 0; e < 4; ++e) s += bflo(in[p][e]) + bfhi(in[p][e]);
      s = sum8up(s);
      if (lane < 8) lds_st<float>(L_SX + lane * 16 + cw * 4, s);
      lds_signal(F_CBAR, lane); cb_n += 4; lds_wait<1>(F_CBAR, cb_n);
      v4f t = lds_ld<v4f>(L_SX + (n & 7) * 16);
      const float m = ((t[0] + t[1]) + (t[2] + t[3])) * (1.f / 1024.f);
      s = 0.f;
#pragma unroll
      for (int p = 0; p < 4; ++p)
#pragma unroll
        for (int e = 0; e < 4; ++e) { float a = bflo(in[p][e]) - m, b = bfhi(in[p][e]) - m; s = fmaf(a, a, s); s = fmaf(b, b, s); }
      s = sum8up(s);
      if (lane < 8) lds_st<float>(L_SX + 128 + lane * 16 + cw * 4, s);
      lds_signal(F_CBAR, lane); cb_n += 4; lds_wait<1>(F_CBAR, cb_n);
      t = lds_ld<v4f>(L_SX + 128 + (n & 7) * 16);
      const Stat st = mk_stat(m, ((t[0] + t[1]) + (t[2] + t[3])) * (1.f / 1024.f));
#pragma unroll
      for (int p = 0; p < 4; ++p)
#pragma unroll
        for (int e = 0; e < 4; ++e) in[p][e] = ln_pair(in[p][e], st);
    }
    STAMP(2);
    if (ffn) {
#pragma unroll
      for (int k = 8; k < 32; ++k) {
        if (role == 1) vmwait<WH1 - 1>(); else vmwait<WH0 - 1>();
        dma16(rh, (k & 1) ? v1 : v0, k * 8 * rowb + col, (cw * 32 + k) * 1024);
      }
    }
    asm volatile("s_waitcnt lgkmcnt(0)" : "+s"(ep) : : "memory");
    const unsigned TAG = ep + 0x01010101u;
    const unsigned sw = ((tq >> 1) & 1) | ((g & 1) << 1);
    const unsigned tb0 = cw * 32768 + (8 * g + tq) * 128 + (((tp >> 1) ^ (sw << 1)) << 4) + 8 * (tp & 1);
    unsigned tba[4];
#pragma unroll
    for (int t = 0; t < 4; ++t) tba[t] = opq(tb0 ^ (t << 5));
    v4u wf[4][4];
#pragma unroll
    for (int kb = 0; kb < 4; ++kb) {
      if (kb == 0) vmwait<28>(); if (kb == 1) vmwait<24>(); if (kb == 2) vmwait<20>(); if (kb == 3) vmwait<16>();
#pragma unroll
      for (int t = 0; t < 4; ++t) wf[kb][t] = trfrag(tba[t] + kb * 4096, 512);
    }
    STAMP(3);
    if (role == 1) {
      Watch w = watch_m(rws, ((lane & 4) ? 32768 : 0) + (lane & 3) * 2048 + 4, O_SC + cw * 8192, TAG);
      STAMP(4);
      v4u a[16];
#pragma nounroll
      for (int it = 0; it < POLL_MAX; ++it) {
        cfence();
        unsigned bad = 0;
#pragma unroll
        for (int k = 0; k < 16; ++k) a[k] = ld_dev(rws, lane * 16 + (k & 3) * 1024, O_SC + cw * 8192 + ((k >> 2) & 1) * 4096 + (k >> 3) * 32768);
#pragma unroll
        for (int k = 0; k < 16; ++k) bad |= (a[k].y ^ TAG) | (a[k].w ^ TAG);
        if (!__any(bad != 0)) break;
        __builtin_amdgcn_s_sleep(DMS);
      }
      release_watch(w);
      STAMP(5);
#pragma unroll
      for (int p = 0; p < 4; ++p)
#pragma unroll
        for (int e = 0; e < 4; ++e) in[p][e] = mod_pair(in[p][e], onep(a[8 + 2 * p + (e >> 1)][(e & 1) * 2]), a[2 * p + (e >> 1)][(e & 1) * 2]);
    }
    v4f d[4];
#pragma unroll
    for (int t = 0; t < 4; ++t) d[t] = (v4f){0.f, 0.f, 0.f, 0.f};
#pragma unroll
    for (int kb = 0; kb < 8; ++kb) {
      const v4u b = (kb & 1) ? dup_hi(in[kb >> 1]) : dup_lo(in[kb >> 1]);
#pragma unroll
      for (int t = 0; t < 4; ++t) d[t] = mfma(wf[kb & 3][t], b, d[t]);
      if (kb < 4) {
        SCHED();
        if (kb == 0) vmwait<12>(); if (kb == 1) vmwait<8>(); if (kb == 2) vmwait<4>(); if (kb == 3) vmwait<0>();
#pragma unroll
        for (int t = 0; t < 4; ++t) wf[kb][t] = trfrag(tba[t] + (kb + 4) * 4096, 512);
        SCHED();
      }
    }
    const unsigned ra = L_RED + cw * 2048 + (n >> 3) * 1024 + (n & 7) * 64 + g * 16;
    lds_st<v4f>(ra, sel4(hi, d[2], d[0]));
    lds_st<v4f>(ra + 512, sel4(hi, d[3], d[1]));
    lds_signal(F_CBAR, lane); cb_n += 4; lds_wait<0>(F_CBAR, cb_n);
    const unsigned rb = L_RED + (lane >> 5) * 4096 + cw * 512 + (lane & 31) * 16;
    v4f s = lds_ld<v4f>(rb) + lds_ld<v4f>(rb + 2048);
#pragma unroll
    for (int i = 0; i < 4; ++i) s[i] = x32sum(s[i]);
    const unsigned vo = ffn ? (lane >> 2) * 16384 + (lane & 3) * 16 : (lane & 1) * 1024 + ((lane >> 1) & 1) * 256 + (lane >> 2) * 16;
    const unsigned so = ffn ? (role == 0 ? O_MOD + (wg - 32) * 256 : O_Q + (wg - 96) * 256) + cw * 64 : O_SC + wg * 2048 + (cw & 1) * 512 + (cw >> 1) * 128;
    if (lane < 32) st_dev((v4u){pk(s[0], s[1]), TAG, pk(s[2], s[3]), TAG}, rws, vo, so);
    STAMP(6);
  } else {
    if (cw == 0 && lane < 32) lds_st<unsigned>(L_FL + 4 * lane, 0u);
    asm volatile("s_waitcnt lgkmcnt(0)\n\ts_barrier\n\ts_load_dword %0, %1, 0x0\n\ts_waitcnt lgkmcnt(0)" : "=s"(ep) : "s"(pws) : "memory");
  }
  const unsigned TAG = ep + 0x01010101u;
  if (cw == 3) {
    if (role == 2) { __builtin_amdgcn_s_sleep(DQ0); __builtin_amdgcn_s_sleep(DQ0); }
    const unsigned qo = O_Q + ab * 16384 + kvh * 4096;
    Watch w = watch_q(rws, (lane & 7) * 256 + ((wg >> 5) & 1) * 2048 + 4, qo, TAG);
    STAMP(7);
    v4u a[4];
    gatherp<4, 8>(a, rws, lane * 16, qo, 1024, TAG);
    release_watch(w);
#pragma unroll
    for (int k = 0; k < 4; ++k) lds_st<v2u>(L_QB + (2 * k + (lane >> 5)) * 288 + (lane & 31) * 8, (v2u){a[k].x, a[k].z});
    lds_signal(F_Q, lane);
    STAMP(8);
    if (PRET) {
      unsigned t[12];
      t[0] = __builtin_amdgcn_raw_buffer_load_b32(rws, lane * 64, O_AO + wg * 4096, 16);
      t[1] = __builtin_amdgcn_raw_buffer_load_b32(rws, lane * 64, O_WP + wg * 4096, 16);
      t[2] = __builtin_amdgcn_raw_buffer_load_b32(rws, lane * 64, O_GP + wg * 3584, 16);
      t[3] = __builtin_amdgcn_raw_buffer_load_b32(rws, lane * 64, O_DP + wg * 3584, 16);
      t[4] = __builtin_amdgcn_raw_buffer_load_b32(rws, (lane & 7) * 32768, O_AO + kvh * 262144 + ab * 4096 + sp * 512, 16);
      t[5] = __builtin_amdgcn_raw_buffer_load_b32(rws, (lane & 7) * 4096, O_AO + oh * 32768, 16);
      t[6] = __builtin_amdgcn_raw_buffer_load_b32(rws, (lane & 7) * 131072 + ((lane >> 3) & 3) * 4096, O_WP + oj * 16384 + oh * 128, 16);
      t[7] = __builtin_amdgcn_raw_buffer_load_b32(rws, (lane & 3) * 4096, O_X1, 16);
      t[8] = __builtin_amdgcn_raw_buffer_load_b32(rws, (lane & 3) * 4096, O_GP + (ffn ? (gct >> 1) * 32768 + (gct & 1) * 16384 + gks * 1024 : 0), 16);
      t[9] = __builtin_amdgcn_raw_buffer_load_b32(rws, (lane & 7) * 4096, O_GP + (ffn ? dft * 32768 : 0), 16);
      t[10] = __builtin_amdgcn_raw_buffer_load_b32(rws, (lane & 7) * (32 * 3584) + ((lane >> 3) & 3) * 3584, O_DP + (ffn ? dj * 4 * 3584 + dft * 128 : 0), 16);
      t[11] = __builtin_amdgcn_raw_buffer_load_b32(mk(pout, 16384), 0, wg * 64, 16);
#pragma unroll
      for (int i = 0; i < 12; ++i) asm volatile("" : : "v"(t[i]));
    }
    lds_wait<8>(F_KML, 8);
    const float mw = lds_ld<float>(L_MX + lane * 4), lw = lds_ld<float>(L_LS + lane * 4);
    const float ms = max8up(mw);
    const float lsum = sum8up(lw * __expf(mw - ms));
    if (lane < 8) st_dev((v4u){fbits(ms), TAG, fbits(lsum), TAG}, rws, lane * 16, O_ML + bk * 1024 + sp * 128);
    STAMP(9);
    __builtin_amdgcn_s_sleep(32);
    v4u ml[1];
    gatherp<1, 24>(ml, rws, lane * 16, O_ML + bk * 1024, 0, TAG);
    const float M = max8up(bitsf(ml[0].x));
    const float L = sum8up(bitsf(ml[0].z) * __expf(bitsf(ml[0].x) - M));
    lds_st<float>(L_WT + (lane & 7) * 32 + (lane >> 3) * 4, __expf(mw - M) / L);
    STAMP(10);
  } else if (cw == 2) {
    asm volatile("s_waitcnt lgkmcnt(0)" ::: "memory");
    const rsrc_t ro = mk(pwo, 4096 * 2048);
    const unsigned c0 = g * 8 * 2048 + ((n ^ (g << 1)) << 4), c1 = c0 ^ 128;
#pragma unroll
    for (int i = 0; i < 32; ++i) {
      if (role == 2) { if (i >= WF) vmwait<WF - 1>(); } else if (i >= WF1) vmwait<WF1 - 1>();
      dma16(ro, (i & 2) ? c1 : c0, (oh * 128 + (i >> 3) * 32 + (i & 7)) * 2048 + oj * 256, L_W + i * 1024);
    }
    STAMP(7);
    const v2u xu = __builtin_amdgcn_raw_buffer_load_b64(mk(px, 16384), (lane & 7) * 8, ub * 2048 + uu * 64, 0);
    v4u a[1];
    gatherp<1, 32>(a, rws, (lane & 7) * 16 + ((lane >> 3) & 1) * 12288, O_MOD + ub * 16384 + uu * 128, 0, TAG);
    if (lane < 8) lds_st<v2u>(L_UN + lane * 8, xu);
    if (lane < 16) lds_st<v2u>(L_UN + 64 + lane * 8, (v2u){a[0].x, a[0].z});
    STAMP(8);
  } else if (ffn) {
    asm volatile("s_waitcnt lgkmcnt(0)" ::: "memory");
    const rsrc_t rg = mk(cw ? pwu : pwg, 1024 * 3584 * 2);
    const unsigned v0 = (lane >> 3) * 7168 + sw64, v1 = v0 ^ 64;
#pragma unroll
    for (int i = 0; i < 32; ++i) {
      if (role == 2) { if (i >= WF) vmwait<WF - 1>(); } else if (i >= WF1) vmwait<WF1 - 1>();
      dma16(rg, (i & 1) ? v1 : v0, (gks * 256 + i * 8) * 7168 + gct * 128, L_G + cw * 32768 + i * 1024);
    }
    STAMP(7);
    v4u a[8];
    gatherp<8, 32>(a, rws, lane * 16, O_MOD + 4096 + cw * 4096 + gks * 1024, 16384, TAG);
#pragma unroll
    for (int k = 0; k < 8; ++k)
      lds_st<v2u>(L_SB + cw * 4096 + k * 512 + lane * 8, cw ? (v2u){onep(a[k].x), onep(a[k].z)} : (v2u){a[k].x, a[k].z});
    STAMP(8);
  }
  STAMP_DUMP();
  asm volatile("s_waitcnt vmcnt(0) lgkmcnt(0)" : : "v"(cpf[0]), "v"(cpf[1]) : "memory");
}

extern "C" void kernel_launch(void* const* d_in, const int* in_sizes, int n_in, void* d_out, int out_size, void* d_ws, size_t ws_size,
                              hipStream_t stream) {
  (void)in_sizes; (void)n_in; (void)out_size; (void)ws_size;
  (void)hipFuncSetAttribute((const void*)fused_kernel, hipFuncAttributeMaxDynamicSharedMemorySize, L_END);
  fused_kernel<<<NWG, NTHR, L_END, stream>>>(d_in[0], d_in[1], d_in[2], d_in[3], d_in[4], d_in[6], d_in[7], d_in[8], d_in[9], d_in[10],
                                             d_out, d_ws);
}
```

```cpp
#include <hip/hip_runtime.h>
#include <stdint.h>

typedef unsigned v4u __attribute__((ext_vector_type(4)));
typedef unsigned v2u __attribute__((ext_vector_type(2)));
typedef float v4f __attribute__((ext_vector_type(4)));
typedef float v2f __attribute__((ext_vector_type(2)));
typedef __bf16 v2bf __attribute__((ext_vector_type(2)));
typedef __bf16 v8bf __attribute__((ext_vector_type(8)));
typedef short v4s __attribute__((ext_vector_type(4)));
#define LAS __attribute__((address_space(3)))
typedef __amdgpu_buffer_rsrc_t rsrc_t;
#define DEV __device__ __forceinline__

#define WK 3
#define WV 4
#define WH1 8
#define WH0 4
#define DQ0 40
#define DQS 16
#define DMS 6
#define D1 8
#define D2 8
#define D2S 5
#define D3 40
#define D4 8
#define D5 8
#define WKA 6
#define WVA 6
#define S1N 3
#define S2N 5
#define DS3 1
#define S2E 1
#define S4E 1
#define KA0 8
#define KD 1
#define THM 2
#define THQ 8
#define DMR 2
#define G0A 0
#define NT2 4
#define DA 40
#define G0 0
#define PS 1

constexpr int NWG = 256, NTHR = 768;
constexpr unsigned SENT = 0xFFFFFFFFu;
constexpr unsigned WS_EPOCH = 0;
constexpr unsigned O_SC = 65536;
constexpr unsigned O_MOD = O_SC + 131072;
constexpr unsigned O_Q = O_MOD + 131072;
constexpr unsigned O_RP = O_Q + 262144;
constexpr unsigned O_ML = O_RP + 131072;
constexpr unsigned O_X1 = O_ML + 32768;
constexpr unsigned O_AO = O_X1 + 131072;
constexpr unsigned O_WP = O_AO + 1048576;
constexpr unsigned O_GP = O_WP + 1048576;
constexpr unsigned O_DP = O_GP + 28 * 32768;
constexpr unsigned O_STAMP = 65536 + 256 * 16384 + 1048576;
static_assert(O_DP + 1048576 <= O_STAMP, "ws map");
constexpr unsigned WS_BYTES = O_STAMP + NWG * 12 * 16 * 8;
constexpr unsigned L_G = 0, L_W = 65536, L_DN = 98304;
constexpr unsigned L_M = 131072;
constexpr unsigned L_RED = L_M;
constexpr unsigned L_SB = L_RED, L_CB = L_SB + 4096;
constexpr unsigned L_A1 = L_RED + 16384;
constexpr unsigned L_A4 = L_A1 + 2304;
constexpr unsigned L_QB = L_A4 + 2304;
constexpr unsigned L_A3 = L_QB + 2304;
constexpr unsigned L_MX = L_A3 + 4352;
constexpr unsigned L_LS = L_MX + 256;
constexpr unsigned L_WT = L_LS + 256;
constexpr unsigned L_UN = L_WT + 256;
constexpr unsigned L_SX = L_UN + 256;
constexpr unsigned L_FL = L_SX + 256;
constexpr unsigned L_ST = L_FL + 128;
constexpr unsigned L_END = L_ST + 12 * 16 * 8;
static_assert(L_END <= 163840, "lds");
enum { F_CBAR, F_Q, F_KML, F_GO, F_HEAD, F_WT, F_LW, F_X1, F_KD };

DEV float bflo(unsigned u) { return __builtin_bit_cast(float, u << 16); }
DEV float bfhi(unsigned u) { return __builtin_bit_cast(float, u & 0xffff0000u); }
DEV unsigned pk(float a, float b) {
  v2f f = {a, b};
  unsigned u = __builtin_bit_cast(unsigned, __builtin_convertvector(f, v2bf));
  asm("" : "+v"(u));
  return u;
}
DEV float rbf(float a) { return bflo(pk(a, a)); }
DEV unsigned fbits(float a) { return __builtin_bit_cast(unsigned, a); }
DEV float bitsf(unsigned a) { return __builtin_bit_cast(float, a); }
DEV unsigned canon(unsigned a) { return a < 0xFFFFFFFEu ? a : 0xFFFFFFFEu; }
DEV v4f sel4(bool c, v4f a, v4f b) { return (v4f){c ? a.x : b.x, c ? a.y : b.y, c ? a.z : b.z, c ? a.w : b.w}; }
DEV v4f asf4(v4u v) { return __builtin_bit_cast(v4f, v); }
DEV unsigned opq(unsigned a) { asm("" : "+v"(a)); return a; }
DEV v4u tagbytes(unsigned T) { return (v4u){T & 255u, (T >> 8) & 255u, (T >> 16) & 255u, T >> 24}; }
template <unsigned M = 255u> DEV v4u tag4(v4f v, v4u tb) {
  v4u o;
  for (int i = 0; i < 4; ++i) o[i] = ((fbits(v[i]) + (M + 1) / 2) & ~M) | tb[i];
  return o;
}

template <class T> DEV T lds_ld(unsigned a) { return *(LAS T*)(size_t)a; }
template <class T> DEV void lds_st(unsigned a, T v) { *(LAS T*)(size_t)a = v; }
DEV void cfence() { asm volatile("" ::: "memory"); }
#define SCHED() __builtin_amdgcn_sched_barrier(0)
DEV void lds_signal(int f, int lane) {
  cfence();
  if (lane == 0) __hip_atomic_fetch_add((LAS unsigned*)(size_t)(L_FL + 4 * f), 1u, __ATOMIC_RELAXED, __HIP_MEMORY_SCOPE_WORKGROUP);
  cfence();
}
DEV unsigned lds_flag(int f) { return __builtin_amdgcn_readfirstlane(*(volatile LAS unsigned*)(size_t)(L_FL + 4 * f)); }
template <int SLEEP> DEV void lds_wait(int f, unsigned target) {
  cfence();
#pragma nounroll
  for (int i = 0; i < 200000; ++i) {
    if (lds_flag(f) >= target) break;
    if (SLEEP) __builtin_amdgcn_s_sleep(SLEEP);
  }
  cfence();
}
constexpr int POLL_MAX = 8000;

DEV rsrc_t mk(const void* p, unsigned bytes) { return __builtin_amdgcn_make_buffer_rsrc((void*)p, 0, bytes, 0x00020000); }
DEV v4u ld_nt(rsrc_t r, unsigned v, unsigned s) { return __builtin_amdgcn_raw_buffer_load_b128(r, v, s, 2); }
DEV v4u ld_dev(rsrc_t r, unsigned v, unsigned s) { return __builtin_amdgcn_raw_buffer_load_b128(r, v, s, 16); }
DEV void st_dev(v4u d, rsrc_t r, unsigned v, unsigned s) {
  asm volatile("s_nop 0\n\tbuffer_store_dwordx4 %0, %1, %2, %3 offen sc1\n\ts_nop 1" : : "v"(d), "v"(v), "s"(r), "s"(s) : "memory");
}
DEV void st_dev2(v2u d, rsrc_t r, unsigned v, unsigned s) {
  asm volatile("s_nop 0\n\tbuffer_store_dwordx2 %0, %1, %2, %3 offen sc1" : : "v"(d), "v"(v), "s"(r), "s"(s) : "memory");
}
DEV void dma16(rsrc_t r, unsigned v, unsigned s, unsigned lds) {
  unsigned keep;
  asm volatile("s_mov_b32 %0, m0\n\ts_mov_b32 m0, %4\n\ts_nop 0\n\tbuffer_load_dwordx4 %1, %2, %3 offen nt lds\n\ts_mov_b32 m0, %0"
               : "=&s"(keep) : "v"(v), "s"(r), "s"(s), "s"(lds) : "memory");
}
template <int N> DEV void vmwait() { asm volatile("s_waitcnt vmcnt(%0)" ::"n"(N) : "memory"); }
DEV v8bf asbf(v4u v) { return __builtin_bit_cast(v8bf, v); }
DEV v4f mfma(v4u a, v4u b, v4f c) { return __builtin_amdgcn_mfma_f32_16x16x32_bf16(asbf(a), asbf(b), c, 0, 0, 0); }
DEV v2u trrd(unsigned a) { return __builtin_bit_cast(v2u, __builtin_amdgcn_ds_read_tr16_b64_v4i16((LAS v4s*)(size_t)a)); }
DEV v4u trfrag(unsigned a, unsigned step) { v2u x = trrd(a), y = trrd(a + step); return (v4u){x.x, x.y, y.x, y.y}; }

typedef unsigned long long u64;
#define GS(x) #x
#define GX(x) GS(x)
#define G_LD4(k, a, d) "s_mov_b64 exec, %[m" #k "]\n\ts_cbranch_execz 1" #k "f\n\tbuffer_load_dwordx4 v[" #a ":" #d "], %[vo], %[rs], %[s" #k "] offen sc1\n1" #k ":\n\t"
#define G_LD2(k, a, b) "s_mov_b64 exec, %[m" #k "]\n\ts_cbranch_execz 1" #k "f\n\tbuffer_load_dwordx2 v[" #a ":" #b "], %[vo], %[rs], %[s" #k "] offen sc1\n1" #k ":\n\t"
#define G_CK0(k, a, b, c, d) "s_mov_b64 exec, %[m" #k "]\n\t" \
  "v_xor_b32 v" #a ", %[t0], v" #a "\n\tv_xor_b32 v" #b ", %[t1], v" #b "\n\tv_xor_b32 v" #c ", %[t2], v" #c "\n\tv_xor_b32 v" #d ", %[t3], v" #d "\n\t" \
  "v_or3_b32 %[tm], v" #a ", v" #b ", v" #c "\n\tv_bitop3_b32 %[tm], %[tm], v" #d ", %[ff] bitop3:0xa8\n\tv_cmp_ne_u32 %[m" #k "], 0, %[tm]\n\t"
#define G_CK2(k, a, b) "s_mov_b64 exec, %[m" #k "]\n\tv_max_u32 %[tm], v" #a ", v" #b "\n\tv_cmp_eq_u32 %[m" #k "], -1, %[tm]\n\t"
#define G_OR(k) "s_or_b64 %[ac], %[ac], %[m" #k "]\n\t"
#define G_TAIL(sl) "s_cbranch_scc0 9f\n\t" G_WQ "s_sub_u32 %[cn], %[cn], 1\n\ts_cbranch_scc1 9f\n\ts_sleep " GX(sl) "\n\ts_branch 0b\n9:\n\ts_mov_b64 exec, -1\n\ts_nop 1"
#define G_M4 [m0] "+&s"(m[0]), [m1] "+&s"(m[1]), [m2] "+&s"(m[2]), [m3] "+&s"(m[3])
#define G_S4 [s0] "s"(so), [s1] "s"(so + step), [s2] "s"(so + 2 * step), [s3] "s"(so + 3 * step)
#define G_A4 "=&{v[128:131]}"(a[0]), "=&{v[132:135]}"(a[1]), "=&{v[136:139]}"(a[2]), "=&{v[140:143]}"(a[3])
#define G_TMP [tm] "=&v"(tm), [ac] "=&s"(ac), [cn] "+&s"(cn)
#define G_L4 G_LD4(0, 128, 131) G_LD4(1, 132, 135) G_LD4(2, 136, 139) G_LD4(3, 140, 143)
#define G_C4(C) C(0, 128, 129, 130, 131) C(1, 132, 133, 134, 135) C(2, 136, 137, 138, 139) C(3, 140, 141, 142, 143)
#define G_O4 "s_nop 3\n\ts_or_b64 %[ac], %[m0], %[m1]\n\t" G_OR(2) G_OR(3)
#define G_WQ "s_wqm_b64 %[m0], %[m0]\n\ts_wqm_b64 %[m1], %[m1]\n\ts_wqm_b64 %[m2], %[m2]\n\ts_wqm_b64 %[m3], %[m3]\n\t"
#define G_DECL(N) u64 m[N]; for (int i = 0; i < N; ++i) m[i] = ~0ull; unsigned tm, cn = POLL_MAX; u64 ac
#define G_T4 [t0] "s"(tb.x), [t1] "s"(tb.y), [t2] "s"(tb.z), [t3] "s"(tb.w), [ff] "s"(255u)
DEV void gather4f(v4u (&a)[4], rsrc_t r, unsigned vo, unsigned so, unsigned step, v4u tb) {
  G_DECL(4);
  asm volatile("0:\n\t" G_L4 "s_waitcnt vmcnt(0)\n\t" G_C4(G_CK0) G_O4 G_TAIL(PS)
               : G_A4, G_M4, G_TMP : [vo] "v"(vo), [rs] "s"(r), G_S4, G_T4 : "memory", "scc");
}
DEV void gather4s(v2u (&a)[4], rsrc_t r, unsigned vo, unsigned s0, unsigned s1, unsigned s2, unsigned s3) {
  G_DECL(4);
  asm volatile("0:\n\t" G_LD2(0, 128, 129) G_LD2(1, 130, 131) G_LD2(2, 132, 133) G_LD2(3, 134, 135) "s_waitcnt vmcnt(0)\n\t"
               G_CK2(0, 128, 129) G_CK2(1, 130, 131) G_CK2(2, 132, 133) G_CK2(3, 134, 135) G_O4 G_TAIL(PS)
               : "=&{v[128:129]}"(a[0]), "=&{v[130:131]}"(a[1]), "=&{v[132:133]}"(a[2]), "=&{v[134:135]}"(a[3]), G_M4, G_TMP
               : [vo] "v"(vo), [rs] "s"(r), [s0] "s"(s0), [s1] "s"(s1), [s2] "s"(s2), [s3] "s"(s3) : "memory", "scc");
}
struct Watch { unsigned r[4]; };
#define W_STEP(k, sl) "s_waitcnt vmcnt(3)\n\tv_and_b32 %[r" #k "], %[mk], %[r" #k "]\n\tv_cmp_ne_u32 vcc, %[tg], %[r" #k "]\n\ts_nop 3\n\ts_bcnt1_i32_b64 %[bc], vcc\n\ts_cmp_le_u32 %[bc], %[th]\n\ts_cbranch_scc1 9f\n\t" \
  "buffer_load_dword %[r" #k "], %[vo], %[rs], %[so] offen sc1\n\ts_sleep " GX(sl) "\n\t"
#define W_INIT(k, sl) "buffer_load_dword %[r" #k "], %[vo], %[rs], %[so] offen sc1\n\ts_sleep " GX(sl) "\n\t"
#define WATCH(name, sl) \
DEV Watch name(rsrc_t r, unsigned vo, unsigned so, unsigned tag, unsigned mask, u64 lanes, unsigned thr) { \
  Watch w; unsigned cn = POLL_MAX, bc; \
  asm volatile("s_mov_b64 exec, %[ln]\n\t" W_INIT(0, sl) W_INIT(1, sl) W_INIT(2, sl) W_INIT(3, sl) \
               "0:\n\t" W_STEP(0, sl) W_STEP(1, sl) W_STEP(2, sl) W_STEP(3, sl) \
               "s_sub_u32 %[cn], %[cn], 1\n\ts_cbranch_scc0 0b\n9:\n\ts_mov_b64 exec, -1\n\ts_nop 1" \
               : [r0] "=&v"(w.r[0]), [r1] "=&v"(w.r[1]), [r2] "=&v"(w.r[2]), [r3] "=&v"(w.r[3]), [cn] "+&s"(cn), [bc] "=&s"(bc) \
               : [vo] "v"(vo), [rs] "s"(r), [so] "s"(so), [tg] "s"(tag), [mk] "s"(mask), [ln] "s"(lanes), [th] "s"(thr) : "memory", "scc", "vcc"); \
  return w; \
}
WATCH(watch_q, DQS)
WATCH(watch_m, DMS)
DEV void release_watch(Watch& w) { asm volatile("s_waitcnt vmcnt(0)" : : "v"(w.r[0]), "v"(w.r[1]), "v"(w.r[2]), "v"(w.r[3]) : "memory"); }
template <int N, int SL> DEV void gatherp(v4u (&a)[N], rsrc_t r, unsigned vo, unsigned so, unsigned step, unsigned tag) {
#pragma nounroll
  for (int it = 0; it < POLL_MAX; ++it) {
    cfence();
    unsigned bad = 0;
#pragma unroll
    for (int k = 0; k < N; ++k) a[k] = ld_dev(r, vo, so + k * step);
#pragma unroll
    for (int k = 0; k < N; ++k) bad |= (a[k].y ^ tag) | (a[k].w ^ tag);
    if (!__any(bad != 0)) break;
    __builtin_amdgcn_s_sleep(SL);
  }
  cfence();
}

template <int N, int SL, unsigned M> DEV void gatherf(v4u (&a)[N], rsrc_t r, unsigned vo, unsigned so, unsigned step, v4u tb) {
#pragma nounroll
  for (int it = 0; it < POLL_MAX; ++it) {
    cfence();
    unsigned bad = 0;
#pragma unroll
    for (int k = 0; k < N; ++k) a[k] = ld_dev(r, vo, so + k * step);
#pragma unroll
    for (int k = 0; k < N; ++k) { a[k] ^= tb; bad |= (a[k].x | a[k].y) | (a[k].z | a[k].w); }
    if (!__any((bad & M) != 0)) break;
    __builtin_amdgcn_s_sleep(SL);
  }
  cfence();
}

template <int SL, class F> DEV void gather8(v4u (&a)[8], rsrc_t r, unsigned vo, unsigned so, unsigned step, F bad) {
  unsigned need = 255u;
#pragma nounroll
  for (int it = 0; it < POLL_MAX; ++it) {
    cfence();
#pragma unroll
    for (int k = 0; k < 8; ++k) if (need & (1u << k)) a[k] = ld_dev(r, vo + (k & 3) * 1024, so + (k >> 2) * step);
    unsigned nn = 0;
#pragma unroll
    for (int k = 0; k < 8; ++k) nn |= __any(bad(a[k])) ? 1u << k : 0u;
    need = nn;
    if (!need) break;
    __builtin_amdgcn_s_sleep(SL);
  }
  cfence();
}

template <int CTRL> DEV float dpp(float x) { return __builtin_bit_cast(float, __builtin_amdgcn_mov_dpp(__builtin_bit_cast(int, x), CTRL, 0xf, 0xf, true)); }
DEV float x16sum(float x) { auto s = __builtin_amdgcn_permlane16_swap(fbits(x), fbits(x), false, false); return bitsf(s[0]) + bitsf(s[1]); }
DEV float x32sum(float x) { auto s = __builtin_amdgcn_permlane32_swap(fbits(x), fbits(x), false, false); return bitsf(s[0]) + bitsf(s[1]); }
DEV float x16max(float x) { auto s = __builtin_amdgcn_permlane16_swap(fbits(x), fbits(x), false, false); return fmaxf(bitsf(s[0]), bitsf(s[1])); }
DEV float x32max(float x) { auto s = __builtin_amdgcn_permlane32_swap(fbits(x), fbits(x), false, false); return fmaxf(bitsf(s[0]), bitsf(s[1])); }
DEV float wave_sum(float x) {
  x += dpp<0xB1>(x); x += dpp<0x4E>(x); x += dpp<0x141>(x); x += dpp<0x140>(x);
  return x32sum(x16sum(x));
}
DEV float sum8up(float x) { x += dpp<0x128>(x); return x32sum(x16sum(x)); }
DEV float max8up(float x) { x = fmaxf(x, dpp<0x128>(x)); return x32max(x16max(x)); }
DEV v4u dup_lo(v4u v) { v4u o; for (int i = 0; i < 4; ++i) o[i] = __builtin_amdgcn_update_dpp(v[i], v[i], 0x128, 0xf, 0xc, false); return o; }
DEV v4u dup_hi(v4u v) { v4u o; for (int i = 0; i < 4; ++i) o[i] = __builtin_amdgcn_update_dpp(v[i], v[i], 0x128, 0xf, 0x3, false); return o; }
DEV float fdiv(float a, float d, float rd) { float q = a * rd; return fmaf(fmaf(-q, d, a), rd, q); }
DEV float dot2(unsigned a, unsigned b, float c) { return __builtin_amdgcn_fdot2_f32_bf16(__builtin_bit_cast(v2bf, a), __builtin_bit_cast(v2bf, b), c, false); }
struct Stat { float mb, sd, rd; };
DEV Stat mk_stat(float m, float var) {
  Stat st;
  st.mb = rbf(m);
  st.sd = rbf(__builtin_amdgcn_sqrtf(rbf(rbf(var) + 1e-5f)));
  st.rd = __builtin_amdgcn_rcpf(st.sd);
  return st;
}
DEV Stat ln_stat8(const unsigned (&w)[8]) {
  float s = 0.f, s2 = 0.f, q = 0.f, q2 = 0.f;
#pragma unroll
  for (int i = 0; i < 8; i += 2) {
    s = dot2(w[i], 0x3f803f80u, s); q = dot2(w[i], w[i], q);
    s2 = dot2(w[i + 1], 0x3f803f80u, s2); q2 = dot2(w[i + 1], w[i + 1], q2);
  }
  const float m = wave_sum(s + s2) * (1.f / 1024.f);
  float var = fmaf(-m, m, wave_sum(q + q2) * (1.f / 1024.f));
  if (__builtin_amdgcn_readfirstlane(!(m * m <= 16.f * var))) {
    q = 0.f;
#pragma unroll
    for (int i = 0; i < 8; ++i) { float a = bflo(w[i]) - m, b = bfhi(w[i]) - m; q = fmaf(a, a, q); q = fmaf(b, b, q); }
    var = wave_sum(q) * (1.f / 1024.f);
  }
  return mk_stat(m, var);
}
DEV unsigned ln_pair(unsigned x, const Stat& st) {
  unsigned d = pk(bflo(x) - st.mb, bfhi(x) - st.mb);
  return pk(fdiv(bflo(d), st.sd, st.rd), fdiv(bfhi(d), st.sd, st.rd));
}
DEV unsigned onep(unsigned c) { return pk(1.f + bflo(c), 1.f + bfhi(c)); }
DEV unsigned mod_pair(unsigned ln, unsigned c1, unsigned s) {
  unsigned p = pk(bflo(ln) * bflo(c1), bfhi(ln) * bfhi(c1));
  return pk(bflo(p) + bflo(s), bfhi(p) + bfhi(s));
}
DEV unsigned res_pair(unsigned x, unsigned g, float a, float b) {
  unsigned y = pk(a, b);
  unsigned p = pk(bflo(g) * bflo(y), bfhi(g) * bfhi(y));
  return pk(bflo(x) + bflo(p), bfhi(x) + bfhi(p));
}
DEV float silu1(float v) {
  const float d = 1.f + __expf(-v);
  return __builtin_amdgcn_div_fixupf(fdiv(v, d, __builtin_amdgcn_rcpf(d)), d, v);
}

#define STAMP(k)
#define STAMP_DUMP()

DEV v4f unit_reduce(v4f a) {
#pragma unroll
  for (int i = 0; i < 4; ++i) a[i] = sum8up(a[i]);
  return a;
}
DEV void wg_barrier() { asm volatile("s_waitcnt lgkmcnt(0)\n\ts_barrier" ::: "memory"); }
DEV v4f mm4(const v4u (&w)[4], unsigned bb) {
  v4u b[4];
#pragma unroll
  for (int kb = 0; kb < 4; ++kb) b[kb] = lds_ld<v4u>(bb + kb * 64);
  const v4f z = {0.f, 0.f, 0.f, 0.f};
  v4f d0 = mfma(w[0], b[0], z), d1 = mfma(w[1], b[1], z);
  d0 = mfma(w[2], b[2], d0); d1 = mfma(w[3], b[3], d1);
  return d0 + d1;
}

__global__ void __launch_bounds__(NTHR, 3)
fused_kernel(const void* __restrict__ px, const void* __restrict__ pk_, const void* __restrict__ pv, const void* __restrict__ pwq,
             const void* __restrict__ pwo, const void* __restrict__ pwg, const void* __restrict__ pwu, const void* __restrict__ pwd,
             const void* __restrict__ pc, const void* __restrict__ pwm, void* __restrict__ pout, void* __restrict__ pws) {
  const int lane = threadIdx.x & 63;
  const int wave = __builtin_amdgcn_readfirstlane(threadIdx.x >> 6);
  const int wg = blockIdx.x;
  const int n = lane & 15, g = lane >> 4;
  asm volatile("" : : "s"(px), "s"(pk_), "s"(pv), "s"(pwq), "s"(pwo), "s"(pwg), "s"(pwu), "s"(pwd), "s"(pc), "s"(pwm), "s"(pout), "s"(pws));
  const rsrc_t rws = mk(pws, WS_BYTES);
  const int bk = wg & 31, sp = wg >> 5;
  const int ab = bk >> 2, kvh = bk & 3;
  const bool ffn = wg >= 32;
  const int fi = wg - 32;
  const int gks = fi & 3, gct = fi >> 2;
  const int dj = fi & 7, dft = fi >> 3;
  const int oh = wg >> 3, oj = wg & 7;
  const int ur = wg >> 6, uc = wg & 63;

  STAMP(0);
  const int tq = (lane >> 2) & 3, tp = lane & 3;
  const bool hi = n >= 8;
  const unsigned sw64 = (((lane & 7) ^ (((lane >> 4) & 1) << 1)) << 4);
  unsigned ep;

  if (wave < 8) {
    const rsrc_t rk = mk((const char*)pk_ + (size_t)bk * 2097152 + sp * 262144 + wave * 32768, 32768);
    const rsrc_t rv = mk((const char*)pv + (size_t)bk * 2097152 + sp * 262144 + wave * 32768, 32768);
    const unsigned rot = wave + wg;
    if (!ffn) {
      asm volatile("s_barrier" ::: "memory");
      lds_wait<8>(F_GO, 4);
    }
    auto kwait = [&]() { if (ffn) vmwait<WK - 1>(); else vmwait<WKA - 1>(); };
    v4u kf[8][4];
    const unsigned kva = ((n & 3) * 4 + ((n >> 2) & 1)) * 256 + g * 16 + (n >> 3) * 64, kvb = kva ^ 64;
#pragma unroll
    for (int i = 0; i < 32; ++i) {
      const int o = i >> 2, t = (o & 4) | ((o & 1) << 1) | ((o >> 1) & 1);
      if (i == WK) {
        if (ffn) asm volatile("s_barrier" ::: "memory");
        asm volatile("s_load_dword %0, %1, 0x0" : "=s"(ep) : "s"(pws) : "memory");
      }
      if (i >= KA0) kwait(); else if (i >= WK) vmwait<WK - 1>();
      kf[t][i & 3] = ld_nt(rk, ((t & 2) ? kvb : kva) + ((i >> 1) & 1) * 512 + (i & 1) * 128, ((t + rot) & 7) * 4096);
      cfence();
    }
    STAMP(1);
    const unsigned gpitch = ffn ? 7168 : 12288;
    const unsigned gso = ffn ? (gks * 256 + wave * 8) * 7168 + gct * 128 : ((wg & 1) * 512 + wave * 8) * 12288 + 10240 + (wg >> 1) * 128;
    auto grp = [&](int k, bool paced) {
      if (k < 2) {
        const rsrc_t ro = mk(pwo, 4096 * 2048);
        const unsigned c0 = opq(g * 8 * 2048 + ((n ^ (g << 1) ^ (k << 3)) << 4));
        const unsigned so = (oh * 128 + (wave >> 1) * 32 + (wave & 1) * 4 + 2 * k) * 2048 + oj * 256;
        dma16(ro, c0, so, L_W + (wave * 4 + 2 * k) * 1024);
        if (paced) kwait();
        dma16(ro, c0, so + 2048, L_W + (wave * 4 + 2 * k + 1) * 1024);
      } else {
        const int i = k - 2;
        const rsrc_t rg = mk(ffn ? pwg : pwm, 1024 * gpitch), ru = mk(ffn ? pwu : (const char*)pwm + 256 * 12288, (ffn ? 1024 : 768) * gpitch);
        const unsigned v0 = opq((lane >> 3) * gpitch + (sw64 ^ ((wave & 1) << 6)));
        dma16(rg, v0, gso + i * 64 * gpitch, L_G + i * 8192 + wave * 1024);
        if (paced) kwait();
        dma16(ru, v0, gso + i * 64 * gpitch, L_G + 32768 + i * 8192 + wave * 1024);
      }
    };
    int ng = 0;
    if (KD) lds_signal(F_HEAD, lane);
    lds_wait<4>(F_HEAD, KD ? 12 : 4);
#pragma unroll
    for (int k = 0; k < 6; ++k) {
      kwait();
      if (k >= (ffn ? G0 : G0A) && lds_flag(F_Q)) break;
      grp(k, true);
      ng = k + 1;
    }
    if (ng < 2) lds_signal(F_LW, lane);
    STAMP(3);
    lds_wait<2>(F_Q, 1);
    asm volatile("s_waitcnt lgkmcnt(0)" : "+s"(ep) : : "memory");
    const v4u tb = tagbytes(ep + 0x01010101u);
    STAMP(2);
    v4u qf[2];
    {
      const unsigned qa = opq(L_QB + (n & 7) * 288 + (n >> 3) * 64 + g * 16);
      qf[0] = lds_ld<v4u>(qa); qf[1] = lds_ld<v4u>(qa + 128);
    }
    const unsigned vvo = lane * 16;
    v4u vf[4][8];
    auto vload = [&](int i) {
      SCHED();
      if (ffn) vmwait<WV - 1>(); else vmwait<WVA - 1>();
      vf[i >> 3][i & 7] = ld_nt(rv, vvo + (i & 3) * 1024, (((i >> 2) + rot) & 7) * 4096);
      cfence();
      SCHED();
    };
    unsigned sc[4][2];
    float mx = -INFINITY;
    const bool va = (g >> 1) == (n >> 3);
#pragma unroll
    for (int q = 0; q < 4; ++q) {
      const int ta = (q >> 1) * 4 + (q & 1), tbb = ta + 2;
      const v4f z = {0.f, 0.f, 0.f, 0.f};
      v4f a0 = mfma(kf[ta][0], qf[0], z), b0 = mfma(kf[tbb][0], qf[0], z), a1 = mfma(kf[ta][2], qf[0], z), b1 = mfma(kf[tbb][2], qf[0], z);
      a0 = mfma(kf[ta][1], qf[1], a0); b0 = mfma(kf[tbb][1], qf[1], b0); a1 = mfma(kf[ta][3], qf[1], a1); b1 = mfma(kf[tbb][3], qf[1], b1);
      if (q < 3) { vload(2 * q); vload(2 * q + 1); }
      const v4f e0 = sel4(va, a0, b0), e1 = sel4(va, a1, b1);
      float s[4];
#pragma unroll
      for (int r = 0; r < 4; ++r) {
        auto w = __builtin_amdgcn_permlane32_swap(fbits(e0[r]), fbits(e1[r]), false, false);
        s[r] = bitsf(w[0]) + dpp<0x128>(bitsf(w[1]));
      }
#pragma unroll
      for (int r = 0; r < 2; ++r) {
        unsigned u = pk(s[2 * r], s[2 * r + 1]);
        u = pk(bflo(u) * 0.08838834764831845f, bfhi(u) * 0.08838834764831845f);
        sc[q][r] = u;
        mx = fmaxf(mx, fmaxf(bflo(u), bfhi(u)));
      }
    }
    vload(6);
    mx = max8up(mx);
    float ls = 0.f;
    v4u pf[4];
#pragma unroll
    for (int b = 0; b < 2; ++b) {
      v4u p;
#pragma unroll
      for (int e = 0; e < 4; ++e) {
        const unsigned u = sc[2 * b + (e >> 1)][e & 1];
        const float p0 = __expf(bflo(u) - mx), p1 = __expf(bfhi(u) - mx);
        ls += p0 + p1;
        p[e] = pk(p0, p1);
      }
      pf[2 * b] = dup_lo(p); pf[2 * b + 1] = dup_hi(p);
      vload(7 + b);
    }
    ls = sum8up(ls);
    if (lane < 8) { lds_st<float>(L_MX + wave * 32 + lane * 4, mx); lds_st<float>(L_LS + wave * 32 + lane * 4, ls); }
    lds_signal(F_KML, lane);
    STAMP(4);
    v4f acc[8];
    auto piece = [&](int k) {
      const int p = k >> 3, i = k & 7;
      v4u a;
#pragma unroll
      for (int e = 0; e < 4; ++e) a[e] = __builtin_amdgcn_perm(vf[p][2 * e + 1][i >> 1], vf[p][2 * e][i >> 1], (i & 1) ? 0x07060302u : 0x05040100u);
      acc[i] = mfma(a, pf[p], p ? acc[i] : (v4f){0.f, 0.f, 0.f, 0.f});
    };
#pragma unroll
    for (int i = 9; i < 32; ++i) {
      vload(i);
      if (i >= 7 + WV) piece(i - 7 - WV);
    }
#pragma unroll
    for (int k = 25 - WV; k < 24; ++k) piece(k);
    SCHED();
    lds_wait<1>(F_WT, 1);
    const int hd = n & 7, ci = lane & 31;
    const float fw = lds_ld<float>(opq(L_WT + wave * 32 + hd * 4));
    const unsigned wa0 = opq(L_DN + ((hd >> 1) | ((g & 1) << 2) | ((n >> 3) << 3) | ((g >> 1) << 4)) * 256 + ((wave ^ ((hd >> 1) | ((n >> 3) << 2))) << 5) + (hd & 1) * 16);
    const unsigned wa1 = opq(wa0 ^ 16);
    unsigned ra[4];
    {
      const unsigned r0 = L_DN + ((wave >> 1) | (((ci >> 3) & 1) << 2) | (((ci >> 2) & 1) << 3) | ((ci >> 4) << 4) | ((ci & 3) << 5)) * 256 +
                          ((((ci >> 1) & 3) | ((((lane >> 5) ^ (ci >> 3)) & 1) << 2)) << 5) + ((wave ^ ci) & 1) * 16;
#pragma unroll
      for (int k = 0; k < 4; ++k) ra[k] = opq(r0 ^ (k << 5));
    }
    SCHED();
#pragma unroll
    for (int k = 24; k < 32; ++k) piece(k);
    STAMP(5);
    {
#pragma unroll
      for (int i = 0; i < 4; ++i) lds_st<v4f>(((i & 1) ? wa1 : wa0) + i * 8192, sel4(hi, acc[i + 4], acc[i]) * fw);
      wg_barrier();
      v4f s = (lds_ld<v4f>(ra[0]) + lds_ld<v4f>(ra[1])) + (lds_ld<v4f>(ra[2]) + lds_ld<v4f>(ra[3]));
#pragma unroll
      for (int i = 0; i < 4; ++i) s[i] = x32sum(s[i]);
      if (lane < 32) st_dev(tag4(s, tb), rws, ci * 16, O_AO + (kvh * 8 + wave) * 32768 + ab * 4096 + sp * 512);
    }
    STAMP(6);
#pragma unroll
    for (int k = 0; k < S1N; ++k) if (ng <= k && (ffn || k < 2)) grp(k, false);
    auto s2 = [&]() {
#pragma unroll
      for (int k = S1N; k < S2N; ++k) if (ng <= k && ffn) grp(k, false);
    };
    v4u wof[4];
    const unsigned tw = (8 * g + tq) * 256 + (((2 * wave + (tp >> 1)) ^ ((tq | ((g & 1) << 2)) << 1)) << 4) + 8 * (tp & 1);
    const unsigned b1w = opq(L_W + tw);
    const bool wlate = lds_flag(F_LW) != 0;
    if (!wlate) {
#pragma unroll
      for (int kb = 0; kb < 4; ++kb) wof[kb] = trfrag(b1w + kb * 8192, 1024);
    }
    const unsigned ua = opq(L_UN + (lane & 7) * 8);
    const v2u xu = lds_ld<v2u>(ua), gau = lds_ld<v2u>(ua + 64);
    const unsigned sa_ = opq(L_SB + wave * 512 + lane * 8);
    const v2u sbv = lds_ld<v2u>(sa_), cbv = lds_ld<v2u>(sa_ + 4096);
    const unsigned bb = opq(L_A1 + (n & 7) * 288 + g * 16);
    const unsigned uo = ((lane >> 2) & 1) * 2048 + (lane & 3) * 8;
    {
      const unsigned wa = opq(L_A1 + wave * 288 + (lane & 31) * 8);
      const unsigned po = ((n >> 1) & 3) * 262144 + (n & 1) * 64 + g * 16;
      if (D1) __builtin_amdgcn_s_sleep(D1);
      v4u a[4];
      gather4f(a, rws, lane * 16, O_AO + oh * 32768 + wave * 4096, 1024, tb);
      STAMP(7);
      if (S2E) s2();
      v4f o = (asf4(a[0]) + asf4(a[1])) + (asf4(a[2]) + asf4(a[3]));
#pragma unroll
      for (int i = 0; i < 4; ++i) o[i] = x32sum(o[i]);
      if (lane < 32) lds_st<v2u>(wa, (v2u){pk(o[0], o[1]), pk(o[2], o[3])});
      wg_barrier();
      if (wlate) {
#pragma unroll
        for (int kb = 0; kb < 4; ++kb) wof[kb] = trfrag(b1w + kb * 8192, 1024);
      }
      const v4f d = mm4(wof, bb);
      if (n < 8) st_dev(tag4(d, tb), rws, po, O_WP + (oj * 8 + wave) * 4096 + oh * 128);
    }
    STAMP(8);
    if (!S2E) s2();
    v2u x1u = {0u, 0u};
    if (wave < NT2) {
      const unsigned xo = uo + (lane >> 3) * 16384;
      if (D2) __builtin_amdgcn_s_sleep(D2);
      if (wave & 1) __builtin_amdgcn_s_sleep(D2S);
      if (wave & 2) __builtin_amdgcn_s_sleep(2 * D2S);
      v4u a[4];
      gather4f(a, rws, lane * 16, O_WP + wg * 4096, 1024, tb);
      STAMP(9);
      const unsigned done = NT2 > 1 ? *(volatile LAS unsigned*)(size_t)(L_FL + 4 * F_X1) : 0u;
      const v4f s = unit_reduce((asf4(a[0]) + asf4(a[1])) + (asf4(a[2]) + asf4(a[3])));
      x1u.x = canon(res_pair(xu.x, gau.x, s[0], s[1]));
      x1u.y = canon(res_pair(xu.y, gau.y, s[2], s[3]));
      if (!done) st_dev2(x1u, rws, xo, O_X1 + ur * 4096 + uc * 32);
      if (NT2 > 1) lds_st<unsigned>(L_FL + 4 * F_X1, 1u);
      STAMP(10);
    } else {
      vmwait<0>();
    }
    wg_barrier();
#pragma unroll
    for (int k = 2; k < 6; ++k) if (ng <= k && (k >= S2N || !ffn)) grp(k, false);
    auto down = [&](int i0, int i1) {
      const rsrc_t rd = mk(pwd, 3584 * 2048);
      const unsigned c0 = opq(g * 2048 + ((n ^ (g << 1) ^ ((wave & 2) << 2)) << 4));
#pragma unroll
      for (int i = i0; i < i1; ++i) dma16(rd, c0, (dft * 128 + i * 32 + wave * 4) * 2048 + dj * 256, L_DN + i * 8192 + wave * 1024);
    };
    if (ffn) down(0, DS3); else { vmwait<0>(); wg_barrier(); }
    v4u wgf[8];
    const unsigned b2 = opq(L_G + (wave >> 2) * 32768 + (8 * g + tq) * 128 + (((2 * (wave & 3) + (tp >> 1)) ^ ((((tq >> 1) & 1) | ((g & 1) << 1)) << 1)) << 4) + 8 * (tp & 1));
#pragma unroll
    for (int kb = 0; kb < 2 * S2N - 4; ++kb) wgf[kb] = trfrag(b2 + kb * 4096, 512);
    const unsigned pd = ((n >> 1) & 3) * 262144 + (n & 1) * 64 + g * 16;
    {
      const unsigned wa = opq(L_A3 + wave * 544 + lane * 8), b3 = opq((ffn || wave < 4 ? L_A3 : L_RED + 8192) + (n & 7) * 544 + g * 16);
      const unsigned po = ffn ? n * 64 + g * 16 : pd;
      const unsigned pso = ffn ? O_GP + (gct >> 1) * 32768 + ((gct & 1) * 4 + (wave & 3)) * 4096 + gks * 1024 + (wave >> 2) * 512
                               : O_DP + ((wg >> 1) * 4 + (wave & 3)) * 4096 + ((wg & 1) * 2 + (wave >> 2)) * 1024 + 896;
      if (ffn) {
        if (D3) __builtin_amdgcn_s_sleep(D3);
        v2u xv[4];
        const unsigned xo = O_X1 + (wg & 7) * 16384 + wave * 2048;
        gather4s(xv, rws, lane * 8, xo + gks * 512, xo + ((gks + 1) & 3) * 512, xo + ((gks + 2) & 3) * 512, xo + ((gks + 3) & 3) * 512);
        const unsigned xr[8] = {xv[0].x, xv[0].y, xv[1].x, xv[1].y, xv[2].x, xv[2].y, xv[3].x, xv[3].y};
        STAMP(11);
        if (S4E) down(DS3, 4);
        const Stat st = ln_stat8(xr);
        v2u h2;
        h2.x = mod_pair(ln_pair(xr[0], st), cbv.x, sbv.x);
        h2.y = mod_pair(ln_pair(xr[1], st), cbv.y, sbv.y);
        lds_st<v2u>(wa, h2);
        wg_barrier();
      }
      v4u b[8];
#pragma unroll
      for (int kb = 0; kb < 8; ++kb) b[kb] = lds_ld<v4u>(b3 + kb * 64);
#pragma unroll
      for (int kb = 2 * S2N - 4; kb < 8; ++kb) wgf[kb] = trfrag(b2 + kb * 4096, 512);
      const v4f z = {0.f, 0.f, 0.f, 0.f};
      v4f d0 = mfma(wgf[0], b[0], z), d1 = mfma(wgf[1], b[1], z);
#pragma unroll
      for (int kb = 2; kb < 8; kb += 2) { d0 = mfma(wgf[kb], b[kb], d0); d1 = mfma(wgf[kb + 1], b[kb + 1], d1); }
      d0 += d1;
      if (n < 8) st_dev(tag4(d0, tb), rws, po, pso);
    }
    STAMP(12);
    if (ffn) {
      {
        if (!S4E) down(DS3, 4);
        v4u wdf[4];
        const unsigned b1 = opq(L_DN + tw);
#pragma unroll
        for (int kb = 0; kb < DS3; ++kb) wdf[kb] = trfrag(b1 + kb * 8192, 1024);
        const unsigned wa = opq(L_A4 + ((lane >> 2) & 7) * 288 + wave * 32 + (lane & 3) * 8);
        if (D4) __builtin_amdgcn_s_sleep(D4);
        v4u a[4];
        gather4f(a, rws, lane * 16, O_GP + dft * 32768 + wave * 4096, 1024, tb);
        STAMP(13);
        const v4f t = (asf4(a[0]) + asf4(a[1])) + (asf4(a[2]) + asf4(a[3]));
        const unsigned t0 = pk(t[0], t[1]), t1 = pk(t[2], t[3]);
        auto w0 = __builtin_amdgcn_permlane32_swap(t0, t0, false, false), w1 = __builtin_amdgcn_permlane32_swap(t1, t1, false, false);
        auto relu = [](float v) { return v > 0.f ? v : v * 0.f; };
        const v2u hv = {pk(relu(bflo(w0[0])) * bflo(w0[1]), relu(bfhi(w0[0])) * bfhi(w0[1])),
                        pk(relu(bflo(w1[0])) * bflo(w1[1]), relu(bfhi(w1[0])) * bfhi(w1[1]))};
        if (lane < 32) lds_st<v2u>(wa, hv);
        wg_barrier();
#pragma unroll
        for (int kb = DS3; kb < 4; ++kb) wdf[kb] = trfrag(b1 + kb * 8192, 1024);
        const v4f d = mm4(wdf, bb + (L_A4 - L_A1));
        if (n < 8) st_dev(tag4(d, tb), rws, pd, O_DP + (dj * 8 + wave) * 4096 + (dft + dft / 7) * 128);
      }
      STAMP(14);
    } else {
      __builtin_amdgcn_s_sleep(DA); __builtin_amdgcn_s_sleep(DA);
    }
    if (wave < NT2) {
      if (D5) __builtin_amdgcn_s_sleep(D5);
      if (wave & 1) __builtin_amdgcn_s_sleep(D2S);
      if (wave & 2) __builtin_amdgcn_s_sleep(2 * D2S);
      v4u a[4];
      gather4f(a, rws, lane * 16, O_DP + wg * 4096, 1024, tb);
      const v4f t = (asf4(a[0]) + asf4(a[1])) + (asf4(a[2]) + asf4(a[3]));
      const v4f s = unit_reduce(sel4(lane >= 56, (v4f){0.f, 0.f, 0.f, 0.f}, t));
      const v2u o = {res_pair(x1u.x, pk(t[0], t[1]), s[0], s[1]), res_pair(x1u.y, pk(t[2], t[3]), s[2], s[3])};
      if (lane >= 56) __builtin_amdgcn_raw_buffer_store_b64(o, mk(pout, 16384), uo, ur * 4096 + uc * 32, 0);
      if (wg == 0 && wave == 0 && lane == 0) __builtin_amdgcn_raw_buffer_store_b32(ep + 0x01010101u, rws, 0, WS_EPOCH, 16);
      STAMP(15);
    }
    STAMP_DUMP();
    return;
  }

  const int cw = wave - 8;
  __builtin_amdgcn_s_setprio(2);
  unsigned cb_n = 0;
  const bool jq = ffn && fi < 128;
  const int ks = !ffn || jq ? (wg & 1) : (fi >= 176);
  const int jt = jq ? (fi >> 1) : fi < 176 ? fi - 128 : fi - 176;
  v4u in[4];
  {
    const rsrc_t ri = mk(jq ? px : pc, 16384);
    const unsigned vo = (n & 7) * 2048 + cw * 256 + (n >> 3) * 64 + g * 16;
    asm volatile("buffer_load_dwordx4 %0, %4, %5, %6 offen\n\tbuffer_load_dwordx4 %1, %4, %5, %6 offen offset:128\n\t"
                 "buffer_load_dwordx4 %2, %4, %5, %7 offen\n\tbuffer_load_dwordx4 %3, %4, %5, %7 offen offset:128"
                 : "=&v"(in[0]), "=&v"(in[1]), "=&v"(in[2]), "=&v"(in[3]) : "v"(vo), "s"(ri), "s"(ks * 1024), "s"(1024 - ks * 1024) : "memory");
  }
  const unsigned rowb = jq ? 8192 : 12288;
  const rsrc_t rh = mk(jq ? pwq : pwm, 1024 * rowb);
  const unsigned v0 = (lane >> 3) * rowb + sw64, v1 = v0 ^ 64;
  const unsigned col0 = ffn ? (ks * 512 + cw * 128) * rowb + (jq ? 0 : 4096) + jt * 128 : ((ks ^ 1) * 512 + cw * 128) * rowb + wg * 128;
#pragma unroll
  for (int k = 0; k < 8; ++k) dma16(rh, (k & 1) ? v1 : v0, k * 8 * rowb + col0, cw * 32768 + k * 1024);
  if (!ffn) st_dev((v4u){SENT, SENT, SENT, SENT}, rws, lane * 16, O_X1 + wg * 4096 + cw * 1024);
  if (cw == 0 && lane < 32) lds_st<unsigned>(L_FL + 4 * lane, 0u);
  asm volatile("s_waitcnt lgkmcnt(0)\n\ts_barrier\n\ts_load_dword %0, %1, 0x0" : "=s"(ep) : "s"(pws) : "memory");
  if (!ffn) {
#pragma unroll
    for (int k = 8; k < 16; ++k) dma16(rh, (k & 1) ? v1 : v0, k * 8 * rowb + col0, cw * 32768 + k * 1024);
    vmwait<17>();
  } else {
    vmwait<8>();
  }
  asm volatile("" : "+v"(in[0]), "+v"(in[1]), "+v"(in[2]), "+v"(in[3]) : : "memory");
  STAMP(1);
  if (!jq) {
#pragma unroll
    for (int p = 0; p < 4; ++p) {
      if (p == 2 && ffn) break;
#pragma unroll
      for (int e = 0; e < 4; ++e) in[p][e] = pk(silu1(bflo(in[p][e])), silu1(bfhi(in[p][e])));
    }
  } else {
    float s = 0.f;
#pragma unroll
    for (int p = 0; p < 4; ++p)
#pragma unroll
      for (int e = 0; e < 4; ++e) s += bflo(in[p][e]) + bfhi(in[p][e]);
    s = sum8up(s);
    if (lane < 8) lds_st<float>(L_SX + lane * 16 + cw * 4, s);
    lds_signal(F_CBAR, lane); cb_n += 4; lds_wait<1>(F_CBAR, cb_n);
    v4f t = lds_ld<v4f>(L_SX + (n & 7) * 16);
    const float m = ((t[0] + t[1]) + (t[2] + t[3])) * (1.f / 1024.f);
    s = 0.f;
#pragma unroll
    for (int p = 0; p < 4; ++p)
#pragma unroll
      for (int e = 0; e < 4; ++e) { float a = bflo(in[p][e]) - m, b = bfhi(in[p][e]) - m; s = fmaf(a, a, s); s = fmaf(b, b, s); }
    s = sum8up(s);
    if (lane < 8) lds_st<float>(L_SX + 128 + lane * 16 + cw * 4, s);
    lds_signal(F_CBAR, lane); cb_n += 4; lds_wait<1>(F_CBAR, cb_n);
    t = lds_ld<v4f>(L_SX + 128 + (n & 7) * 16);
    const Stat st = mk_stat(m, ((t[0] + t[1]) + (t[2] + t[3])) * (1.f / 1024.f));
#pragma unroll
    for (int p = 0; p < 2; ++p)
#pragma unroll
      for (int e = 0; e < 4; ++e) in[p][e] = ln_pair(in[p][e], st);
  }
  STAMP(2);
  asm volatile("s_waitcnt lgkmcnt(0)" : "+s"(ep) : : "memory");
  const unsigned TAG = ep + 0x01010101u;
  const v4u tn = tagbytes(TAG) & 15u;
  unsigned tba[4];
  {
    const unsigned sw = ((tq >> 1) & 1) | ((g & 1) << 1);
    const unsigned tb0 = cw * 32768 + (8 * g + tq) * 128 + (((tp >> 1) ^ (sw << 1)) << 4) + 8 * (tp & 1);
#pragma unroll
    for (int t = 0; t < 4; ++t) tba[t] = opq(tb0 ^ (t << 5));
  }
  v4u wf[4][4];
  v4f d[4];
  auto zero = [&]() {
#pragma unroll
    for (int t = 0; t < 4; ++t) d[t] = (v4f){0.f, 0.f, 0.f, 0.f};
  };
  auto frag = [&](int kb, unsigned off) {
#pragma unroll
    for (int t = 0; t < 4; ++t) wf[kb][t] = trfrag(tba[t] + off + kb * 4096, 512);
  };
  auto mm = [&](int kb, const v4u& x) {
    const v4u b = (kb & 1) ? dup_hi(x) : dup_lo(x);
#pragma unroll
    for (int t = 0; t < 4; ++t) d[t] = mfma(wf[kb][t], b, d[t]);
  };
  auto reduce = [&](unsigned buf, unsigned rb) {
    const unsigned ra = buf + cw * 2048 + (n >> 3) * 1024 + (n & 7) * 64 + g * 16;
    lds_st<v4f>(ra, sel4(hi, d[2], d[0]));
    lds_st<v4f>(ra + 512, sel4(hi, d[3], d[1]));
    lds_signal(F_CBAR, lane); cb_n += 4; lds_wait<0>(F_CBAR, cb_n);
    rb += buf + (lane >> 5) * 4096;
    v4f s = lds_ld<v4f>(rb) + lds_ld<v4f>(rb + 2048);
#pragma unroll
    for (int i = 0; i < 4; ++i) s[i] = x32sum(s[i]);
    return s;
  };
  if (!ffn) {
#pragma unroll
    for (int k = 0; k < 16; ++k) dma16(rh, (k & 1) ? v1 : v0, (ks * 1024 - 512 + k * 8) * rowb + col0, cw * 32768 + 16384 + k * 1024);
    lds_signal(F_GO, lane);
    {
      const unsigned a = (cw < 2 ? L_A3 : L_RED + 8192) + (n & 7) * 544 + (cw & 1) * 256 + (n >> 3) * 64 + g * 16;
      lds_st<v4u>(a, in[0]); lds_st<v4u>(a + 128, in[1]);
    }
    zero();
    vmwait<16>();
#pragma unroll
    for (int kb = 0; kb < 4; ++kb) frag(kb, 0);
#pragma unroll
    for (int kb = 0; kb < 4; ++kb) {
      mm(kb, in[2 + (kb >> 1)]);
      SCHED();
      if (kb == 0) vmwait<12>(); if (kb == 1) vmwait<8>(); if (kb == 2) vmwait<4>(); if (kb == 3) vmwait<0>();
      frag(kb, 16384);
      if (kb) mm(kb - 1, in[(kb - 1) >> 1]);
      SCHED();
    }
    mm(3, in[1]);
    lds_signal(F_HEAD, lane);
    const v4f s = reduce(L_RED, cw * 512 + (lane & 31) * 16);
    st_dev((v4u){pk(s[0], s[1]), TAG, pk(s[2], s[3]), TAG}, rws, (lane >> 5) * 65536 + (lane & 1) * 1024 + ((lane >> 1) & 1) * 256 + ((lane >> 2) & 7) * 16,
           O_SC + wg * 2048 + (cw & 1) * 512 + (cw >> 1) * 128);
    STAMP(11);
  } else {
#pragma unroll
    for (int k = 8; k < 16; ++k) {
      if (jq) vmwait<WH1 - 1>(); else vmwait<WH0 - 1>();
      dma16(rh, (k & 1) ? v1 : v0, k * 8 * rowb + col0, cw * 32768 + k * 1024);
    }
    vmwait<12>(); frag(0, 0);
    vmwait<8>(); frag(1, 0);
    vmwait<4>(); frag(2, 0);
    vmwait<0>(); frag(3, 0);
    asm volatile("s_waitcnt lgkmcnt(0)" ::: "memory");
    lds_signal(F_HEAD, lane);
    STAMP(3);
    if (jq) {
      const unsigned so = O_SC + ((fi >> 1) & 1) * 65536 + ks * 16384 + cw * 4096;
      Watch w = watch_m(rws, ((lane & 2) ? 32768 : 0) + (lane & 1) * 2048 + 4, so, TAG, ~0u, 0xfull, THM);
      STAMP(4);
      v4u a[8];
      gather8<DMR>(a, rws, lane * 16, so, 32768, [&](v4u x) { return ((x.y ^ TAG) | (x.w ^ TAG)) != 0; });
      release_watch(w);
      STAMP(5);
#pragma unroll
      for (int p = 0; p < 2; ++p)
#pragma unroll
        for (int e = 0; e < 4; ++e) in[p][e] = mod_pair(in[p][e], onep(a[4 + 2 * p + (e >> 1)][(e & 1) * 2]), a[2 * p + (e >> 1)][(e & 1) * 2]);
    }
    zero();
#pragma unroll
    for (int kb = 0; kb < 4; ++kb) mm(kb, in[kb >> 1]);
    const unsigned r1 = (lane >> 4) & 1;
    v4f s = reduce(L_RED + 8192, (n >> 2) * 512 + (2 * cw + r1) * 64 + (n & 3) * 16);
    if (jq || !ks) {
      if (lane < 32) st_dev(tag4<15u>(s, tn), rws, r1 * (jq ? 32768 : 256) + n * 16,
                            jq ? O_Q + cw * 65536 + (jt >> 4) * 8192 + ks * 4096 + (jt & 15) * 256 : O_RP + jt * 2048 + cw * 512);
    } else {
      v4u a[1];
      gatherf<1, 32, 15u>(a, rws, (lane & 31) * 16, O_RP + jt * 2048 + cw * 512, 0, tn);
      s += asf4(a[0]);
      if (lane < 32) st_dev((v4u){pk(s[0], s[1]), TAG, pk(s[2], s[3]), TAG}, rws, r1 * 16384 + n * 16, O_MOD + cw * 32768 + jt * 256);
    }
  }
  STAMP(6);
  if (cw == 3) {
    if (!jq) __builtin_amdgcn_s_sleep(DQ0);
    const unsigned qo = O_Q + ab * 32768 + kvh * 8192;
    Watch w = watch_q(rws, (lane & 31) * 256 + (sp & 1) * 128, qo, tn.x, 15u, 0xffffffffull, THQ);
    STAMP(7);
    v4u a[8];
    gather8<DMR>(a, rws, lane * 16, qo, 4096, [&](v4u x) { x ^= tn; return (((x.x | x.y) | (x.z | x.w)) & 15u) != 0; });
    release_watch(w);
#pragma unroll
    for (int k = 0; k < 4; ++k) {
      const v4f t = asf4(a[k] ^ tn) + asf4(a[4 + k] ^ tn);
      lds_st<v2u>(L_QB + (2 * k + (lane >> 5)) * 288 + (lane & 31) * 8, (v2u){pk(t[0], t[1]), pk(t[2], t[3])});
    }
    lds_signal(F_Q, lane);
    if (lane == 0) lds_st<unsigned>(L_FL + 4 * F_HEAD, 64u);
    STAMP(8);
    lds_wait<8>(F_KML, 8);
    const float mw = lds_ld<float>(L_MX + lane * 4), lw = lds_ld<float>(L_LS + lane * 4);
    const float ms = max8up(mw);
    const float lsum = sum8up(lw * __expf(mw - ms));
    if (lane < 8) st_dev((v4u){fbits(ms), TAG, fbits(lsum), TAG}, rws, lane * 16, O_ML + bk * 1024 + sp * 128);
    STAMP(9);
    __builtin_amdgcn_s_sleep(32);
    v4u ml[1];
    gatherp<1, 24>(ml, rws, lane * 16, O_ML + bk * 1024, 0, TAG);
    const float M = max8up(bitsf(ml[0].x));
    const float L = sum8up(bitsf(ml[0].z) * __expf(bitsf(ml[0].x) - M));
    lds_st<float>(L_WT + lane * 4, __expf(mw - M) / L);
    lds_signal(F_WT, lane);
    STAMP(10);
  } else {
    lds_wait<32>(F_Q, 1);
    if (cw == 2) {
      const unsigned r1 = (lane >> 2) & 1;
      const v2u xu = __builtin_amdgcn_raw_buffer_load_b64(mk(px, 16384), r1 * 2048 + (lane & 3) * 8, ur * 4096 + uc * 32, 0);
      v4u a[1];
      gatherp<1, 32>(a, rws, r1 * 16384 + (lane & 3) * 16, O_MOD + ur * 32768 + uc * 64, 0, TAG);
      if (lane < 8) { lds_st<v2u>(L_UN + lane * 8, xu); lds_st<v2u>(L_UN + 64 + lane * 8, (v2u){a[0].x, a[0].z}); }
      STAMP(8);
    } else if (ffn) {
      v4u a[8];
      gatherp<8, 32>(a, rws, lane * 16, O_MOD + 4096 + cw * 4096 + gks * 1024, 16384, TAG);
#pragma unroll
      for (int k = 0; k < 8; ++k)
        lds_st<v2u>(L_SB + cw * 4096 + k * 512 + lane * 8, cw ? (v2u){onep(a[k].x), onep(a[k].z)} : (v2u){a[k].x, a[k].z});
      STAMP(8);
    }
  }
  STAMP_DUMP();
  asm volatile("s_waitcnt vmcnt(0) lgkmcnt(0)" ::: "memory");
}

extern "C" void kernel_launch(void* const* d_in, const int* in_sizes, int n_in, void* d_out, int out_size, void* d_ws, size_t ws_size,
                              hipStream_t stream) {
  (void)in_sizes; (void)n_in; (void)out_size; (void)ws_size;
  (void)hipFuncSetAttribute((const void*)fused_kernel, hipFuncAttributeMaxDynamicSharedMemorySize, L_END);
  fused_kernel<<<NWG, NTHR, L_END, stream>>>(d_in[0], d_in[1], d_in[2], d_in[3], d_in[4], d_in[6], d_in[7], d_in[8], d_in[9], d_in[10],
                                             d_out, d_ws);
}
```

```cpp
#include <hip/hip_runtime.h>
#include <stdint.h>

typedef unsigned v4u __attribute__((ext_vector_type(4)));
typedef unsigned v2u __attribute__((ext_vector_type(2)));
typedef float v4f __attribute__((ext_vector_type(4)));
typedef float v2f __attribute__((ext_vector_type(2)));
typedef __bf16 v2bf __attribute__((ext_vector_type(2)));
typedef __bf16 v8bf __attribute__((ext_vector_type(8)));
typedef short v4s __attribute__((ext_vector_type(4)));
#define LAS __attribute__((address_space(3)))
typedef __amdgpu_buffer_rsrc_t rsrc_t;
#define DEV __device__ __forceinline__

#define WS 2
#define WB 6
#define TAU 33
#define C0 330
#define KA0 8
#define WH1 8
#define WH0 8
#define DQ0 40
#define DQS 16
#define DMS 6
#define THM 2
#define THQ 8
#define DMR 2
#define D1 8
#define D2 8
#define D3 0
#define D4 0
#define D5 60
#define TH1 32
#define TH2 256
#define TH3 32
#define TH4 64
#define TH5 128
#define PSL 3
#define GB 0
#define GW 8
#define DW 2
#define GT 0
#define DT 0
#define QT 1350
#define THW 2
#define FW 6
#define SD 300
#define DD 0

constexpr int NWG = 256, NTHR = 768;
constexpr unsigned SENT = 0xFFFFFFFFu;
constexpr unsigned WS_EPOCH = 0;
constexpr unsigned O_SC = 65536;
constexpr unsigned O_MOD = O_SC + 131072;
constexpr unsigned O_Q = O_MOD + 65536;
constexpr unsigned O_GX = O_Q + 262144;
constexpr unsigned O_ML = O_GX + 262144;
constexpr unsigned O_X1 = O_ML + 32768;
constexpr unsigned O_AO = O_X1 + 131072;
constexpr unsigned O_WP = O_AO + 1048576;
constexpr unsigned O_GP = O_WP + 1048576;
constexpr unsigned O_DP = O_GP + 14 * 32768;
constexpr unsigned O_STAMP = 65536 + 256 * 16384 + 1048576;
static_assert(O_DP + 524288 <= O_STAMP, "ws map");
constexpr unsigned WS_BYTES = O_STAMP + NWG * 12 * 16 * 8;
constexpr unsigned L_MG = 98304;
constexpr unsigned L_M = 131072;
constexpr unsigned L_RED = L_M;
constexpr unsigned L_SB = L_RED;
constexpr unsigned L_AX = L_RED + 16384;
constexpr unsigned L_QB = L_AX + 8448;
constexpr unsigned L_MX = L_QB + 2304;
constexpr unsigned L_LS = L_MX + 256;
constexpr unsigned L_SX = L_LS + 256;
constexpr unsigned L_FL = L_SX + 256;
constexpr unsigned L_ST = L_FL + 128;
constexpr unsigned L_END = L_ST + 12 * 16 * 8;
static_assert(L_END <= 163840, "lds");
static_assert(WB <= 8 && GB <= 8, "windows");
enum { F_CBAR, F_Q, F_KML, F_GO, F_HEAD, F_WT };

DEV float bflo(unsigned u) { return __builtin_bit_cast(float, u << 16); }
DEV float bfhi(unsigned u) { return __builtin_bit_cast(float, u & 0xffff0000u); }
DEV unsigned pk(float a, float b) {
  v2f f = {a, b};
  unsigned u = __builtin_bit_cast(unsigned, __builtin_convertvector(f, v2bf));
  asm("" : "+v"(u));
  return u;
}
DEV float rbf(float a) { return bflo(pk(a, a)); }
DEV unsigned fbits(float a) { return __builtin_bit_cast(unsigned, a); }
DEV float bitsf(unsigned a) { return __builtin_bit_cast(float, a); }
DEV unsigned canon(unsigned a) { return a < 0xFFFFFFFEu ? a : 0xFFFFFFFEu; }
DEV v4f sel4(bool c, v4f a, v4f b) { return (v4f){c ? a.x : b.x, c ? a.y : b.y, c ? a.z : b.z, c ? a.w : b.w}; }
DEV v4f asf4(v4u v) { return __builtin_bit_cast(v4f, v); }
DEV unsigned opq(unsigned a) { asm("" : "+v"(a)); return a; }
DEV v4u tagbytes(unsigned T) { return (v4u){T & 255u, (T >> 8) & 255u, (T >> 16) & 255u, T >> 24}; }
template <unsigned M = 255u> DEV v4u tag4(v4f v, v4u tb) {
  v4u o;
  for (int i = 0; i < 4; ++i) o[i] = ((fbits(v[i]) + (M + 1) / 2) & ~M) | tb[i];
  return o;
}

DEV v4u tagv(v4f v, unsigned tg) {
  v4u o;
  for (int i = 0; i < 4; ++i) o[i] = __builtin_amdgcn_perm(fbits(v[i]), tg, 0x07060500u + i);
  return o;
}

template <class T> DEV T lds_ld(unsigned a) { return *(LAS T*)(size_t)a; }
template <class T> DEV void lds_st(unsigned a, T v) { *(LAS T*)(size_t)a = v; }
DEV void cfence() { asm volatile("" ::: "memory"); }
#define SCHED() __builtin_amdgcn_sched_barrier(0)
DEV void lds_signal(int f, int lane) {
  cfence();
  if (lane == 0) __hip_atomic_fetch_add((LAS unsigned*)(size_t)(L_FL + 4 * f), 1u, __ATOMIC_RELAXED, __HIP_MEMORY_SCOPE_WORKGROUP);
  cfence();
}
DEV unsigned lds_flag(int f) { return __builtin_amdgcn_readfirstlane(*(volatile LAS unsigned*)(size_t)(L_FL + 4 * f)); }
template <int SLEEP> DEV void lds_wait(int f, unsigned target) {
  cfence();
#pragma nounroll
  for (int i = 0; i < 4000000; ++i) {
    if (lds_flag(f) >= target) break;
    if (SLEEP) __builtin_amdgcn_s_sleep(SLEEP);
  }
  cfence();
}
constexpr int POLL_MAX = 200000;

DEV rsrc_t mk(const void* p, unsigned bytes) { return __builtin_amdgcn_make_buffer_rsrc((void*)p, 0, bytes, 0x00020000); }
DEV v4u ld_nt(rsrc_t r, unsigned v, unsigned s) { return __builtin_amdgcn_raw_buffer_load_b128(r, v, s, 2); }
DEV v4u ld_dev(rsrc_t r, unsigned v, unsigned s) { return __builtin_amdgcn_raw_buffer_load_b128(r, v, s, 16); }
DEV void st_dev(v4u d, rsrc_t r, unsigned v, unsigned s) {
  asm volatile("s_nop 0\n\tbuffer_store_dwordx4 %0, %1, %2, %3 offen sc1\n\ts_nop 1" : : "v"(d), "v"(v), "s"(r), "s"(s) : "memory");
}
DEV void st_dev2(v2u d, rsrc_t r, unsigned v, unsigned s) {
  asm volatile("s_nop 0\n\tbuffer_store_dwordx2 %0, %1, %2, %3 offen sc1" : : "v"(d), "v"(v), "s"(r), "s"(s) : "memory");
}
template <bool DEVSCOPE = false> DEV void dma16(rsrc_t r, unsigned v, unsigned s, unsigned lds) {
  unsigned keep;
  if (DEVSCOPE)
    asm volatile("s_mov_b32 %0, m0\n\ts_mov_b32 m0, %4\n\ts_nop 0\n\tbuffer_load_dwordx4 %1, %2, %3 offen sc1 lds\n\ts_mov_b32 m0, %0"
                 : "=&s"(keep) : "v"(v), "s"(r), "s"(s), "s"(lds) : "memory");
  else
  asm volatile("s_mov_b32 %0, m0\n\ts_mov_b32 m0, %4\n\ts_nop 0\n\tbuffer_load_dwordx4 %1, %2, %3 offen nt lds\n\ts_mov_b32 m0, %0"
               : "=&s"(keep) : "v"(v), "s"(r), "s"(s), "s"(lds) : "memory");
}
#define LD_NT(d, r, v, s, off) asm volatile("buffer_load_dwordx4 %0, %1, %2, %3 offen offset:%4 nt" : "=v"(d) : "v"(v), "s"(r), "s"(s), "n"(off) : "memory")
#define TIE8(a, b, c, d, e, f, g, h) asm volatile("" : "+v"(a), "+v"(b), "+v"(c), "+v"(d), "+v"(e), "+v"(f), "+v"(g), "+v"(h) : : "memory")
template <int N> DEV void vmwait() { asm volatile("s_waitcnt vmcnt(%0)" ::"n"(N) : "memory"); }
DEV v8bf asbf(v4u v) { return __builtin_bit_cast(v8bf, v); }
DEV v4f mfma(v4u a, v4u b, v4f c) { return __builtin_amdgcn_mfma_f32_16x16x32_bf16(asbf(a), asbf(b), c, 0, 0, 0); }
DEV v2u trrd(unsigned a) { return __builtin_bit_cast(v2u, __builtin_amdgcn_ds_read_tr16_b64_v4i16((LAS v4s*)(size_t)a)); }
DEV v4u trfrag(unsigned a, unsigned step) { v2u x = trrd(a), y = trrd(a + step); return (v4u){x.x, x.y, y.x, y.y}; }

typedef unsigned long long u64;
#define GS(x) #x
#define GX(x) GS(x)
#define P_LD(k, a, d, so, off) "s_mov_b64 exec, %[m" #k "]\n\ts_cbranch_execz 1" #k "f\n\tbuffer_load_dwordx4 v[" #a ":" #d "], %[vo], %[rs], %[" #so "] offen offset:" #off " sc1\n1" #k ":\n\t"
#define P_CK(k, a, b, c, d, e, f, g, h) "s_mov_b64 exec, %[m" #k "]\n\t" \
  "v_xor_b32 v" #e ", %[t0], v" #a "\n\tv_xor_b32 v" #f ", %[t1], v" #b "\n\tv_xor_b32 v" #g ", %[t2], v" #c "\n\tv_xor_b32 v" #h ", %[t3], v" #d "\n\t" \
  "v_or3_b32 %[tm], v" #e ", v" #f ", v" #g "\n\tv_bitop3_b32 %[tm], %[tm], v" #h ", %[ff] bitop3:0xa8\n\tv_cmp_ne_u32 %[m" #k "], 0, %[tm]\n\t"
#define P_CS(k, a, b, c, d, e, f, g, h) "s_mov_b64 exec, %[m" #k "]\n\t" \
  "v_mov_b32 v" #e ", v" #a "\n\tv_mov_b32 v" #f ", v" #b "\n\tv_mov_b32 v" #g ", v" #c "\n\tv_mov_b32 v" #h ", v" #d "\n\t" \
  "v_max3_u32 %[tm], v" #e ", v" #f ", v" #g "\n\tv_max_u32 %[tm], %[tm], v" #h "\n\tv_cmp_eq_u32 %[m" #k "], -1, %[tm]\n\t"
#define P_CNT2 "s_bcnt1_i32_b64 %[bc], %[m0]\n\ts_bcnt1_i32_b64 %[b2], %[m1]\n\ts_add_u32 %[bc], %[bc], %[b2]\n\t"
#define P_CNT4 P_CNT2 "s_bcnt1_i32_b64 %[b2], %[m2]\n\ts_add_u32 %[bc], %[bc], %[b2]\n\ts_bcnt1_i32_b64 %[b2], %[m3]\n\ts_add_u32 %[bc], %[bc], %[b2]\n\t"
#define P_WAIT "s_cmpk_le_u32 %[bc], %[th]\n\ts_cbranch_scc1 2f\n\ts_waitcnt vmcnt(0)\n\ts_branch 3f\n2:\n\ts_sleep %[sl]\n3:\n\t"
#define P_OR2 "s_nop 3\n\ts_or_b64 %[ac], %[m0], %[m1]\n\t"
#define P_OR4 P_OR2 "s_or_b64 %[ac], %[ac], %[m2]\n\ts_or_b64 %[ac], %[ac], %[m3]\n\t"
#define P_WQ2 "s_cbranch_scc0 9f\n\ts_wqm_b64 %[m0], %[m0]\n\ts_wqm_b64 %[m1], %[m1]\n\t"
#define P_WQ4 P_WQ2 "s_wqm_b64 %[m2], %[m2]\n\ts_wqm_b64 %[m3], %[m3]\n\t"
#define P_END "s_sub_u32 %[cn], %[cn], 1\n\ts_cbranch_scc0 0b\n9:\n\ts_mov_b64 exec, -1\n\ts_nop 1"
#define P_DECL u64 m[4] = {~0ull, ~0ull, ~0ull, ~0ull}, ac; unsigned tm, cn = POLL_MAX, bc, b2
#define P_O2 "=&{v[112:115]}"(a[0]), "=&{v[116:119]}"(a[1]), "+{v[128:131]}"(w[0]), "+{v[132:135]}"(w[1]), [m0] "+&s"(m[0]), [m1] "+&s"(m[1]), \
  [tm] "=&v"(tm), [ac] "=&s"(ac), [cn] "+&s"(cn), [bc] "=&s"(bc), [b2] "=&s"(b2)
#define P_O4 P_O2, "=&{v[120:123]}"(a[2]), "=&{v[124:127]}"(a[3]), "+{v[136:139]}"(w[2]), "+{v[140:143]}"(w[3]), [m2] "+&s"(m[2]), [m3] "+&s"(m[3])
#define P_IN [vo] "v"(vo), [rs] "s"(r), [so] "s"(so), [th] "n"(TH), [sl] "n"(PSL)
#define P_TB [t0] "s"(tb.x), [t1] "s"(tb.y), [t2] "s"(tb.z), [t3] "s"(tb.w), [ff] "s"(255u)
#define P_C01(C) C(0, 128, 129, 130, 131, 112, 113, 114, 115) C(1, 132, 133, 134, 135, 116, 117, 118, 119)
template <int TH> DEV void poll4(v4u (&a)[4], v4u (&w)[4], rsrc_t r, unsigned vo, unsigned so, v4u tb) {
  P_DECL;
  asm volatile("0:\n\t" P_LD(0, 128, 131, so, 0) P_LD(1, 132, 135, so, 1024) P_LD(2, 136, 139, so, 2048) P_LD(3, 140, 143, so, 3072) P_CNT4 P_WAIT
               P_C01(P_CK) P_CK(2, 136, 137, 138, 139, 120, 121, 122, 123) P_CK(3, 140, 141, 142, 143, 124, 125, 126, 127) P_OR4 P_WQ4 P_END
               : P_O4 : P_IN, P_TB : "memory", "scc");
}
template <int TH> DEV void poll2(v4u (&a)[2], v4u (&w)[4], rsrc_t r, unsigned vo, unsigned so, v4u tb) {
  P_DECL;
  m[1] = 0xffffffffffffull;
  asm volatile("0:\n\t" P_LD(0, 128, 131, so, 0) P_LD(1, 132, 135, so, 1024) P_CNT2 P_WAIT P_C01(P_CK) P_OR2 P_WQ2 P_END
               : P_O2 : P_IN, P_TB : "memory", "scc");
}
template <int TH> DEV void poll2s(v4u (&a)[2], v4u (&w)[4], rsrc_t r, unsigned vo, unsigned so, unsigned s1) {
  P_DECL;
  asm volatile("0:\n\t" P_LD(0, 128, 131, so, 0) P_LD(1, 132, 135, s1, 0) P_CNT2 P_WAIT P_C01(P_CS) P_OR2 P_WQ2 P_END
               : P_O2 : P_IN, [s1] "s"(s1) : "memory", "scc");
}
#define W_REGS(c) c "{v[128:131]}"(w[0]), c "{v[132:135]}"(w[1]), c "{v[136:139]}"(w[2]), c "{v[140:143]}"(w[3])
DEV void land_drain(v4u (&w)[4]) { asm volatile("s_waitcnt vmcnt(0)" : : W_REGS("") : "memory"); }
DEV void land_set(v4u (&w)[4], v4u v) {
  for (int i = 0; i < 4; ++i) w[i] = v;
  asm volatile("" : W_REGS("+") : : "memory");
}
DEV void land_keep(v4u (&w)[4]) { asm volatile("" : : W_REGS("") : "memory"); }
DEV void land_set2(v4u (&w)[4], v4u v) { w[0] = v; w[1] = v; asm volatile("" : "+{v[128:131]}"(w[0]), "+{v[132:135]}"(w[1]) : : "memory"); }
DEV void land_keep2(v4u (&w)[4]) { asm volatile("" : : "{v[128:131]}"(w[0]), "{v[132:135]}"(w[1]) : "memory"); }
struct Watch { unsigned r[4]; };
#define W_STEP(k, sl) "s_waitcnt vmcnt(3)\n\tv_and_b32 %[r" #k "], %[mk], %[r" #k "]\n\tv_cmp_ne_u32 vcc, %[tg], %[r" #k "]\n\ts_nop 3\n\ts_bcnt1_i32_b64 %[bc], vcc\n\ts_cmp_le_u32 %[bc], %[th]\n\ts_cbranch_scc1 9f\n\t" \
  "buffer_load_dword %[r" #k "], %[vo], %[rs], %[so] offen sc1\n\ts_sleep " GX(sl) "\n\t"
#define W_INIT(k, sl) "buffer_load_dword %[r" #k "], %[vo], %[rs], %[so] offen sc1\n\ts_sleep " GX(sl) "\n\t"
#define WATCH(name, sl) \
DEV Watch name(rsrc_t r, unsigned vo, unsigned so, unsigned tag, unsigned mask, u64 lanes, unsigned thr) { \
  Watch w; unsigned cn = POLL_MAX, bc; \
  asm volatile("s_mov_b64 exec, %[ln]\n\t" W_INIT(0, sl) W_INIT(1, sl) W_INIT(2, sl) W_INIT(3, sl) \
               "0:\n\t" W_STEP(0, sl) W_STEP(1, sl) W_STEP(2, sl) W_STEP(3, sl) \
               "s_sub_u32 %[cn], %[cn], 1\n\ts_cbranch_scc0 0b\n9:\n\ts_mov_b64 exec, -1\n\ts_nop 1" \
               : [r0] "=&v"(w.r[0]), [r1] "=&v"(w.r[1]), [r2] "=&v"(w.r[2]), [r3] "=&v"(w.r[3]), [cn] "+&s"(cn), [bc] "=&s"(bc) \
               : [vo] "v"(vo), [rs] "s"(r), [so] "s"(so), [tg] "s"(tag), [mk] "s"(mask), [ln] "s"(lanes), [th] "s"(thr) : "memory", "scc", "vcc"); \
  return w; \
}
WATCH(watch_q, DQS)
WATCH(watch_m, DMS)
DEV void release_watch(Watch& w) { asm volatile("s_waitcnt vmcnt(0)" : : "v"(w.r[0]), "v"(w.r[1]), "v"(w.r[2]), "v"(w.r[3]) : "memory"); }
template <int N, int SL> DEV void gatherp(v4u (&a)[N], rsrc_t r, unsigned vo, unsigned so, unsigned step, unsigned tag) {
#pragma nounroll
  for (int it = 0; it < POLL_MAX; ++it) {
    cfence();
    unsigned bad = 0;
#pragma unroll
    for (int k = 0; k < N; ++k) a[k] = ld_dev(r, vo, so + k * step);
#pragma unroll
    for (int k = 0; k < N; ++k) bad |= (a[k].y ^ tag) | (a[k].w ^ tag);
    if (!__any(bad != 0)) break;
    __builtin_amdgcn_s_sleep(SL);
  }
  cfence();
}

template <int N, int SL, unsigned M> DEV bool gatherf(v4u (&a)[N], rsrc_t r, unsigned vo, unsigned so, unsigned step, v4u tb, int tries = POLL_MAX) {
  bool ok;
#pragma nounroll
  for (int it = 1;; ++it) {
    cfence();
    unsigned bad = 0;
#pragma unroll
    for (int k = 0; k < N; ++k) a[k] = ld_dev(r, vo, so + k * step);
#pragma unroll
    for (int k = 0; k < N; ++k) { a[k] ^= tb; bad |= (a[k].x | a[k].y) | (a[k].z | a[k].w); }
    ok = !__any((bad & M) != 0);
    if (ok || it >= tries) break;
    __builtin_amdgcn_s_sleep(SL);
  }
  cfence();
  return ok;
}

template <int SL, class F> DEV void gather8(v4u (&a)[8], rsrc_t r, unsigned vo, unsigned so, unsigned step, F bad) {
  unsigned need = 255u;
#pragma nounroll
  for (int it = 0; it < POLL_MAX; ++it) {
    cfence();
#pragma unroll
    for (int k = 0; k < 8; ++k) if (need & (1u << k)) a[k] = ld_dev(r, vo + (k & 3) * 1024, so + (k >> 2) * step);
    unsigned nn = 0;
#pragma unroll
    for (int k = 0; k < 8; ++k) nn |= __any(bad(a[k])) ? 1u << k : 0u;
    need = nn;
    if (!need) break;
    __builtin_amdgcn_s_sleep(SL);
  }
  cfence();
}

template <int CTRL> DEV float dpp(float x) { return __builtin_bit_cast(float, __builtin_amdgcn_mov_dpp(__builtin_bit_cast(int, x), CTRL, 0xf, 0xf, true)); }
DEV float x16sum(float x) { auto s = __builtin_amdgcn_permlane16_swap(fbits(x), fbits(x), false, false); return bitsf(s[0]) + bitsf(s[1]); }
DEV float x32sum(float x) { auto s = __builtin_amdgcn_permlane32_swap(fbits(x), fbits(x), false, false); return bitsf(s[0]) + bitsf(s[1]); }
DEV float x16max(float x) { auto s = __builtin_amdgcn_permlane16_swap(fbits(x), fbits(x), false, false); return fmaxf(bitsf(s[0]), bitsf(s[1])); }
DEV float x32max(float x) { auto s = __builtin_amdgcn_permlane32_swap(fbits(x), fbits(x), false, false); return fmaxf(bitsf(s[0]), bitsf(s[1])); }
DEV float wave_sum(float x) {
  x += dpp<0xB1>(x); x += dpp<0x4E>(x); x += dpp<0x141>(x); x += dpp<0x140>(x);
  return x32sum(x16sum(x));
}
DEV float s32(float a, float b) { auto s = __builtin_amdgcn_permlane32_swap(fbits(a), fbits(b), false, false); return bitsf(s[0]) + bitsf(s[1]); }
DEV float s16(float a, float b) { auto s = __builtin_amdgcn_permlane16_swap(fbits(a), fbits(b), false, false); return bitsf(s[0]) + bitsf(s[1]); }
DEV float sum8up(float x) { x += dpp<0x128>(x); return x32sum(x16sum(x)); }
DEV float max8up(float x) { x = fmaxf(x, dpp<0x128>(x)); return x32max(x16max(x)); }
DEV v4u dup_lo(v4u v) { v4u o; for (int i = 0; i < 4; ++i) o[i] = __builtin_amdgcn_update_dpp(v[i], v[i], 0x128, 0xf, 0xc, false); return o; }
DEV v4u dup_hi(v4u v) { v4u o; for (int i = 0; i < 4; ++i) o[i] = __builtin_amdgcn_update_dpp(v[i], v[i], 0x128, 0xf, 0x3, false); return o; }
DEV float fdiv(float a, float d, float rd) { float q = a * rd; return fmaf(fmaf(-q, d, a), rd, q); }
DEV float dot2(unsigned a, unsigned b, float c) { return __builtin_amdgcn_fdot2_f32_bf16(__builtin_bit_cast(v2bf, a), __builtin_bit_cast(v2bf, b), c, false); }
struct Stat { float mb, sd, rd; };
DEV Stat mk_stat(float m, float var) {
  Stat st;
  st.mb = rbf(m);
  st.sd = rbf(__builtin_amdgcn_sqrtf(rbf(rbf(var) + 1e-5f)));
  st.rd = __builtin_amdgcn_rcpf(st.sd);
  return st;
}
DEV Stat ln_stat8(const unsigned (&w)[8]) {
  float s = 0.f, s2 = 0.f, q = 0.f, q2 = 0.f;
#pragma unroll
  for (int i = 0; i < 8; i += 2) {
    s = dot2(w[i], 0x3f803f80u, s); q = dot2(w[i], w[i], q);
    s2 = dot2(w[i + 1], 0x3f803f80u, s2); q2 = dot2(w[i + 1], w[i + 1], q2);
  }
  float t = s32(s + s2, q + q2);
  t += dpp<0xB1>(t); t += dpp<0x4E>(t); t += dpp<0x141>(t); t += dpp<0x140>(t);
  t = x16sum(t) * (1.f / 1024.f);
  auto mq = __builtin_amdgcn_permlane32_swap(fbits(t), fbits(t), false, false);
  const float m = bitsf(mq[0]);
  float var = fmaf(-m, m, bitsf(mq[1]));
  if (__builtin_amdgcn_ballot_w64(!(m * m <= 16.f * var)) != 0) {
    q = 0.f;
#pragma unroll
    for (int i = 0; i < 8; ++i) { float a = bflo(w[i]) - m, b = bfhi(w[i]) - m; q = fmaf(a, a, q); q = fmaf(b, b, q); }
    var = wave_sum(q) * (1.f / 1024.f);
  }
  return mk_stat(m, var);
}
DEV unsigned ln_pair(unsigned x, const Stat& st) {
  unsigned d = pk(bflo(x) - st.mb, bfhi(x) - st.mb);
  return pk(bflo(d) * st.rd, bfhi(d) * st.rd);
}
DEV unsigned onep(unsigned c) { return pk(1.f + bflo(c), 1.f + bfhi(c)); }
DEV unsigned mod_pair(unsigned ln, unsigned c1, unsigned s) {
  unsigned p = pk(bflo(ln) * bflo(c1), bfhi(ln) * bfhi(c1));
  return pk(bflo(p) + bflo(s), bfhi(p) + bfhi(s));
}
DEV unsigned sopq(unsigned a) { asm("" : "+s"(a)); return a; }
DEV float fopq(float a) { asm volatile("" : "+v"(a)); return a; }
DEV unsigned res1(float x, unsigned g, float a, float zero) {
  const float p = dot2(pk(a, a), g, zero);
  const float r = dot2(pk(p, p), sopq(0x3f80u), x);
  return pk(r, r);
}
DEV float silu1(float v) {
  const float d = 1.f + __expf(-v);
  return __builtin_amdgcn_div_fixupf(fdiv(v, d, __builtin_amdgcn_rcpf(d)), d, v);
}

#define STAMP(k)
#define STAMP_DUMP()

#define FIN() do { STAMP_DUMP(); __builtin_amdgcn_endpgm(); } while (0)
DEV float unit_reduce(v4f a) {
  const float r = s16(s32(a[0], a[2]), s32(a[1], a[3]));
  return r + dpp<0x128>(r);
}
DEV void wg_barrier() { asm volatile("s_waitcnt lgkmcnt(0)\n\ts_barrier" ::: "memory"); }

typedef u64 v4l __attribute__((ext_vector_type(4)));
#define CPF 1
__global__ void __launch_bounds__(NTHR, 3)
fused_kernel(const void*, const void*, const void*, const void*, const void*, const void*, const void*, const void*, const void*, const void*,
             void*, void*) {
  const int lane = threadIdx.x & 63;
  const int wave = __builtin_amdgcn_readfirstlane(threadIdx.x >> 6);
  const int wg = blockIdx.x;
  const int n = lane & 15, g = lane >> 4;
  v4l a0, a1, a2;
  unsigned cpf;
  asm volatile("s_load_dwordx8 %0, %3, 0x0\n\ts_load_dwordx8 %1, %3, 0x20\n\ts_load_dwordx8 %2, %3, 0x40"
               : "=&s"(a0), "=&s"(a1), "=&s"(a2) : "s"(__builtin_amdgcn_kernarg_segment_ptr()));
  asm volatile("s_getpc_b64 s[92:93]\n.Lfk_a:\n\ts_and_b32 s93, s93, 0xffff\n\ts_cmp_eq_u32 %3, 0\n\ts_cselect_b32 s94, .Lfk_z-.Lfk_a, 0\n\t"
               "s_mov_b32 s95, 0x20000\n\tbuffer_load_dword %0, %1, s[92:95], %2 offen"
               : "=v"(cpf) : "v"(lane * 128), "s"((wg & 56) * 1024), "s"(CPF ? wave : 1) : "s92", "s93", "s94", "s95", "scc");
  asm volatile("s_waitcnt lgkmcnt(0)" : "+s"(a0), "+s"(a1), "+s"(a2));
  const char *px = (const char*)a0[0], *pk_ = (const char*)a0[1], *pv = (const char*)a0[2], *pwq = (const char*)a0[3];
  const char *pwo = (const char*)a1[0], *pwg = (const char*)a1[1], *pwu = (const char*)a1[2], *pwd = (const char*)a1[3];
  const char *pc = (const char*)a2[0], *pwm = (const char*)a2[1], *pout = (const char*)a2[2], *pws = (const char*)a2[3];
  const rsrc_t rws = mk(pws, WS_BYTES);
  const int bk = wg & 31, sp = wg >> 5;
  const int ab = bk >> 2, kvh = bk & 3;
  const bool first = wg < 32;
  const int xj = wg >> 3, xc = wg & 7;
  const bool rW = xj >= 24, rD = xj >= 2 && xj < 9, rB = xj < 2, rG = xj >= 9 && xj < 23;
  const int wc = xc * 8 + xj - 24, oh = wc >> 1;
  const int gl = rB ? wg : xc * 14 + xj - 9, gks = gl & 1;
  const int dl = xc * 7 + xj - 2, dft = dl >> 2, dj = dl & 3;

  STAMP(0);
  const int tq = (lane >> 2) & 3, tp = lane & 3;
  const bool hi = n >= 8;
  const unsigned sw64 = (((lane & 7) ^ (((lane >> 4) & 1) << 1)) << 4);
  unsigned ep;

  if (wave < 8) {
    const rsrc_t rk = mk((const char*)pk_ + (size_t)bk * 2097152 + sp * 262144 + wave * 32768, 32768);
    const rsrc_t rv = mk((const char*)pv + (size_t)bk * 2097152 + sp * 262144 + wave * 32768, 32768);
    const unsigned rot = wave + wg;
    const unsigned t0 = (unsigned)__builtin_amdgcn_s_memrealtime();
    if (first) {
      asm volatile("s_barrier" ::: "memory");
      lds_wait<8>(F_GO, 4);
    }
    unsigned due = t0 + C0 + (KA0 - 1) * TAU;
    auto pace = [&]() {
      SCHED();
      if ((int)((unsigned)__builtin_amdgcn_s_memrealtime() - due) <= 0) vmwait<WS - 1>();
      vmwait<WB - 1>();
      due += TAU;
      SCHED();
    };
    v4u kf[8][4];
    const unsigned kva = ((n & 3) * 4 + ((n >> 2) & 1)) * 256 + g * 16 + (n >> 3) * 64, kvb = kva ^ 64;
#pragma unroll
    for (int i = 0; i < 32; ++i) {
      const int o = i >> 2, t = (o & 4) | ((o & 1) << 1) | ((o >> 1) & 1);
      if (i == WS) {
        if (!first) asm volatile("s_barrier" ::: "memory");
        asm volatile("s_load_dword %0, %1, 0x0" : "=s"(ep) : "s"(pws) : "memory");
      }
      if (i >= KA0) pace(); else if (i >= WS) vmwait<WS - 1>();
      LD_NT(kf[t][i & 3], rk, (t & 2) ? kvb : kva, ((t + rot) & 7) * 4096, ((i >> 1) & 1) * 512 + (i & 1) * 128);
    }
    asm volatile("" : : "v"(cpf));
    STAMP(1);
    const unsigned vvo = lane * 16;
    lds_wait<4>(F_HEAD, 4);
#pragma nounroll
    for (int i = 0; i < 16; ++i) { pace(); dma16(rv, vvo, (((i >> 2) + rot) & 7) * 4096 + (i & 3) * 1024, i * 8192 + wave * 1024); }
    STAMP(3);
    lds_wait<2>(F_Q, 1);
    asm volatile("s_waitcnt vmcnt(16) lgkmcnt(0)" : "+s"(ep) : : "memory");
#pragma unroll
    for (int t = 0; t < 8; t += 2) TIE8(kf[t][0], kf[t][1], kf[t][2], kf[t][3], kf[t + 1][0], kf[t + 1][1], kf[t + 1][2], kf[t + 1][3]);
    const v4u tb = tagbytes(ep + 0x01010101u);
    const unsigned tg = opq(ep + 0x01010101u);
    STAMP(2);
    v4u qf[2];
    {
      const unsigned qa = opq(L_QB + (n & 7) * 288 + (n >> 3) * 64 + g * 16);
      qf[0] = lds_ld<v4u>(qa); qf[1] = lds_ld<v4u>(qa + 128);
    }
    v4u vf[4][8];
    auto vload = [&](int i) {
      pace();
      LD_NT(vf[2 + (i >> 3)][i & 7], rv, vvo, ((4 + (i >> 2) + rot) & 7) * 4096, (i & 3) * 1024);
      SCHED();
    };
    unsigned sc[4][2];
    float mx = -INFINITY;
    const bool va = (g >> 1) == (n >> 3);
#pragma unroll
    for (int q = 0; q < 4; ++q) {
      const int ta = (q >> 1) * 4 + (q & 1), tbb = ta + 2;
      const v4f z = {0.f, 0.f, 0.f, 0.f};
      v4f a0 = mfma(kf[ta][0], qf[0], z), b0 = mfma(kf[tbb][0], qf[0], z), a1 = mfma(kf[ta][2], qf[0], z), b1 = mfma(kf[tbb][2], qf[0], z);
      a0 = mfma(kf[ta][1], qf[1], a0); b0 = mfma(kf[tbb][1], qf[1], b0); a1 = mfma(kf[ta][3], qf[1], a1); b1 = mfma(kf[tbb][3], qf[1], b1);
      if (q & 1) vload(q >> 1);
      const v4f e0 = sel4(va, a0, b0), e1 = sel4(va, a1, b1);
      float s[4];
#pragma unroll
      for (int r = 0; r < 4; ++r) {
        auto w = __builtin_amdgcn_permlane32_swap(fbits(e0[r]), fbits(e1[r]), false, false);
        s[r] = bitsf(w[0]) + dpp<0x128>(bitsf(w[1]));
      }
#pragma unroll
      for (int r = 0; r < 2; ++r) {
        unsigned u = pk(s[2 * r], s[2 * r + 1]);
        u = pk(bflo(u) * 0.08838834764831845f, bfhi(u) * 0.08838834764831845f);
        sc[q][r] = u;
        mx = fmaxf(mx, fmaxf(bflo(u), bfhi(u)));
      }
    }
    mx = max8up(mx);
    float ls = 0.f;
    v4u pf[4];
#pragma unroll
    for (int b = 0; b < 2; ++b) {
      v4u p;
#pragma unroll
      for (int e = 0; e < 4; ++e) {
        const unsigned u = sc[2 * b + (e >> 1)][e & 1];
        const float p0 = __expf(bflo(u) - mx), p1 = __expf(bfhi(u) - mx);
        ls += p0 + p1;
        p[e] = pk(p0, p1);
      }
      pf[2 * b] = dup_lo(p); pf[2 * b + 1] = dup_hi(p);
    }
    ls = sum8up(ls);
    if (lane < 8) { lds_st<float>(L_MX + wave * 32 + lane * 4, mx); lds_st<float>(L_LS + wave * 32 + lane * 4, ls); }
    lds_signal(F_KML, lane);
    STAMP(4);
    v4f acc[8];
    auto piece = [&](int p, int i, bool first) {
      v4u a;
#pragma unroll
      for (int e = 0; e < 4; ++e) a[e] = __builtin_amdgcn_perm(vf[p][2 * e + 1][i >> 1], vf[p][2 * e][i >> 1], (i & 1) ? 0x07060302u : 0x05040100u);
      acc[i] = mfma(a, pf[p], first ? (v4f){0.f, 0.f, 0.f, 0.f} : acc[i]);
    };
    auto tie = [&](int p) { TIE8(vf[p][0], vf[p][1], vf[p][2], vf[p][3], vf[p][4], vf[p][5], vf[p][6], vf[p][7]); };
    const unsigned wp = rB ? 12288 : rG ? 7168 : 2048;
    const rsrc_t rt = mk(rD ? pwd : rB ? pwm : (gl & 2) ? pwu : pwg, (rD ? 3584 : 1024) * wp);
    const unsigned wsh = rD ? 3 : 4;
    const unsigned wb = rD ? (dft * 256 + wave * 4) * wp + dj * 512 : (gks * 512 + wave * 4) * wp + (rB ? 10240 + (gl >> 1) * 256 : (gl >> 2) * 256);
    unsigned wv = g * wp + ((n ^ (g << 1) ^ ((wave & 2) << 2)) << 4), wm = (1u << wsh) - 1, w32 = 32 * wp;
    asm volatile("" : "+v"(wv), "+s"(wm), "+s"(w32));
    auto wpiece = [&](unsigned f) { dma16(rt, wv, wb + (f & wm) * w32 + (f >> wsh) * 256, f * 8192 + wave * 1024); };
    v4u wf[16];
    const unsigned tw = (8 * g + tq) * 256 + (((2 * wave + (tp >> 1)) ^ ((tq | ((g & 1) << 2)) << 1)) << 4) + 8 * (tp & 1);
    auto wfrag = [&](int f0, int f1) {
      const unsigned b0 = opq(tw), b1 = opq(tw + 65536);
#pragma unroll
      for (int f = f0; f < f1; ++f) wf[f] = trfrag((f & 8 ? b1 : b0) + (f & 7) * 8192, 1024);
    };
    const int hd = n & 7, ci = lane & 31;
    unsigned wa0, wa1, ra[4];
    float fw;
    v4u ml[1];
    const unsigned mlo = (lane >> 5) * 64 + (lane & 3) * 16 + (rW ? 0u : 0x80000000u), mls = rW ? O_ML + oh * 1024 + wave * 128 : 0;
    auto prep = [&]() {
      wa0 = opq(L_MG + ((hd >> 1) | ((g & 1) << 2) | ((n >> 3) << 3) | ((g >> 1) << 4)) * 256 + ((wave ^ ((hd >> 1) | ((n >> 3) << 2))) << 5) + (hd & 1) * 16);
      wa1 = opq(wa0 ^ 16);
      const unsigned r0 = L_MG + ((wave >> 1) | (((ci >> 3) & 1) << 2) | (((ci >> 2) & 1) << 3) | ((ci >> 4) << 4) | ((ci & 3) << 5)) * 256 +
                          ((((ci >> 1) & 3) | ((((lane >> 5) ^ (ci >> 3)) & 1) << 2)) << 5) + ((wave ^ ci) & 1) * 16;
#pragma unroll
      for (int k = 0; k < 4; ++k) ra[k] = opq(r0 ^ (k << 5));
      lds_wait<1>(F_WT, 8);
      fw = __expf(mx - max8up(lds_ld<float>(opq(L_MX + lane * 4))));
      SCHED();
    };
    {
      const unsigned vl = opq(wave * 1024 + lane * 16), vl2 = opq(65536 + wave * 1024 + lane * 16);
#pragma unroll
      for (int i = 2; i < 16; ++i) {
        vload(i);
        if (i == 3 || i == 9) {
          const int p = i == 9;
          if (p) vmwait<10>(); else vmwait<12>();
#pragma unroll
          for (int j = 0; j < 8; ++j) vf[p][j] = lds_ld<v4u>((p ? vl2 : vl) + j * 8192);
        }
        if (i >= 4 && i < 8) { piece(0, 2 * i - 8, true); piece(0, 2 * i - 7, true); }
        if (i == 10) { tie(1); lds_signal(F_WT, lane); }
        if (i >= 10 && i < 14) { piece(1, 2 * i - 20, false); piece(1, 2 * i - 19, false); }
      }
      asm volatile("buffer_load_dwordx4 %0, %1, %2, %3 offen sc1" : "=v"(ml[0]) : "v"(mlo), "s"(rws), "s"(mls) : "memory");
      SCHED();
      vmwait<9>();
      tie(2);
#pragma unroll
      for (int k = 0; k < 8; ++k) piece(2, k, false);
      SCHED();
      prep();
      if (GB && rG) {
#pragma unroll
        for (int f = 0; f < GB; ++f) wpiece(f);
        vmwait<GB + 1>();
      } else {
        vmwait<1>();
      }
      tie(3);
#pragma unroll
      for (int k = 0; k < 8; ++k) piece(3, k, false);
    }
    STAMP(5);
    {
#pragma unroll
      for (int i = 0; i < 4; ++i) lds_st<v4f>(((i & 1) ? wa1 : wa0) + i * 8192, sel4(hi, acc[i + 4], acc[i]) * fw);
      wg_barrier();
      v4f s = (lds_ld<v4f>(ra[0]) + lds_ld<v4f>(ra[1])) + (lds_ld<v4f>(ra[2]) + lds_ld<v4f>(ra[3]));
#pragma unroll
      for (int i = 0; i < 4; ++i) s[i] = x32sum(s[i]);
      if (lane < 32) st_dev(tagv(s, tg), rws, ci * 16, O_AO + (kvh * 8 + wave) * 32768 + ab * 4096 + sp * 512);
    }
    STAMP(6);
    v4u la[4];
    const v4f z = {0.f, 0.f, 0.f, 0.f};
    if (rW) {
      land_set(la, ~tb);
      {
        const unsigned wa = opq(L_AX + wave * 288 + (lane & 31) * 8 + (lane >> 5) * 4);
        asm volatile("s_waitcnt vmcnt(1)" : "+v"(ml[0]) : : "memory");
        ml[0] ^= tb;
        if (__any((((ml[0].x | ml[0].y) | (ml[0].z | ml[0].w)) & 255u) != 0)) gatherf<1, 4, 255u>(ml, rws, mlo, mls, 0, tb);
        float M = fmaxf(bitsf(ml[0].x), dpp<0xB1>(bitsf(ml[0].x)));
        M = x32max(fmaxf(M, dpp<0x4E>(M)));
        const float e = __expf(bitsf(ml[0].x) - M);
        float L = bitsf(ml[0].y) * e;
        L += dpp<0xB1>(L); L = x32sum(L + dpp<0x4E>(L));
        const float f = e / L;
        float f0 = dpp<0x00>(f), f1 = dpp<0x55>(f), f2 = dpp<0xAA>(f), f3 = dpp<0xFF>(f);
        asm volatile("" : "+v"(f0), "+v"(f1), "+v"(f2), "+v"(f3));
        if (D1) __builtin_amdgcn_s_sleep(D1);
        v4u a[4];
        poll4<TH1>(a, la, rws, lane * 16, O_AO + oh * 32768 + wave * 4096, tb);
        STAMP(7);
        v4f o = (asf4(a[0]) * f0 + asf4(a[1]) * f1) + (asf4(a[2]) * f2 + asf4(a[3]) * f3);
        lds_st<unsigned>(wa, pk(s32(o[0], o[2]), s32(o[1], o[3])));
        wg_barrier();
      }
      if (wave < 4) {
        const int un = wc * 4 + wave;
        const unsigned uo = ((lane >> 2) & 1) * 2048 + (lane & 3) * 8, us = (un >> 6) * 4096 + (un & 63) * 32;
        const unsigned oo = opq(uo + g * 2), xo = opq(uo + (lane >> 3) * 16384);
        auto gate = [&](int k, int tries, bool& ok) {
          v4u a[1];
          ok = gatherf<1, 4, 255u>(a, rws, (lane & (k ? 15 : 31)) * 16, O_GX + un * 1024 + k * 512, 0, tb, tries);
          return pk(unit_reduce(asf4(a[0])) * (k ? 0.25f : 0.5f), 0.f);
        };
        float xe = bflo(__builtin_amdgcn_raw_buffer_load_b16(mk(px, 16384), oo, us, 0));
        bool ok;
        unsigned gt = gate(0, 1, ok);
        land_drain(la);
        land_set(la, ~tb);
        xe = fopq(xe);
        gt = fbits(fopq(bitsf(gt)));
        float zero = fopq(0.f);
        STAMP(8);
        if (D2) __builtin_amdgcn_s_sleep(D2);
        v4u p[4];
        poll4<TH2>(p, la, rws, lane * 16, O_WP + un * 4096, tb);
        STAMP(9);
        float s = unit_reduce((asf4(p[0]) + asf4(p[1])) + (asf4(p[2]) + asf4(p[3])));
        if (!ok) gt = gate(0, POLL_MAX, ok);
        const unsigned x1 = res1(xe, gt, s, zero);
        auto h = __builtin_amdgcn_permlane16_swap(x1, x1, false, false);
        const unsigned x2 = canon(__builtin_amdgcn_perm(h[1], h[0], 0x07060100u));
        h = __builtin_amdgcn_permlane32_swap(x2, x2, false, false);
        st_dev2((v2u){h[0], h[1]}, rws, xo, O_X1 + us);
        STAMP(10);
        __builtin_amdgcn_s_sleep(D5);
        gt = gate(1, 1, ok);
        land_drain(la);
        land_set(la, ~tb);
        xe = fopq(bflo(x1));
        gt = fbits(fopq(bitsf(gt)));
        zero = fopq(0.f);
        const rsrc_t ro = mk(pout, 16384);
        STAMP(11);
        v4u c[2];
        poll2<TH5>(c, la, rws, lane * 16, O_DP + un * 2048, tb);
        STAMP(12);
        s = unit_reduce(asf4(c[0]) + sel4(lane >= 48, z, asf4(c[1])));
        if (!ok) gt = gate(1, POLL_MAX, ok);
        const unsigned o = res1(xe, gt, s, zero);
        if (n < 8) __builtin_amdgcn_raw_buffer_store_b16((unsigned short)o, ro, oo, us, 0);
        if (un == 0 && lane == 0) __builtin_amdgcn_raw_buffer_store_b32(ep + 0x01010101u, rws, 0, WS_EPOCH, 16);
        STAMP(15);
      }
      land_keep(la);
      FIN();
    } else if (xj != 23) {
      asm volatile("s_barrier" ::: "memory");
      if (GT + DT) {
        const unsigned tt = t0 + (rG ? GT : DT);
#pragma nounroll
        for (int i = 0; i < 2000 && (int)((unsigned)__builtin_amdgcn_s_memrealtime() - tt) < 0; ++i) __builtin_amdgcn_s_sleep(2);
      }
      const unsigned px_ = ((n >> 1) & 3) * 65536 + (n & 1) * 64 + g * 16;
      if (rG) {
#pragma nounroll
        for (int f = GB; f < 16; ++f) { vmwait<GW - 1>(); wpiece(f); }
      } else {
        if (first) {
          const rsrc_t rm = mk(pwm, 1024 * 12288);
          const unsigned mv = g * 12288 + ((n ^ (g << 1) ^ ((wave & 2) << 2)) << 4), kq = (wg & 1) * 2 + ((wg >> 1) & 1);
#pragma unroll
          for (int f = 0; f < 8; ++f) dma16(rm, mv, (kq * 256 + f * 32 + wave * 4) * 12288 + 4096 + (wg >> 2) * 256, f * 8192 + wave * 1024);
          wpiece(8); wpiece(9);
          v4u b[8];
#pragma unroll
          for (int kb = 0; kb < 8; ++kb) b[kb] = lds_ld<v4u>(L_AX + (n & 7) * 1056 + g * 16 + (wg & 2) * 264 + kb * 64);
          vmwait<2>();
          asm volatile("s_barrier" ::: "memory");
          wfrag(0, 8);
          v4f d0 = z, d1 = z;
#pragma unroll
          for (int kb = 0; kb < 8; kb += 2) { d0 = mfma(wf[kb], b[kb], d0); d1 = mfma(wf[kb + 1], b[kb + 1], d1); }
          if (n < 8) st_dev(tagv(d0 + d1, tg), rws, px_, O_GX + ((wg >> 2) * 8 + wave) * 1024 + kq * 128);
          STAMP(8);
          asm volatile("s_barrier" ::: "memory");
        } else if (DD) {
          __builtin_amdgcn_s_sleep(DD);
        }
#pragma nounroll
        for (int f = first ? 2 : 0; f < 16; ++f) { if (!first) vmwait<DW - 1>(); vmwait<FW - 1>(); wpiece(f ^ 8); }
      }
      vmwait<0>();
      asm volatile("s_barrier" ::: "memory");
      STAMP(7);
      if (!rD) {
        wfrag(0, 16);
        cfence();
        const unsigned b3 = opq(L_AX + (n & 7) * 1056 + (n >> 3) * 528 + g * 16);
        const unsigned po = opq(rG ? n * 64 + g * 16 : px_);
        const unsigned pso = rG ? O_GP + (gl >> 3) * 32768 + (((gl >> 2) & 1) * 4 + (wave >> 1)) * 4096 + (gks * 2 + ((gl >> 1) & 1)) * 1024 + (wave & 1) * 512
                                : O_GX + ((gl >> 1) * 8 + wave) * 1024 + (4 + gks) * 128;
        land_set2(la, (v4u){SENT, SENT, SENT, SENT});
        if (rG) {
          const unsigned wa = opq(L_AX + wave * 1056 + lane * 16 + (lane >> 5) * 16), xo = O_X1 + xc * 16384 + wave * 2048;
          const unsigned sa_ = opq(L_SB + wave * 1024 + lane * 16);
          const v4u sbv = lds_ld<v4u>(sa_), cbv = lds_ld<v4u>(sa_ + 8192);
          float sf[8], pf_[8];
          unsigned cm[8];
#pragma unroll
          for (int e = 0; e < 4; ++e) {
            sf[2 * e] = fopq(bflo(sbv[e])); sf[2 * e + 1] = fopq(bfhi(sbv[e]));
            cm[2 * e] = fbits(fopq(bitsf(cbv[e] & 0xffffu))); cm[2 * e + 1] = fbits(fopq(bfhi(cbv[e])));
            pf_[2 * e] = fopq(0.f); pf_[2 * e + 1] = fopq(0.f);
          }
          const unsigned k10 = sopq(0x3f80u), k01 = sopq(0x3f800000u);
          if (D3) __builtin_amdgcn_s_sleep(D3);
          v4u xv[2];
          poll2s<TH3>(xv, la, rws, lane * 16, xo + gks * 1024, xo + 1024 - gks * 1024);
          const unsigned xr[8] = {xv[0].x, xv[0].y, xv[0].z, xv[0].w, xv[1].x, xv[1].y, xv[1].z, xv[1].w};
          STAMP(11);
          const Stat st = ln_stat8(xr);
          v4u h2;
#pragma unroll
          for (int e = 0; e < 4; ++e) h2[e] = pk(bflo(xr[e]) - st.mb, bfhi(xr[e]) - st.mb);
          SCHED();
#pragma unroll
          for (int e = 0; e < 4; ++e) h2[e] = pk(bflo(h2[e]) * st.rd, bfhi(h2[e]) * st.rd);
          SCHED();
#pragma unroll
          for (int e = 0; e < 8; ++e) pf_[e] = dot2(h2[e >> 1], cm[e], pf_[e]);
          SCHED();
#pragma unroll
          for (int e = 0; e < 4; ++e) h2[e] = pk(pf_[2 * e], pf_[2 * e + 1]);
          SCHED();
#pragma unroll
          for (int e = 0; e < 8; ++e) sf[e] = dot2(h2[e >> 1], (e & 1) ? k01 : k10, sf[e]);
          SCHED();
#pragma unroll
          for (int e = 0; e < 4; ++e) h2[e] = pk(sf[2 * e], sf[2 * e + 1]);
          lds_st<v4u>(wa, h2);
          wg_barrier();
        }
        v4u b[8];
#pragma unroll
        for (int kb = 0; kb < 8; ++kb) b[kb] = lds_ld<v4u>(b3 + kb * 64);
        SCHED();
        v4f d0 = z, d1 = z;
#pragma unroll
        for (int kb = 0; kb < 8; ++kb) { d0 = mfma(wf[kb], b[kb], d0); d1 = mfma(wf[8 + kb], b[kb], d1); }
#pragma unroll
        for (int i = 0; i < 4; ++i) d0[i] += dpp<0x128>(d1[i]);
        if (n < 8) st_dev(tagv(d0, tg), rws, po, pso);
        STAMP(12);
        land_keep2(la);
        FIN();
      } else {
        cfence();
        wfrag(0, 16);
        const unsigned pd = opq(((n >> 1) & 3) * 131072 + (n >> 3) * 16384 + (n & 1) * 64 + g * 16);
        const unsigned wa = opq(L_AX + ((lane >> 2) & 7) * 544 + wave * 64 + (wave >> 2) * 16 + (lane >> 5) * 32 + (lane & 3) * 8);
        const unsigned bb = opq(L_AX + (n & 7) * 544 + (n >> 3) * 272 + g * 16);
        land_set(la, ~tb);
        float hf[4];
#pragma unroll
        for (int e = 0; e < 4; ++e) hf[e] = fopq(0.f);
        if (D4) __builtin_amdgcn_s_sleep(D4);
        v4u a[4];
        poll4<TH4>(a, la, rws, lane * 16, O_GP + dft * 32768 + wave * 4096, tb);
        STAMP(13);
        const v4f tgt = asf4(a[0]) + asf4(a[2]), tu = asf4(a[1]) + asf4(a[3]);
#pragma unroll
        for (int e = 0; e < 4; ++e) {
          const float r = __builtin_elementwise_maximum(tgt[e], 0.f);
          hf[e] = dot2((e & 1) ? pk(0.f, r) : pk(r, 0.f), pk(tu[e & 2], tu[e | 1]), hf[e]);
        }
        lds_st<v2u>(wa, (v2u){pk(hf[0], hf[1]), pk(hf[2], hf[3])});
        wg_barrier();
        v4u b[4];
#pragma unroll
        for (int kb = 0; kb < 4; ++kb) b[kb] = lds_ld<v4u>(bb + kb * 64);
        SCHED();
        v4f d[4];
#pragma unroll
        for (int kb = 0; kb < 4; ++kb)
#pragma unroll
          for (int t = 0; t < 4; ++t) d[t] = mfma(wf[4 * t + kb], b[kb], kb ? d[t] : z);
        const v4f x = sel4(hi, d[1], d[2]);
        v4f y = sel4(hi, d[3], d[0]);
#pragma unroll
        for (int i = 0; i < 4; ++i) y[i] += dpp<0x128>(x[i]);
        st_dev(tagv(y, tg), rws, pd, O_DP + (dj * 16 + wave) * 2048 + dft * 128);
        STAMP(14);
        land_keep(la);
        FIN();
      }
    }
    FIN();
  }

  const int cw = wave - 8;
  __builtin_amdgcn_s_setprio(2);
  unsigned cb_n = 0;
  const bool jq = !first && wg < 160;
  const int ks = wg & 1, jt = (wg - 32) >> 1;
  if (cw == 0 && lane < 32) lds_st<unsigned>(L_FL + 4 * lane, 0u);
  const unsigned h0 = (unsigned)__builtin_amdgcn_s_memrealtime();
  auto later = [&]() {
#pragma nounroll
    for (int i = 0; i < 1000 && (int)((unsigned)__builtin_amdgcn_s_memrealtime() - h0 - SD) < 0; ++i) __builtin_amdgcn_s_sleep(4);
  };
  auto early = [&]() { return (int)((unsigned)__builtin_amdgcn_s_memrealtime() - h0 - QT) < 0; };
  auto softmax_stats = [&](unsigned TAG) {
    lds_wait<8>(F_KML, 8);
    const float mw = lds_ld<float>(L_MX + lane * 4), lw = lds_ld<float>(L_LS + lane * 4);
    const float ms = max8up(mw);
    const float lsum = sum8up(lw * __expf(mw - ms));
    if (lane < 8) st_dev(tag4((v4f){ms, lsum, ms, lsum}, tagbytes(TAG)), rws, lane * 1024, O_ML + kvh * 8192 + ab * 128 + (sp & 1) * 64 + (sp >> 1) * 16);
    STAMP(9);
  };
  const unsigned qo = O_Q + ab * 32768 + kvh * 8192;
  if (rW) {
    asm volatile("s_waitcnt lgkmcnt(0)\n\ts_barrier\n\ts_load_dword %0, %1, 0x0\n\ts_waitcnt lgkmcnt(0) vmcnt(0)" : "=s"(ep) : "s"(pws), "v"(cpf) : "memory");
    const unsigned TAG = ep + 0x01010101u;
    later();
    const rsrc_t rt = mk(pwo, 4096 * 2048);
    const unsigned wv = g * 16384 + ((n ^ (g << 1) ^ ((cw & 1) << 3)) << 4);
#pragma nounroll
    for (int i = 0; i < 32; ++i) {
      vmwait<WH0 - 1>();
      const unsigned w = 2 * cw + ((i >> 2) & 1);
      dma16(rt, wv, (oh * 128 + w + (i >> 3) * 32) * 2048 + (wc & 1) * 1024 + (i & 3) * 256, (((i & 3) << 2) | (i >> 3)) * 8192 + w * 1024);
    }
    vmwait<0>();
    lds_signal(F_CBAR, lane); lds_wait<1>(F_CBAR, 4);
    v4u wf[2][16];
#pragma unroll
    for (int j = 0; j < 2; ++j) {
      const unsigned tw = (8 * g + tq) * 256 + (((4 * cw + 2 * j + (tp >> 1)) ^ ((tq | ((g & 1) << 2)) << 1)) << 4) + 8 * (tp & 1);
      const unsigned b0 = opq(tw), b1 = opq(tw + 65536);
#pragma unroll
      for (int f = 0; f < 16; ++f) wf[j][f] = trfrag((f & 8 ? b1 : b0) + (f & 7) * 8192, 1024);
    }
#pragma unroll
    for (int f = 0; f < 16; f += 4) TIE8(wf[0][f], wf[0][f + 1], wf[0][f + 2], wf[0][f + 3], wf[1][f], wf[1][f + 1], wf[1][f + 2], wf[1][f + 3]);
    lds_signal(F_HEAD, lane);
    STAMP(6);
    if (cw == 3) {
      const v4u tn = tagbytes(TAG) & 15u;
      if (early()) {
        Watch w = watch_q(rws, (lane & 31) * 256 + (sp & 1) * 128, qo, tn.x, 15u, 0xffffffffull, THW);
        release_watch(w);
      }
      STAMP(7);
#pragma nounroll
      for (int it = 0; it < POLL_MAX; ++it) {
#pragma unroll
        for (int k = 0; k < 8; ++k) dma16<true>(rws, lane * 16, qo + k * 1024, L_RED + k * 1024);
        vmwait<0>();
        unsigned bad = 0;
#pragma unroll
        for (int k = 0; k < 4; ++k) {
          const v4u a = lds_ld<v4u>(L_RED + k * 1024 + lane * 16) ^ tn, b = lds_ld<v4u>(L_RED + 4096 + k * 1024 + lane * 16) ^ tn;
          bad |= ((a.x | a.y) | (a.z | a.w)) | ((b.x | b.y) | (b.z | b.w));
          const v4f t = asf4(a) + asf4(b);
          lds_st<v2u>(L_QB + (2 * k + (lane >> 5)) * 288 + (lane & 31) * 8, (v2u){pk(t[0], t[1]), pk(t[2], t[3])});
          SCHED();
        }
        if (!__any((bad & 15u) != 0)) break;
      }
      lds_signal(F_Q, lane);
      STAMP(8);
      softmax_stats(TAG);
    }
    const unsigned bb = opq(L_AX + (n & 7) * 288 + g * 16), tg = opq(TAG);
    const unsigned po = opq(((n >> 1) & 3) * 262144 + (n >> 3) * 4096 + (n & 1) * 64 + g * 16);
    const unsigned so = sopq(O_WP + ((wc & 1) * 32 + 2 * cw) * 4096 + oh * 128);
    asm volatile("s_barrier\n\ts_barrier" ::: "memory");
    v4u b[4];
#pragma unroll
    for (int kb = 0; kb < 4; ++kb) b[kb] = lds_ld<v4u>(bb + kb * 64);
    SCHED();
    const v4f z = {0.f, 0.f, 0.f, 0.f};
#pragma unroll
    for (int h = 0; h < 2; ++h) {
      v4f d[4];
#pragma unroll
      for (int kb = 0; kb < 4; ++kb)
#pragma unroll
        for (int t = 0; t < 4; ++t) d[t] = mfma(wf[t & 1][8 * h + 4 * (t >> 1) + kb], b[kb], kb ? d[t] : z);
#pragma unroll
      for (int t = 0; t < 2; ++t) st_dev(tagv(sel4(hi, d[2 * t + 1], d[2 * t]), tg), rws, po, so + (2 * h + t) * 32768);
    }
    STAMP(10);
    FIN();
  }
  const bool slow = !first && !jq;
  v4u in[4];
  {
    const rsrc_t ri = mk(jq ? px : pc, 16384);
    const unsigned vo = (n & 7) * 2048 + cw * 256 + (n >> 3) * 64 + g * 16;
    asm volatile("buffer_load_dwordx4 %0, %4, %5, %6 offen\n\tbuffer_load_dwordx4 %1, %4, %5, %6 offen offset:128\n\t"
                 "buffer_load_dwordx4 %2, %4, %5, %7 offen\n\tbuffer_load_dwordx4 %3, %4, %5, %7 offen offset:128"
                 : "=&v"(in[0]), "=&v"(in[1]), "=&v"(in[2]), "=&v"(in[3]) : "v"(vo), "s"(ri), "s"(ks * 1024), "s"(1024 - ks * 1024) : "memory");
  }
  const unsigned rowb = jq ? 8192 : 12288;
  const rsrc_t rh = mk(jq ? pwq : pwm, 1024 * rowb);
  const unsigned v0 = (lane >> 3) * rowb + sw64, v1 = v0 ^ 64;
  const unsigned col0 = jq ? (ks * 512 + cw * 128) * rowb + jt * 128 : ((ks ^ 1) * 512 + cw * 128) * rowb + (first ? wg : wg - 112) * 128;
  if (!slow) {
#pragma unroll
    for (int k = 0; k < 8; ++k) dma16(rh, (k & 1) ? v1 : v0, k * 8 * rowb + col0, cw * 32768 + k * 1024);
  }
  if (first) st_dev((v4u){SENT, SENT, SENT, SENT}, rws, lane * 16, O_X1 + wg * 4096 + cw * 1024);
  asm volatile("s_waitcnt lgkmcnt(0)\n\ts_barrier\n\ts_load_dword %0, %1, 0x0" : "=s"(ep) : "s"(pws) : "memory");
  if (first) {
#pragma unroll
    for (int k = 8; k < 16; ++k) dma16(rh, (k & 1) ? v1 : v0, k * 8 * rowb + col0, cw * 32768 + k * 1024);
    vmwait<17>();
  } else {
    if (slow) vmwait<0>();
    vmwait<8>();
  }
  asm volatile("" : "+v"(in[0]), "+v"(in[1]), "+v"(in[2]), "+v"(in[3]) : "v"(cpf) : "memory");
  STAMP(1);
  if (!jq) {
#pragma unroll
    for (int p = 0; p < 4; ++p) {
#pragma unroll
      for (int e = 0; e < 4; ++e) in[p][e] = pk(silu1(bflo(in[p][e])), silu1(bfhi(in[p][e])));
    }
  } else {
    float s = 0.f;
#pragma unroll
    for (int p = 0; p < 4; ++p)
#pragma unroll
      for (int e = 0; e < 4; ++e) s += bflo(in[p][e]) + bfhi(in[p][e]);
    s = sum8up(s);
    if (lane < 8) lds_st<float>(L_SX + lane * 16 + cw * 4, s);
    lds_signal(F_CBAR, lane); cb_n += 4; lds_wait<1>(F_CBAR, cb_n);
    v4f t = lds_ld<v4f>(L_SX + (n & 7) * 16);
    const float m = ((t[0] + t[1]) + (t[2] + t[3])) * (1.f / 1024.f);
    s = 0.f;
#pragma unroll
    for (int p = 0; p < 4; ++p)
#pragma unroll
      for (int e = 0; e < 4; ++e) { float a = bflo(in[p][e]) - m, b = bfhi(in[p][e]) - m; s = fmaf(a, a, s); s = fmaf(b, b, s); }
    s = sum8up(s);
    if (lane < 8) lds_st<float>(L_SX + 128 + lane * 16 + cw * 4, s);
    lds_signal(F_CBAR, lane); cb_n += 4; lds_wait<1>(F_CBAR, cb_n);
    t = lds_ld<v4f>(L_SX + 128 + (n & 7) * 16);
    const Stat st = mk_stat(m, ((t[0] + t[1]) + (t[2] + t[3])) * (1.f / 1024.f));
#pragma unroll
    for (int p = 0; p < 2; ++p)
#pragma unroll
      for (int e = 0; e < 4; ++e) in[p][e] = ln_pair(in[p][e], st);
  }
  STAMP(2);
  asm volatile("s_waitcnt lgkmcnt(0)" : "+s"(ep) : : "memory");
  const unsigned TAG = ep + 0x01010101u;
  const v4u tn = tagbytes(TAG) & 15u;
  unsigned tba[4];
  {
    const unsigned sw = ((tq >> 1) & 1) | ((g & 1) << 1);
    const unsigned tb0 = cw * 32768 + (8 * g + tq) * 128 + (((tp >> 1) ^ (sw << 1)) << 4) + 8 * (tp & 1);
#pragma unroll
    for (int t = 0; t < 4; ++t) tba[t] = opq(tb0 ^ (t << 5));
  }
  v4u wf[4][4];
  v4f d[4];
  auto zero = [&]() {
#pragma unroll
    for (int t = 0; t < 4; ++t) d[t] = (v4f){0.f, 0.f, 0.f, 0.f};
  };
  auto frag = [&](int kb, unsigned off) {
#pragma unroll
    for (int t = 0; t < 4; ++t) wf[kb][t] = trfrag(tba[t] + off + kb * 4096, 512);
  };
  auto mm = [&](int kb, const v4u& x) {
    const v4u b = (kb & 1) ? dup_hi(x) : dup_lo(x);
#pragma unroll
    for (int t = 0; t < 4; ++t) d[t] = mfma(wf[kb][t], b, d[t]);
  };
  auto reduce = [&](unsigned buf, unsigned rb) {
    const unsigned ra = buf + cw * 2048 + (n >> 3) * 1024 + (n & 7) * 64 + g * 16;
    lds_st<v4f>(ra, sel4(hi, d[2], d[0]));
    lds_st<v4f>(ra + 512, sel4(hi, d[3], d[1]));
    lds_signal(F_CBAR, lane); cb_n += 4; lds_wait<0>(F_CBAR, cb_n);
    rb += buf + (lane >> 5) * 4096;
    v4f s = lds_ld<v4f>(rb) + lds_ld<v4f>(rb + 2048);
#pragma unroll
    for (int i = 0; i < 4; ++i) s[i] = x32sum(s[i]);
    return s;
  };
  if (!jq) {
    if (!first) {
      later();
#pragma nounroll
      for (int k = 0; k < 16; ++k) { vmwait<WH0 - 1>(); dma16(rh, v0 ^ ((k & 1) << 6), k * 8 * rowb + col0, cw * 32768 + k * 1024); }
    }
#pragma unroll
    for (int k = 0; k < 16; ++k) {
      if (!first) vmwait<WH0 - 1>();
      dma16(rh, (k & 1) ? v1 : v0, (ks * 1024 - 512 + k * 8) * rowb + col0, cw * 32768 + 16384 + k * 1024);
    }
    lds_signal(F_GO, lane);
    {
      const unsigned a = L_AX + (n & 7) * 1056 + cw * 256 + (cw >> 1) * 16 + (n >> 3) * 64 + g * 16;
      lds_st<v4u>(a, in[0]); lds_st<v4u>(a + 128, in[1]);
    }
    zero();
    vmwait<16>();
#pragma unroll
    for (int kb = 0; kb < 4; ++kb) frag(kb, 0);
#pragma unroll
    for (int kb = 0; kb < 4; ++kb) {
      mm(kb, in[2 + (kb >> 1)]);
      SCHED();
      if (kb == 0) vmwait<12>(); if (kb == 1) vmwait<8>(); if (kb == 2) vmwait<4>(); if (kb == 3) vmwait<0>();
      frag(kb, 16384);
      if (kb) mm(kb - 1, in[(kb - 1) >> 1]);
      SCHED();
    }
    mm(3, in[1]);
    lds_signal(F_HEAD, lane);
    const v4f s = reduce(L_RED, cw * 512 + (lane & 31) * 16);
    st_dev((v4u){pk(s[0], s[1]), TAG, pk(s[2], s[3]), TAG}, rws,
           first ? (lane >> 5) * 65536 + (lane & 1) * 1024 + ((lane >> 1) & 1) * 256 + ((lane >> 2) & 7) * 16 : ((lane >> 2) & 7) * 8192 + (lane & 3) * 16,
           first ? O_SC + wg * 2048 + (cw & 1) * 512 + (cw >> 1) * 128 : O_MOD + (wg - 160) * 256 + cw * 64);
    STAMP(11);
  } else {
#pragma unroll
    for (int k = 8; k < 16; ++k) {
      vmwait<WH1 - 1>();
      dma16(rh, (k & 1) ? v1 : v0, k * 8 * rowb + col0, cw * 32768 + k * 1024);
    }
    vmwait<12>(); frag(0, 0);
    vmwait<8>(); frag(1, 0);
    vmwait<4>(); frag(2, 0);
    vmwait<0>(); frag(3, 0);
    asm volatile("s_waitcnt lgkmcnt(0)" ::: "memory");
    lds_signal(F_HEAD, lane);
    STAMP(3);
    {
      const unsigned so = O_SC + (jt & 1) * 65536 + ks * 16384 + cw * 4096;
      Watch w = watch_m(rws, ((lane & 2) ? 32768 : 0) + (lane & 1) * 2048 + 4, so, TAG, ~0u, 0xfull, THM);
      STAMP(4);
      v4u a[8];
      gather8<DMR>(a, rws, lane * 16, so, 32768, [&](v4u x) { return ((x.y ^ TAG) | (x.w ^ TAG)) != 0; });
      release_watch(w);
      STAMP(5);
#pragma unroll
      for (int p = 0; p < 2; ++p)
#pragma unroll
        for (int e = 0; e < 4; ++e) in[p][e] = mod_pair(in[p][e], onep(a[4 + 2 * p + (e >> 1)][(e & 1) * 2]), a[2 * p + (e >> 1)][(e & 1) * 2]);
    }
    zero();
#pragma unroll
    for (int kb = 0; kb < 4; ++kb) mm(kb, in[kb >> 1]);
    const unsigned r1 = (lane >> 4) & 1;
    v4f s = reduce(L_RED + 8192, (n >> 2) * 512 + (2 * cw + r1) * 64 + (n & 3) * 16);
    if (lane < 32) st_dev(tag4<15u>(s, tn), rws, r1 * 32768 + n * 16, O_Q + cw * 65536 + (jt >> 4) * 8192 + ks * 4096 + (jt & 15) * 256);
  }
  STAMP(6);
  lds_signal(F_CBAR, lane); cb_n += 4;
  if (cw == 3) {
    if (first) __builtin_amdgcn_s_sleep(DQ0);
    Watch w = {};
    if (!slow || early()) w = watch_q(rws, (lane & 31) * 256 + (sp & 1) * 128, qo, tn.x, 15u, 0xffffffffull, THQ);
    STAMP(7);
    v4u a[8];
    gather8<DMR>(a, rws, lane * 16, qo, 4096, [&](v4u x) { x ^= tn; return (((x.x | x.y) | (x.z | x.w)) & 15u) != 0; });
    release_watch(w);
#pragma unroll
    for (int k = 0; k < 4; ++k) {
      const v4f t = asf4(a[k] ^ tn) + asf4(a[4 + k] ^ tn);
      lds_st<v2u>(L_QB + (2 * k + (lane >> 5)) * 288 + (lane & 31) * 8, (v2u){pk(t[0], t[1]), pk(t[2], t[3])});
    }
    lds_signal(F_Q, lane);
    STAMP(8);
    softmax_stats(TAG);
  } else {
    if (rG && cw < 2) {
      lds_wait<32>(F_Q, 1);
      lds_wait<8>(F_CBAR, cb_n);
#pragma nounroll
      for (int h = 0; h < 2; ++h) {
        v4u a[8];
        gatherp<8, 32>(a, rws, lane * 16, O_MOD + cw * 4096 + gks * 2048 + h * 1024, 8192, TAG);
#pragma unroll
        for (int k = 0; k < 8; ++k)
          lds_st<v2u>(L_SB + cw * 8192 + k * 1024 + h * 512 + lane * 8, cw ? (v2u){onep(a[k].x), onep(a[k].z)} : (v2u){a[k].x, a[k].z});
      }
    }
    STAMP(8);
  }
  STAMP_DUMP();
  asm volatile("s_waitcnt vmcnt(0) lgkmcnt(0)" ::: "memory");
}

__global__ void code_end_mark() { asm volatile(".Lfk_z:"); }

extern "C" void kernel_launch(void* const* d_in, const int* in_sizes, int n_in, void* d_out, int out_size, void* d_ws, size_t ws_size,
                              hipStream_t stream) {
  (void)in_sizes; (void)n_in; (void)out_size; (void)ws_size;
  (void)hipFuncSetAttribute((const void*)fused_kernel, hipFuncAttributeMaxDynamicSharedMemorySize, L_END);
  fused_kernel<<<NWG, NTHR, L_END, stream>>>(d_in[0], d_in[1], d_in[2], d_in[3], d_in[4], d_in[6], d_in[7], d_in[8], d_in[9], d_in[10],
                                             d_out, d_ws);
}
```

```cpp
#include <hip/hip_runtime.h>
#include <stdint.h>

typedef unsigned v4u __attribute__((ext_vector_type(4)));
typedef unsigned v2u __attribute__((ext_vector_type(2)));
typedef float v4f __attribute__((ext_vector_type(4)));
typedef float v2f __attribute__((ext_vector_type(2)));
typedef __bf16 v2bf __attribute__((ext_vector_type(2)));
typedef __bf16 v8bf __attribute__((ext_vector_type(8)));
typedef short v4s __attribute__((ext_vector_type(4)));
#define LAS __attribute__((address_space(3)))
typedef __amdgpu_buffer_rsrc_t rsrc_t;
#define DEV __device__ __forceinline__

#define WS 2
#define WB 5
#define TAU 33
#define C0 330
#define KA0 8
#define WH1 8
#define WH0 8
#define DQ0 40
#define DQS 16
#define DMS 6
#define THM 2
#define THQ 8
#define DMR 2
#define D1 8
#define D2 24
#define D3 0
#define D4 0
#define D5 60
#define TH1 32
#define PB 2
#define PW 3
#define TL 40
#define PBT0 1
#define PBT 1
#define TH2 128
#define TH3 32
#define TH4 64
#define TH5 128
#define PSL 5
#define GB 6
#define GW 8
#define DW 3
#define GT 0
#define DT 0
#define QT 1350
#define THW 2
#define FW 8
#define SD 300
#define G5 POLL_MAX
#define GAP 1
#define CF 60
#define MAP 0
#define PL 0
#define PL2 (2 * PL)
#define WX 16
#define LX 120
#define ROT 0
#define GA 2
#define PX 0
#define DTS 4
#define WL2 0
#define WT2 0
#define WL3 2
#define WT3 1
#define WL4 2
#define WT4 1
#define WL5 0
#define WT5 0
#define DD 0

constexpr int NWG = 256, NTHR = 768;
constexpr unsigned SENT = 0xFFFFFFFFu;
constexpr unsigned WS_EPOCH = 0;
constexpr unsigned O_RF = 32768;
constexpr unsigned O_SC = 65536;
constexpr unsigned O_MOD = O_SC + 131072;
constexpr unsigned O_Q = O_MOD + 65536;
constexpr unsigned O_GX = O_Q + 262144;
constexpr unsigned O_ML = O_GX + 524288;
constexpr unsigned O_X1 = O_ML + 32768;
constexpr unsigned O_WP = O_X1 + 131072;
constexpr unsigned O_AO = O_WP + 524288;
constexpr unsigned O_GP = O_AO + 524288;
constexpr unsigned O_DP = O_GP + 14 * 32768;
constexpr unsigned O_STAMP = 65536 + 256 * 16384 + 1048576;
static_assert(O_DP + 524288 <= O_STAMP, "ws map");
constexpr unsigned WS_BYTES = O_STAMP + NWG * 12 * 16 * 8;
constexpr unsigned L_MG = 98304;
constexpr unsigned L_M = 131072;
constexpr unsigned L_RED = L_M;
constexpr unsigned L_SB = L_RED;
constexpr unsigned L_AX = L_RED + 16384;
constexpr unsigned L_QB = L_AX + 8448;
constexpr unsigned L_MX = L_QB + 2304;
constexpr unsigned L_LS = L_MX + 256;
constexpr unsigned L_SX = L_LS + 256;
constexpr unsigned L_FL = L_SX + 256;
constexpr unsigned L_ST = L_FL + 128;
constexpr unsigned L_END = L_ST + 12 * 16 * 8;
static_assert(L_END <= 163840, "lds");
static_assert(GB <= 8, "windows");
enum { F_CBAR, F_Q, F_KML, F_GO, F_HEAD, F_WT };

DEV float bflo(unsigned u) { return __builtin_bit_cast(float, u << 16); }
DEV float bfhi(unsigned u) { return __builtin_bit_cast(float, u & 0xffff0000u); }
DEV unsigned pk(float a, float b) {
  v2f f = {a, b};
  unsigned u = __builtin_bit_cast(unsigned, __builtin_convertvector(f, v2bf));
  asm("" : "+v"(u));
  return u;
}
DEV float rbf(float a) { return bflo(pk(a, a)); }
DEV unsigned fbits(float a) { return __builtin_bit_cast(unsigned, a); }
DEV float bitsf(unsigned a) { return __builtin_bit_cast(float, a); }
DEV unsigned canon(unsigned a) { return a < 0xFFFFFFFEu ? a : 0xFFFFFFFEu; }
DEV v4f sel4(bool c, v4f a, v4f b) { return (v4f){c ? a.x : b.x, c ? a.y : b.y, c ? a.z : b.z, c ? a.w : b.w}; }
DEV v4f asf4(v4u v) { return __builtin_bit_cast(v4f, v); }
DEV unsigned opq(unsigned a) { asm("" : "+v"(a)); return a; }
DEV v4u tagbytes(unsigned T) { return (v4u){T & 255u, (T >> 8) & 255u, (T >> 16) & 255u, T >> 24}; }
template <unsigned M = 255u> DEV v4u tag4(v4f v, v4u tb) {
  v4u o;
  for (int i = 0; i < 4; ++i) o[i] = ((fbits(v[i]) + (M + 1) / 2) & ~M) | tb[i];
  return o;
}

DEV v4u tagv(v4f v, unsigned tg) {
  v4u o;
  for (int i = 0; i < 4; ++i) o[i] = __builtin_amdgcn_perm(fbits(v[i]), tg, 0x07060500u + i);
  return o;
}

template <class T> DEV T lds_ld(unsigned a) { return *(LAS T*)(size_t)a; }
template <class T> DEV void lds_st(unsigned a, T v) { *(LAS T*)(size_t)a = v; }
DEV void cfence() { asm volatile("" ::: "memory"); }
#define SCHED() __builtin_amdgcn_sched_barrier(0)
DEV void lds_signal(int f, int lane) {
  cfence();
  if (lane == 0) __hip_atomic_fetch_add((LAS unsigned*)(size_t)(L_FL + 4 * f), 1u, __ATOMIC_RELAXED, __HIP_MEMORY_SCOPE_WORKGROUP);
  cfence();
}
DEV unsigned lds_flag(int f) { return __builtin_amdgcn_readfirstlane(*(volatile LAS unsigned*)(size_t)(L_FL + 4 * f)); }
template <int SLEEP> DEV void lds_wait(int f, unsigned target) {
  cfence();
#pragma nounroll
  for (int i = 0; i < 4000000; ++i) {
    if (lds_flag(f) >= target) break;
    if (SLEEP) __builtin_amdgcn_s_sleep(SLEEP);
  }
  cfence();
}
constexpr int POLL_MAX = 200000;

DEV rsrc_t mk(const void* p, unsigned bytes) { return __builtin_amdgcn_make_buffer_rsrc((void*)p, 0, bytes, 0x00020000); }
DEV v4u ld_nt(rsrc_t r, unsigned v, unsigned s) { return __builtin_amdgcn_raw_buffer_load_b128(r, v, s, 2); }
DEV v4u ld_dev(rsrc_t r, unsigned v, unsigned s) { return __builtin_amdgcn_raw_buffer_load_b128(r, v, s, 16); }
DEV void st_dev(v4u d, rsrc_t r, unsigned v, unsigned s) {
  asm volatile("s_nop 0\n\tbuffer_store_dwordx4 %0, %1, %2, %3 offen sc1\n\ts_nop 1" : : "v"(d), "v"(v), "s"(r), "s"(s) : "memory");
}
DEV void st_dev2(v2u d, rsrc_t r, unsigned v, unsigned s) {
  asm volatile("s_nop 0\n\tbuffer_store_dwordx2 %0, %1, %2, %3 offen sc1" : : "v"(d), "v"(v), "s"(r), "s"(s) : "memory");
}
template <bool DEVSCOPE = false> DEV void dma16(rsrc_t r, unsigned v, unsigned s, unsigned lds) {
  unsigned keep;
  if (DEVSCOPE)
    asm volatile("s_mov_b32 %0, m0\n\ts_mov_b32 m0, %4\n\ts_nop 0\n\tbuffer_load_dwordx4 %1, %2, %3 offen sc1 lds\n\ts_mov_b32 m0, %0"
                 : "=&s"(keep) : "v"(v), "s"(r), "s"(s), "s"(lds) : "memory");
  else
  asm volatile("s_mov_b32 %0, m0\n\ts_mov_b32 m0, %4\n\ts_nop 0\n\tbuffer_load_dwordx4 %1, %2, %3 offen nt lds\n\ts_mov_b32 m0, %0"
               : "=&s"(keep) : "v"(v), "s"(r), "s"(s), "s"(lds) : "memory");
}
#define LD_NT(d, r, v, s, off) asm volatile("buffer_load_dwordx4 %0, %1, %2, %3 offen offset:%4 nt" : "=v"(d) : "v"(v), "s"(r), "s"(s), "n"(off) : "memory")
#define TIE8(a, b, c, d, e, f, g, h) asm volatile("" : "+v"(a), "+v"(b), "+v"(c), "+v"(d), "+v"(e), "+v"(f), "+v"(g), "+v"(h) : : "memory")
template <int N> DEV void vmwait() { asm volatile("s_waitcnt vmcnt(%0)" ::"n"(N) : "memory"); }
DEV v8bf asbf(v4u v) { return __builtin_bit_cast(v8bf, v); }
DEV v4f mfma(v4u a, v4u b, v4f c) { return __builtin_amdgcn_mfma_f32_16x16x32_bf16(asbf(a), asbf(b), c, 0, 0, 0); }
DEV v2u trrd(unsigned a) { return __builtin_bit_cast(v2u, __builtin_amdgcn_ds_read_tr16_b64_v4i16((LAS v4s*)(size_t)a)); }
DEV v4u trfrag(unsigned a, unsigned step) { v2u x = trrd(a), y = trrd(a + step); return (v4u){x.x, x.y, y.x, y.y}; }

typedef unsigned long long u64;
#define GS(x) #x
#define GX(x) GS(x)
#define P_LD(k, a, d, so, off) "s_mov_b64 exec, %[m" #k "]\n\ts_cbranch_execz 1" #k "f\n\tbuffer_load_dwordx4 v[" #a ":" #d "], %[vo], %[rs], %[" #so "] offen offset:" #off " sc1\n1" #k ":\n\t"
#define P_CK(k, a, b, c, d, e, f, g, h) "s_mov_b64 exec, %[m" #k "]\n\t" \
  "v_xor_b32 v" #e ", %[t0], v" #a "\n\tv_xor_b32 v" #f ", %[t1], v" #b "\n\tv_xor_b32 v" #g ", %[t2], v" #c "\n\tv_xor_b32 v" #h ", %[t3], v" #d "\n\t" \
  "v_or3_b32 %[tm], v" #e ", v" #f ", v" #g "\n\tv_bitop3_b32 %[tm], %[tm], v" #h ", %[ff] bitop3:0xa8\n\tv_cmp_ne_u32 %[m" #k "], 0, %[tm]\n\t"
#define P_CS(k, a, b, c, d, e, f, g, h) "s_mov_b64 exec, %[m" #k "]\n\t" \
  "v_mov_b32 v" #e ", v" #a "\n\tv_mov_b32 v" #f ", v" #b "\n\tv_mov_b32 v" #g ", v" #c "\n\tv_mov_b32 v" #h ", v" #d "\n\t" \
  "v_max3_u32 %[tm], v" #e ", v" #f ", v" #g "\n\tv_max_u32 %[tm], %[tm], v" #h "\n\tv_cmp_eq_u32 %[m" #k "], -1, %[tm]\n\t"
#define P_CNT2 "s_bcnt1_i32_b64 %[bc], %[m0]\n\ts_bcnt1_i32_b64 %[b2], %[m1]\n\ts_add_u32 %[bc], %[bc], %[b2]\n\t"
#define P_CNT4 P_CNT2 "s_bcnt1_i32_b64 %[b2], %[m2]\n\ts_add_u32 %[bc], %[bc], %[b2]\n\ts_bcnt1_i32_b64 %[b2], %[m3]\n\ts_add_u32 %[bc], %[bc], %[b2]\n\t"
#define P_WAIT "s_cmpk_le_u32 %[bc], %[th]\n\ts_cbranch_scc1 2f\n\ts_waitcnt vmcnt(0)\n\ts_branch 3f\n2:\n\ts_sleep %[sl]\n3:\n\t"
#define P_OR2 "s_nop 3\n\ts_or_b64 %[ac], %[m0], %[m1]\n\t"
#define P_OR4 P_OR2 "s_or_b64 %[ac], %[ac], %[m2]\n\ts_or_b64 %[ac], %[ac], %[m3]\n\t"
#define P_WQ2 "s_cbranch_scc0 9f\n\ts_wqm_b64 %[m0], %[m0]\n\ts_wqm_b64 %[m1], %[m1]\n\t"
#define P_WQ4 P_WQ2 "s_wqm_b64 %[m2], %[m2]\n\ts_wqm_b64 %[m3], %[m3]\n\t"
#define P_END "s_sub_u32 %[cn], %[cn], 1\n\ts_cbranch_scc0 0b\n9:\n\ts_mov_b64 exec, -1\n\ts_nop 1"
#define P_DECL u64 m[4] = {~0ull, ~0ull, ~0ull, ~0ull}, ac; unsigned tm, cn = POLL_MAX, bc, b2
#define P_O2 "=&{v[112:115]}"(a[0]), "=&{v[116:119]}"(a[1]), "+{v[128:131]}"(w[0]), "+{v[132:135]}"(w[1]), [m0] "+&s"(m[0]), [m1] "+&s"(m[1]), \
  [tm] "=&v"(tm), [ac] "=&s"(ac), [cn] "+&s"(cn), [bc] "=&s"(bc), [b2] "=&s"(b2)
#define P_O4 P_O2, "=&{v[120:123]}"(a[2]), "=&{v[124:127]}"(a[3]), "+{v[136:139]}"(w[2]), "+{v[140:143]}"(w[3]), [m2] "+&s"(m[2]), [m3] "+&s"(m[3])
#define P_IN [vo] "v"(vo), [rs] "s"(r), [so] "s"(so), [th] "n"(TH), [sl] "n"(PSL)
#define P_TB [t0] "s"(tb.x), [t1] "s"(tb.y), [t2] "s"(tb.z), [t3] "s"(tb.w), [ff] "s"(255u)
#define P_C01(C) C(0, 128, 129, 130, 131, 112, 113, 114, 115) C(1, 132, 133, 134, 135, 116, 117, 118, 119)
template <int TH> DEV void poll4(v4u (&a)[4], v4u (&w)[4], rsrc_t r, unsigned vo, unsigned so, v4u tb) {
  P_DECL;
  asm volatile("0:\n\t" P_LD(0, 128, 131, so, 0) P_LD(1, 132, 135, so, 1024) P_LD(2, 136, 139, so, 2048) P_LD(3, 140, 143, so, 3072) P_CNT4 P_WAIT
               P_C01(P_CK) P_CK(2, 136, 137, 138, 139, 120, 121, 122, 123) P_CK(3, 140, 141, 142, 143, 124, 125, 126, 127) P_OR4 P_WQ4 P_END
               : P_O4 : P_IN, P_TB : "memory", "scc");
}
template <int TH> DEV void poll2(v4u (&a)[2], v4u (&w)[4], rsrc_t r, unsigned vo, unsigned so, v4u tb) {
  P_DECL;
  m[1] = 0xffffffffffffull;
  asm volatile("0:\n\t" P_LD(0, 128, 131, so, 0) P_LD(1, 132, 135, so, 1024) P_CNT2 P_WAIT P_C01(P_CK) P_OR2 P_WQ2 P_END
               : P_O2 : P_IN, P_TB : "memory", "scc");
}
template <int TH, bool PRE> DEV void poll2s(v4u (&a)[2], v4u (&w)[4], rsrc_t r, unsigned vo, unsigned so, unsigned s1) {
  P_DECL;
  asm volatile(".if %[pre]\n\ts_waitcnt vmcnt(0)\n\ts_branch 3f\n.endif\n0:\n\t" P_LD(0, 128, 131, so, 0) P_LD(1, 132, 135, s1, 0) P_CNT2 P_WAIT
               P_C01(P_CS) P_OR2 P_WQ2 P_END
               : P_O2 : P_IN, [s1] "s"(s1), [pre] "n"(PRE) : "memory", "scc");
}
DEV void poll2s_first(v4u (&w)[4], rsrc_t r, unsigned vo, unsigned so, unsigned s1) {
  asm volatile("buffer_load_dwordx4 v[128:131], %2, %3, %4 offen sc1\n\tbuffer_load_dwordx4 v[132:135], %2, %3, %5 offen sc1"
               : "=&{v[128:131]}"(w[0]), "=&{v[132:135]}"(w[1]) : "v"(vo), "s"(r), "s"(so), "s"(s1) : "memory");
}
DEV void probe_ld(v4u (&w)[4], rsrc_t r, unsigned vo, unsigned so) {
  asm volatile("s_mov_b64 exec, %5\n\tbuffer_load_dword v128, %2, %3, %4 offen sc1\n\ts_mov_b64 exec, -1"
               : "=&{v[128:131]}"(w[0]), "=&{v[132:135]}"(w[1]) : "v"(vo), "s"(r), "s"(so), "s"((1ull << PB) - 1) : "memory");
}
#define F_LD "buffer_load_dwordx4 v[128:131], %[vo], %[rs], %[so] offen sc1\n\tbuffer_load_dwordx4 v[132:135], %[vo], %[rs], %[s1] offen sc1\n1:"
template <bool AGAIN> DEV void probe_first(v4u (&w)[4], rsrc_t r, unsigned vo, unsigned so, unsigned s1, unsigned pv, unsigned ps, unsigned& go) {
  asm volatile("s_cmp_lg_u32 %[go], 0\n\ts_cbranch_scc1 1f\n\ts_mov_b64 exec, %[ln]\n\tv_cmp_eq_u32 vcc, -1, v128\n\ts_nop 3\n\ts_bcnt1_i32_b64 %[go], vcc\n\t"
               "s_cmp_le_u32 %[go], %[th]\n\ts_cselect_b32 %[go], 1, 0\n\ts_cbranch_scc1 2f\n\t"
               ".if %[ag]\n\tbuffer_load_dword v128, %[pv], %[rs], %[ps] offen sc1\n.endif\n\ts_mov_b64 exec, -1\n\ts_branch 1f\n"
               "2:\n\ts_mov_b64 exec, -1\n\t" F_LD
               : "+{v[128:131]}"(w[0]), "+{v[132:135]}"(w[1]), [go] "+&s"(go)
               : [vo] "v"(vo), [rs] "s"(r), [so] "s"(so), [s1] "s"(s1), [ln] "s"((1ull << PB) - 1), [th] "n"(AGAIN ? PBT0 : PBT), [ag] "n"(AGAIN),
                 [pv] "v"(pv), [ps] "s"(ps) : "memory", "scc", "vcc");
}
DEV void late_first(v4u (&w)[4], rsrc_t r, unsigned vo, unsigned so, unsigned s1, unsigned go) {
  asm volatile("s_cmp_lg_u32 %[go], 0\n\ts_cbranch_scc1 1f\n\t" F_LD
               : "+{v[128:131]}"(w[0]), "+{v[132:135]}"(w[1]) : [vo] "v"(vo), [rs] "s"(r), [so] "s"(so), [s1] "s"(s1), [go] "s"(go) : "memory", "scc");
}
#define W_REGS(c) c "{v[128:131]}"(w[0]), c "{v[132:135]}"(w[1]), c "{v[136:139]}"(w[2]), c "{v[140:143]}"(w[3])
DEV void land_drain(v4u (&w)[4]) { asm volatile("s_waitcnt vmcnt(0)" : : W_REGS("") : "memory"); }
DEV void land_set(v4u (&w)[4], v4u v) {
  for (int i = 0; i < 4; ++i) w[i] = v;
  asm volatile("" : W_REGS("+") : : "memory");
}
DEV void land_keep(v4u (&w)[4]) { asm volatile("" : : W_REGS("") : "memory"); }
DEV void land_set2(v4u (&w)[4], v4u v) { w[0] = v; w[1] = v; asm volatile("" : "+{v[128:131]}"(w[0]), "+{v[132:135]}"(w[1]) : : "memory"); }
DEV void land_keep2(v4u (&w)[4]) { asm volatile("" : : "{v[128:131]}"(w[0]), "{v[132:135]}"(w[1]) : "memory"); }
struct Watch { unsigned r[4]; };
#define W_STEP(k, sl, op) "s_waitcnt vmcnt(3)\n\tv_and_b32 %[r" #k "], %[mk], %[r" #k "]\n\tv_cmp_" op "_u32 vcc, %[tg], %[r" #k "]\n\ts_nop 3\n\ts_bcnt1_i32_b64 %[bc], vcc\n\ts_cmp_le_u32 %[bc], %[th]\n\ts_cbranch_scc1 9f\n\t" \
  "buffer_load_dword %[r" #k "], %[vo], %[rs], %[so] offen sc1\n\ts_sleep " GX(sl) "\n\t"
#define W_INIT(k, sl) "buffer_load_dword %[r" #k "], %[vo], %[rs], %[so] offen sc1\n\ts_sleep " GX(sl) "\n\t"
#define WATCH(name, sl, op) \
DEV Watch name(rsrc_t r, unsigned vo, unsigned so, unsigned tag, unsigned mask, u64 lanes, unsigned thr, unsigned skip = 0) { \
  Watch w; unsigned cn = POLL_MAX, bc; \
  asm volatile("s_cmp_lg_u32 %[sk], 0\n\ts_cbranch_scc1 9f\n\ts_mov_b64 exec, %[ln]\n\t" W_INIT(0, sl) W_INIT(1, sl) W_INIT(2, sl) W_INIT(3, sl) \
               "0:\n\t" W_STEP(0, sl, op) W_STEP(1, sl, op) W_STEP(2, sl, op) W_STEP(3, sl, op) \
               "s_sub_u32 %[cn], %[cn], 1\n\ts_cbranch_scc0 0b\n9:\n\ts_mov_b64 exec, -1\n\ts_nop 1" \
               : [r0] "=&v"(w.r[0]), [r1] "=&v"(w.r[1]), [r2] "=&v"(w.r[2]), [r3] "=&v"(w.r[3]), [cn] "+&s"(cn), [bc] "=&s"(bc) \
               : [vo] "v"(vo), [rs] "s"(r), [so] "s"(so), [tg] "s"(tag), [mk] "s"(mask), [ln] "s"(lanes), [th] "s"(thr), [sk] "s"(skip) : "memory", "scc", "vcc"); \
  return w; \
}
WATCH(watch_q, DQS, "ne")
WATCH(watch_m, DMS, "ne")
WATCH(watch_t, DTS, "ne")
WATCH(watch_x, DTS, "eq")
DEV void keep_watch(Watch& w) { asm volatile("" : : "v"(w.r[0]), "v"(w.r[1]), "v"(w.r[2]), "v"(w.r[3]) : "memory"); }
DEV void release_watch(Watch& w) { asm volatile("s_waitcnt vmcnt(0)" : : "v"(w.r[0]), "v"(w.r[1]), "v"(w.r[2]), "v"(w.r[3]) : "memory"); }
template <int N, int SL> DEV void gatherp(v4u (&a)[N], rsrc_t r, unsigned vo, unsigned so, unsigned step, unsigned tag) {
#pragma nounroll
  for (int it = 0; it < POLL_MAX; ++it) {
    cfence();
    unsigned bad = 0;
#pragma unroll
    for (int k = 0; k < N; ++k) a[k] = ld_dev(r, vo, so + k * step);
#pragma unroll
    for (int k = 0; k < N; ++k) bad |= (a[k].y ^ tag) | (a[k].w ^ tag);
    if (!__any(bad != 0)) break;
    __builtin_amdgcn_s_sleep(SL);
  }
  cfence();
}

template <int N, int SL, unsigned M> DEV bool gatherf(v4u (&a)[N], rsrc_t r, unsigned vo, unsigned so, unsigned step, v4u tb, int tries = POLL_MAX) {
  bool ok;
#pragma nounroll
  for (int it = 1;; ++it) {
    cfence();
    unsigned bad = 0;
#pragma unroll
    for (int k = 0; k < N; ++k) a[k] = ld_dev(r, vo, so + k * step);
#pragma unroll
    for (int k = 0; k < N; ++k) { a[k] ^= tb; bad |= (a[k].x | a[k].y) | (a[k].z | a[k].w); }
    ok = !__any((bad & M) != 0);
    if (ok || it >= tries) break;
    __builtin_amdgcn_s_sleep(SL);
  }
  cfence();
  return ok;
}

template <int SL, class F> DEV void gather8(v4u (&a)[8], rsrc_t r, unsigned vo, unsigned so, unsigned step, F bad) {
  unsigned need = 255u;
#pragma nounroll
  for (int it = 0; it < POLL_MAX; ++it) {
    cfence();
#pragma unroll
    for (int k = 0; k < 8; ++k) if (need & (1u << k)) a[k] = ld_dev(r, vo + (k & 3) * 1024, so + (k >> 2) * step);
    unsigned nn = 0;
#pragma unroll
    for (int k = 0; k < 8; ++k) nn |= __any(bad(a[k])) ? 1u << k : 0u;
    need = nn;
    if (!need) break;
    __builtin_amdgcn_s_sleep(SL);
  }
  cfence();
}

template <int CTRL> DEV float dpp(float x) { return __builtin_bit_cast(float, __builtin_amdgcn_mov_dpp(__builtin_bit_cast(int, x), CTRL, 0xf, 0xf, true)); }
DEV float x16sum(float x) { auto s = __builtin_amdgcn_permlane16_swap(fbits(x), fbits(x), false, false); return bitsf(s[0]) + bitsf(s[1]); }
DEV float x32sum(float x) { auto s = __builtin_amdgcn_permlane32_swap(fbits(x), fbits(x), false, false); return bitsf(s[0]) + bitsf(s[1]); }
DEV float x16max(float x) { auto s = __builtin_amdgcn_permlane16_swap(fbits(x), fbits(x), false, false); return fmaxf(bitsf(s[0]), bitsf(s[1])); }
DEV float x32max(float x) { auto s = __builtin_amdgcn_permlane32_swap(fbits(x), fbits(x), false, false); return fmaxf(bitsf(s[0]), bitsf(s[1])); }
DEV float wave_sum(float x) {
  x += dpp<0xB1>(x); x += dpp<0x4E>(x); x += dpp<0x141>(x); x += dpp<0x140>(x);
  return x32sum(x16sum(x));
}
DEV float s32(float a, float b) { auto s = __builtin_amdgcn_permlane32_swap(fbits(a), fbits(b), false, false); return bitsf(s[0]) + bitsf(s[1]); }
DEV float s16(float a, float b) { auto s = __builtin_amdgcn_permlane16_swap(fbits(a), fbits(b), false, false); return bitsf(s[0]) + bitsf(s[1]); }
DEV float sum8up(float x) { x += dpp<0x128>(x); return x32sum(x16sum(x)); }
DEV float max8up(float x) { x = fmaxf(x, dpp<0x128>(x)); return x32max(x16max(x)); }
DEV v4u dup_lo(v4u v) { v4u o; for (int i = 0; i < 4; ++i) o[i] = __builtin_amdgcn_update_dpp(v[i], v[i], 0x128, 0xf, 0xc, false); return o; }
DEV v4u dup_hi(v4u v) { v4u o; for (int i = 0; i < 4; ++i) o[i] = __builtin_amdgcn_update_dpp(v[i], v[i], 0x128, 0xf, 0x3, false); return o; }
DEV float fdiv(float a, float d, float rd) { float q = a * rd; return fmaf(fmaf(-q, d, a), rd, q); }
DEV float dot2(unsigned a, unsigned b, float c) { return __builtin_amdgcn_fdot2_f32_bf16(__builtin_bit_cast(v2bf, a), __builtin_bit_cast(v2bf, b), c, false); }
struct Stat { float mb, sd, rd; };
DEV Stat mk_stat(float m, float var) {
  Stat st;
  st.mb = rbf(m);
  st.sd = rbf(__builtin_amdgcn_sqrtf(rbf(rbf(var) + 1e-5f)));
  st.rd = __builtin_amdgcn_rcpf(st.sd);
  return st;
}
DEV Stat ln_stat8(const unsigned (&w)[8]) {
  float s = 0.f, s2 = 0.f, q = 0.f, q2 = 0.f;
#pragma unroll
  for (int i = 0; i < 8; i += 2) {
    s = dot2(w[i], 0x3f803f80u, s); q = dot2(w[i], w[i], q);
    s2 = dot2(w[i + 1], 0x3f803f80u, s2); q2 = dot2(w[i + 1], w[i + 1], q2);
  }
  float t = s32(s + s2, q + q2);
  t += dpp<0xB1>(t); t += dpp<0x4E>(t); t += dpp<0x141>(t); t += dpp<0x140>(t);
  t = x16sum(t) * (1.f / 1024.f);
  auto mq = __builtin_amdgcn_permlane32_swap(fbits(t), fbits(t), false, false);
  const float m = bitsf(mq[0]);
  float var = fmaf(-m, m, bitsf(mq[1]));
  if (__builtin_expect(__builtin_amdgcn_ballot_w64(!(m * m <= 16.f * var)) != 0, 0)) {
    q = 0.f;
#pragma unroll
    for (int i = 0; i < 8; ++i) { float a = bflo(w[i]) - m, b = bfhi(w[i]) - m; q = fmaf(a, a, q); q = fmaf(b, b, q); }
    var = wave_sum(q) * (1.f / 1024.f);
  }
  return mk_stat(m, var);
}
DEV unsigned ln_pair(unsigned x, const Stat& st) {
  unsigned d = pk(bflo(x) - st.mb, bfhi(x) - st.mb);
  return pk(bflo(d) * st.rd, bfhi(d) * st.rd);
}
DEV unsigned onep(unsigned c) { return pk(1.f + bflo(c), 1.f + bfhi(c)); }
DEV unsigned mod_pair(unsigned ln, unsigned c1, unsigned s) {
  unsigned p = pk(bflo(ln) * bflo(c1), bfhi(ln) * bfhi(c1));
  return pk(bflo(p) + bflo(s), bfhi(p) + bfhi(s));
}
DEV unsigned sopq(unsigned a) { asm("" : "+s"(a)); return a; }
DEV float fopq(float a) { asm volatile("" : "+v"(a)); return a; }
DEV unsigned res1(float x, unsigned g, float a, float zero) {
  const float p = dot2(pk(a, a), g, zero);
  const float r = dot2(pk(p, p), sopq(0x3f80u), x);
  return pk(r, r);
}
DEV float silu1(float v) {
  const float d = 1.f + __expf(-v);
  return __builtin_amdgcn_div_fixupf(fdiv(v, d, __builtin_amdgcn_rcpf(d)), d, v);
}

#define STAMP(k)
#define STAMP_DUMP()

#define FIN() do { STAMP_DUMP(); __builtin_amdgcn_endpgm(); } while (0)
DEV float unit_reduce(v4f a) {
  const float r = s16(s32(a[0], a[2]), s32(a[1], a[3]));
  return r + dpp<0x128>(r);
}
DEV void wg_barrier() { asm volatile("s_waitcnt lgkmcnt(0)\n\ts_barrier" ::: "memory"); }

typedef u64 v4l __attribute__((ext_vector_type(4)));
#define CPF 1
__global__ void __launch_bounds__(NTHR, 3)
fused_kernel(const void*, const void*, const void*, const void*, const void*, const void*, const void*, const void*, const void*, const void*,
             void*, void*) {
  const int lane = threadIdx.x & 63;
  const int wave = __builtin_amdgcn_readfirstlane(threadIdx.x >> 6);
  const int wg = blockIdx.x;
  const int n = lane & 15, g = lane >> 4;
  v4l a0, a1, a2;
  unsigned cpf;
  asm volatile("s_load_dwordx8 %0, %3, 0x0\n\ts_load_dwordx8 %1, %3, 0x20\n\ts_load_dwordx8 %2, %3, 0x40"
               : "=&s"(a0), "=&s"(a1), "=&s"(a2) : "s"(__builtin_amdgcn_kernarg_segment_ptr()));
  asm volatile("s_getpc_b64 s[92:93]\n.Lfk_a:\n\ts_and_b32 s93, s93, 0xffff\n\ts_cmp_eq_u32 %3, 0\n\ts_cselect_b32 s94, .Lfk_z-.Lfk_a, 0\n\t"
               "s_mov_b32 s95, 0x20000\n\tbuffer_load_dword %0, %1, s[92:95], %2 offen"
               : "=v"(cpf) : "v"(lane * 128), "s"((wg & 56) * 1024), "s"(CPF ? wave : 1) : "s92", "s93", "s94", "s95", "scc");
  asm volatile("s_waitcnt lgkmcnt(0)" : "+s"(a0), "+s"(a1), "+s"(a2));
  const char *px = (const char*)a0[0], *pk_ = (const char*)a0[1], *pv = (const char*)a0[2], *pwq = (const char*)a0[3];
  const char *pwo = (const char*)a1[0], *pwg = (const char*)a1[1], *pwu = (const char*)a1[2], *pwd = (const char*)a1[3];
  const char *pc = (const char*)a2[0], *pwm = (const char*)a2[1], *pout = (const char*)a2[2], *pws = (const char*)a2[3];
  const rsrc_t rws = mk(pws, WS_BYTES);
  const int bk = wg & 31, sp = wg >> 5;
  const int ab = bk >> 2, kvh = bk & 3;
  const bool first = wg < 32;
  const int xj = wg >> 3, xc = wg & 7;
  const bool rW = xj >= 24, rB = xj < 2, rG = xj >= 5 && xj < 19, rD = !rW && !rB && !rG;
  const int wc = xc * 8 + xj - 24, oh = wc >> 1;
  const int gl = rB ? wg : xc * 14 + xj - 5, gks = gl & 1;
  const int dl = xc * 7 + xj - (xj < 5 ? 2 : 17), dft = dl >> 2, dj = dl & 3;

  STAMP(0);
  const int tq = (lane >> 2) & 3, tp = lane & 3;
  const bool hi = n >= 8;
  const unsigned sw64 = (((lane & 7) ^ (((lane >> 4) & 1) << 1)) << 4);
  unsigned ep;

  if (wave < 8) {
    const rsrc_t rk = mk((const char*)pk_ + (size_t)bk * 2097152 + sp * 262144 + wave * 32768, 32768);
    const rsrc_t rv = mk((const char*)pv + (size_t)bk * 2097152 + sp * 262144 + wave * 32768, 32768);
    const unsigned rot = wave + wg;
    const unsigned t0 = (unsigned)__builtin_amdgcn_s_memrealtime();
    if (first) {
      asm volatile("s_barrier" ::: "memory");
      lds_wait<8>(F_GO, 4);
    }
    bool ahead = rG && (GA >> ((gl >> 2) & 3) & 1);
    int late;
    unsigned due = t0 + C0 + (KA0 - 1) * TAU - (ahead ? CF : 0);
    auto pace = [&]() __attribute__((always_inline)) {
      SCHED();
      late = (int)((unsigned)__builtin_amdgcn_s_memrealtime() - due);
      if (PL)
        asm volatile("s_setprio 0\n\ts_cmp_lt_i32 %0, %1\n\ts_cbranch_scc1 1f\n\ts_setprio 1\n\ts_cmp_lt_i32 %0, %2\n\ts_cbranch_scc1 1f\n\ts_setprio 2\n1:"
                     : : "s"(late), "n"(PL), "n"(PL2) : "scc");
      if (late <= 0) vmwait<WS - 1>();
      if (WX > WB) { if (late < LX) vmwait<WB - 1>(); vmwait<WX - 1>(); } else vmwait<WB - 1>();
      due += TAU;
      SCHED();
    };
    v4u kf[8][4];
    const unsigned kva = ((n & 3) * 4 + ((n >> 2) & 1)) * 256 + g * 16 + (n >> 3) * 64, kvb = kva ^ 64;
#pragma unroll
    for (int i = 0; i < 32; ++i) {
      const int o = i >> 2, t = (o & 4) | ((o & 1) << 1) | ((o >> 1) & 1);
      if (i == WS) {
        if (!first) asm volatile("s_barrier" ::: "memory");
        asm volatile("s_load_dword %0, %1, 0x0" : "=s"(ep) : "s"(pws) : "memory");
      }
      if (i >= KA0) pace(); else if (i >= WS) vmwait<WS - 1>();
      LD_NT(kf[t][i & 3], rk, (t & 2) ? kvb : kva, ((t + rot) & 7) * 4096, ((i >> 1) & 1) * 512 + (i & 1) * 128);
    }
    asm volatile("" : : "v"(cpf));
    STAMP(1);
    const unsigned vvo = lane * 16;
    lds_wait<4>(F_HEAD, 4);
#pragma nounroll
    for (int i = 0; i < 16; ++i) { pace(); dma16(rv, vvo, (((i >> 2) + rot) & 7) * 4096 + (i & 3) * 1024, i * 8192 + wave * 1024); }
    STAMP(3);
    lds_wait<2>(F_Q, 1);
    asm volatile("s_waitcnt vmcnt(16) lgkmcnt(0)" : "+s"(ep) : : "memory");
#pragma unroll
    for (int t = 0; t < 8; t += 2) TIE8(kf[t][0], kf[t][1], kf[t][2], kf[t][3], kf[t + 1][0], kf[t + 1][1], kf[t + 1][2], kf[t + 1][3]);
    const v4u tb = tagbytes(ep + 0x01010101u);
    const unsigned tg = opq(ep + 0x01010101u);
    STAMP(2);
    v4u qf[2];
    {
      const unsigned qa = opq(L_QB + (n & 7) * 288 + (n >> 3) * 64 + g * 16);
      qf[0] = lds_ld<v4u>(qa); qf[1] = lds_ld<v4u>(qa + 128);
    }
    v4u vf[4][8];
    auto vload = [&](int i) __attribute__((always_inline)) {
      pace();
      LD_NT(vf[2 + (i >> 3)][i & 7], rv, vvo, ((4 + (i >> 2) + rot) & 7) * 4096, (i & 3) * 1024);
      SCHED();
    };
    unsigned sc[4][2];
    float mx = -INFINITY;
    const bool va = (g >> 1) == (n >> 3);
#pragma unroll
    for (int q = 0; q < 4; ++q) {
      const int ta = (q >> 1) * 4 + (q & 1), tbb = ta + 2;
      const v4f z = {0.f, 0.f, 0.f, 0.f};
      v4f a0 = mfma(kf[ta][0], qf[0], z), b0 = mfma(kf[tbb][0], qf[0], z), a1 = mfma(kf[ta][2], qf[0], z), b1 = mfma(kf[tbb][2], qf[0], z);
      a0 = mfma(kf[ta][1], qf[1], a0); b0 = mfma(kf[tbb][1], qf[1], b0); a1 = mfma(kf[ta][3], qf[1], a1); b1 = mfma(kf[tbb][3], qf[1], b1);
      if (q & 1) vload(q >> 1);
      const v4f e0 = sel4(va, a0, b0), e1 = sel4(va, a1, b1);
      float s[4];
#pragma unroll
      for (int r = 0; r < 4; ++r) {
        auto w = __builtin_amdgcn_permlane32_swap(fbits(e0[r]), fbits(e1[r]), false, false);
        s[r] = bitsf(w[0]) + dpp<0x128>(bitsf(w[1]));
      }
#pragma unroll
      for (int r = 0; r < 2; ++r) {
        unsigned u = pk(s[2 * r], s[2 * r + 1]);
        u = pk(bflo(u) * 0.08838834764831845f, bfhi(u) * 0.08838834764831845f);
        sc[q][r] = u;
        mx = fmaxf(mx, fmaxf(bflo(u), bfhi(u)));
      }
    }
    mx = max8up(mx);
    float ls = 0.f;
    v4u pf[4];
#pragma unroll
    for (int b = 0; b < 2; ++b) {
      v4u p;
#pragma unroll
      for (int e = 0; e < 4; ++e) {
        const unsigned u = sc[2 * b + (e >> 1)][e & 1];
        const float p0 = __expf(bflo(u) - mx), p1 = __expf(bfhi(u) - mx);
        ls += p0 + p1;
        p[e] = pk(p0, p1);
      }
      pf[2 * b] = dup_lo(p); pf[2 * b + 1] = dup_hi(p);
    }
    ls = sum8up(ls);
    if (lane < 8) { lds_st<float>(L_MX + wave * 32 + lane * 4, mx); lds_st<float>(L_LS + wave * 32 + lane * 4, ls); }
    lds_signal(F_KML, lane);
    STAMP(4);
    v4f acc[8];
    auto piece = [&](int p, int i, bool first) __attribute__((always_inline)) {
      v4u a;
#pragma unroll
      for (int e = 0; e < 4; ++e) a[e] = __builtin_amdgcn_perm(vf[p][2 * e + 1][i >> 1], vf[p][2 * e][i >> 1], (i & 1) ? 0x07060302u : 0x05040100u);
      acc[i] = mfma(a, pf[p], first ? (v4f){0.f, 0.f, 0.f, 0.f} : acc[i]);
    };
    auto tie = [&](int p) __attribute__((always_inline)) { TIE8(vf[p][0], vf[p][1], vf[p][2], vf[p][3], vf[p][4], vf[p][5], vf[p][6], vf[p][7]); };
    const unsigned wp = rB ? 12288 : rG ? 7168 : 2048;
    const rsrc_t rt = mk(rD ? pwd : rB ? pwm : (gl & 2) ? pwu : pwg, (rD ? 3584 : 1024) * wp);
    const unsigned wsh = rD ? 3 : 4;
    const unsigned wb = rD ? (dft * 256 + wave * 4) * wp + dj * 512 : (gks * 512 + wave * 4) * wp + (rB ? 10240 + (gl >> 1) * 256 : (gl >> 2) * 256);
    unsigned wv = g * wp + ((n ^ (g << 1) ^ ((wave & 2) << 2)) << 4), wm = (1u << wsh) - 1, w32 = 32 * wp;
    asm volatile("" : "+v"(wv), "+s"(wm), "+s"(w32));
    auto wpiece = [&](unsigned f) __attribute__((always_inline)) { dma16(rt, wv, wb + (f & wm) * w32 + (f >> wsh) * 256, f * 8192 + wave * 1024); };
    v4u wf[16];
    const unsigned tw = (8 * g + tq) * 256 + (((2 * wave + (tp >> 1)) ^ ((tq | ((g & 1) << 2)) << 1)) << 4) + 8 * (tp & 1);
    auto wfrag = [&](int f0, int f1) __attribute__((always_inline)) {
      const unsigned b0 = opq(tw), b1 = opq(tw + 65536);
#pragma unroll
      for (int f = f0; f < f1; ++f) wf[f] = trfrag((f & 8 ? b1 : b0) + (f & 7) * 8192, 1024);
    };
    const int hd = n & 7, ci = lane & 31;
    unsigned wa0, wa1, ra[4];
    float fw;
    v4u ml[1];
    const unsigned mlo = (lane & 1) * 64 + (lane >> 4) * 16 + (rW ? 0u : 0x80000000u), mls = rW ? O_ML + oh * 1024 + wave * 128 : 0;
    auto prep = [&]() __attribute__((always_inline)) {
      wa0 = opq(L_MG + ((hd >> 1) | ((g & 1) << 2) | ((n >> 3) << 3) | ((g >> 1) << 4)) * 256 + ((wave ^ ((hd >> 1) | ((n >> 3) << 2))) << 5) + (hd & 1) * 16);
      wa1 = opq(wa0 ^ 16);
      const unsigned r0 = L_MG + ((wave >> 1) | (((ci >> 3) & 1) << 2) | (((ci >> 2) & 1) << 3) | ((ci >> 4) << 4) | ((ci & 3) << 5)) * 256 +
                          ((((ci >> 1) & 3) | ((((lane >> 5) ^ (ci >> 3)) & 1) << 2)) << 5) + ((wave ^ ci) & 1) * 16;
#pragma unroll
      for (int k = 0; k < 4; ++k) ra[k] = opq(r0 ^ (k << 5));
      lds_wait<1>(F_WT, 8);
      fw = __expf(mx - max8up(lds_ld<float>(opq(L_MX + lane * 4))));
      SCHED();
    };
    {
      const unsigned vl = opq(wave * 1024 + lane * 16), vl2 = opq(65536 + wave * 1024 + lane * 16);
#pragma unroll
      for (int i = 2; i < 16; ++i) {
        vload(i);
        if (i == 3 || i == 9) {
          const int p = i == 9;
          if (p) vmwait<10>(); else vmwait<12>();
#pragma unroll
          for (int j = 0; j < 8; ++j) vf[p][j] = lds_ld<v4u>((p ? vl2 : vl) + j * 8192);
        }
        if (i >= 4 && i < 8) { piece(0, 2 * i - 8, true); piece(0, 2 * i - 7, true); }
        if (i == 10) { tie(1); lds_signal(F_WT, lane); }
        if (i >= 10 && i < 14) { piece(1, 2 * i - 20, false); piece(1, 2 * i - 19, false); }
      }
      asm volatile("buffer_load_dwordx4 %0, %1, %2, %3 offen sc1" : "=v"(ml[0]) : "v"(mlo), "s"(rws), "s"(mls) : "memory");
      SCHED();
      vmwait<9>();
      tie(2);
#pragma unroll
      for (int k = 0; k < 8; ++k) piece(2, k, false);
      SCHED();
      prep();
      if (rG && late > TL) ahead = true;
      if (GB && ahead) {
#pragma unroll
        for (int f = 0; f < GB; ++f) wpiece(f);
        vmwait<GB + 1>();
      } else {
        vmwait<1>();
      }
      tie(3);
#pragma unroll
      for (int k = 0; k < 8; ++k) piece(3, k, false);
    }
    STAMP(5);
    {
#pragma unroll
      for (int i = 0; i < 4; ++i) lds_st<v4f>(((i & 1) ? wa1 : wa0) + i * 8192, sel4(hi, acc[i + 4], acc[i]) * fw);
      wg_barrier();
      v4f s = (lds_ld<v4f>(ra[0]) + lds_ld<v4f>(ra[1])) + (lds_ld<v4f>(ra[2]) + lds_ld<v4f>(ra[3]));
#pragma unroll
      for (int i = 0; i < 4; ++i) s[i] = x32sum(s[i]);
      if (lane < 32) st_dev2((v2u){canon(pk(s[0], s[1])), canon(pk(s[2], s[3]))}, rws, ci * 8, O_AO + (kvh * 8 + wave) * 16384 + ab * 2048 + sp * 256);
    }
    STAMP(6);
    v4u la[4];
    const v4f z = {0.f, 0.f, 0.f, 0.f};
    if (rW) {
      land_set(la, (v4u){SENT, SENT, SENT, SENT});
      const int un = wc * 4 + wave;
      const unsigned uo = ((lane >> 2) & 1) * 2048 + (lane & 3) * 8, us = (un >> 6) * 4096 + (un & 63) * 32;
      const unsigned oo = opq(uo + g * 2), xo = opq(uo + (lane >> 3) * 16384);
      v4u gv;
      unsigned xr;
      {
        const unsigned wa = opq(L_AX + wave * 288 + n * 16 + g * 4);
        asm volatile("s_waitcnt vmcnt(1)" : "+v"(ml[0]) : : "memory");
        ml[0] ^= tb;
        if (__any((((ml[0].x | ml[0].y) | (ml[0].z | ml[0].w)) & 255u) != 0)) gatherf<1, 4, 255u>(ml, rws, mlo, mls, 0, tb);
        float M = fmaxf(bitsf(ml[0].x), dpp<0xB1>(bitsf(ml[0].x)));
        M = x32max(x16max(M));
        const float e = __expf(bitsf(ml[0].x) - M);
        float L = bitsf(ml[0].y) * e;
        L = x32sum(x16sum(L + dpp<0xB1>(L)));
        const float f = e / L;
        float f0 = dpp<0xA0>(f), f1 = dpp<0xF5>(f);
        asm volatile("" : "+v"(f0), "+v"(f1));
        const unsigned far = wave < 4 ? 0u : 0x80000000u;
        asm volatile("buffer_load_dwordx4 %0, %2, %3, %4 offen sc1\n\tbuffer_load_ushort %1, %5, %6, %7 offen"
                     : "=&v"(gv), "=&v"(xr) : "v"(lane * 16 + far), "s"(rws), "s"(O_GX + (un & 255) * 2048), "v"(oo + far), "s"(mk(px, 16384)),
                       "s"(us & 16383) : "memory");
        if (D1) __builtin_amdgcn_s_sleep(D1);
        v4u a[2];
        poll2s<TH1, false>(a, la, rws, lane * 16, O_AO + oh * 16384 + wave * 2048, O_AO + oh * 16384 + wave * 2048 + 1024);
        STAMP(7);
        float lo[4], up[4];
#pragma unroll
        for (int i = 0; i < 4; ++i) {
          lo[i] = bflo(a[0][i]) * f0 + bflo(a[1][i]) * f1;
          up[i] = bfhi(a[0][i]) * f0 + bfhi(a[1][i]) * f1;
        }
        lds_st<unsigned>(wa, pk(s16(s32(lo[0], lo[2]), s32(lo[1], lo[3])), s16(s32(up[0], up[2]), s32(up[1], up[3]))));
        wg_barrier();
      }
      if (wave < 4) {
        auto gate = [&](int k, int tries, bool& ok) __attribute__((always_inline)) {
          v4u a[1];
          ok = gatherf<1, 4, 255u>(a, rws, (lane & (k ? 15 : 63)) * 16, O_GX + un * 2048 + k * 1024, 0, tb, tries);
          return pk(unit_reduce(asf4(a[0])) * (k ? 0.25f : 1.f), 0.f);
        };
        land_drain(la);
        asm volatile("" : "+v"(gv), "+v"(xr));
        float xe = bflo(xr);
        gv ^= tb;
        bool ok = !__any((((gv.x | gv.y) | (gv.z | gv.w)) & 255u) != 0);
        unsigned gt = pk(unit_reduce(asf4(gv)), 0.f);
        land_set(la, (v4u){SENT, SENT, SENT, SENT});
        xe = fopq(xe);
        gt = fbits(fopq(bitsf(gt)));
        float zero = fopq(0.f);
        STAMP(8);
        if (D2) __builtin_amdgcn_s_sleep(D2);
        v4u p[2];
        poll2s<TH2, false>(p, la, rws, lane * 16, O_WP + un * 2048, O_WP + un * 2048 + 1024);
        STAMP(9);
        float s;
        {
          float r[2];
#pragma unroll
          for (int i = 0; i < 2; ++i)
            r[i] = s16(s32(bflo(p[0][2 * i]) + bflo(p[1][2 * i]), bflo(p[0][2 * i + 1]) + bflo(p[1][2 * i + 1])),
                       s32(bfhi(p[0][2 * i]) + bfhi(p[1][2 * i]), bfhi(p[0][2 * i + 1]) + bfhi(p[1][2 * i + 1])));
          const bool r1 = lane & 4;
          s = (r1 ? r[1] : r[0]) + dpp<0x124>(r1 ? r[0] : r[1]);
          s += dpp<0x128>(s);
        }
        if (!ok) gt = gate(0, POLL_MAX, ok);
        const unsigned x1 = res1(xe, gt, s, zero);
        auto h = __builtin_amdgcn_permlane16_swap(x1, x1, false, false);
        const unsigned x2 = canon(__builtin_amdgcn_perm(h[1], h[0], 0x07060100u));
        h = __builtin_amdgcn_permlane32_swap(x2, x2, false, false);
        st_dev2((v2u){h[0], h[1]}, rws, xo, O_X1 + us);
        STAMP(10);
        __builtin_amdgcn_s_sleep(D5);
        gt = gate(1, G5, ok);
        land_drain(la);
        land_set(la, ~tb);
        xe = fopq(bflo(x1));
        gt = fbits(fopq(bitsf(gt)));
        zero = fopq(0.f);
        const rsrc_t ro = mk(pout, 16384);
        STAMP(11);
        v4u c[2];
        Watch w5 = {};
        if (WL5) w5 = watch_t(rws, lane * 512 + (un & 1) * 128, O_DP + un * 2048, tb.x, 255u, (1ull << WL5) - 1, WT5);
        poll2<TH5>(c, la, rws, lane * 16, O_DP + un * 2048, tb);
        keep_watch(w5);
        STAMP(12);
        s = unit_reduce(asf4(c[0]) + sel4(lane >= 48, z, asf4(c[1])));
        if (!ok) gt = gate(1, POLL_MAX, ok);
        const unsigned o = res1(xe, gt, s, zero);
        if (n < 8) __builtin_amdgcn_raw_buffer_store_b16((unsigned short)o, ro, oo, us, 0);
        if (un == 0 && lane == 0) __builtin_amdgcn_raw_buffer_store_b32(ep + 0x01010101u, rws, 0, WS_EPOCH, 16);
        STAMP(15);
      }
      land_keep(la);
      FIN();
    } else if (xj != 19) {
      const unsigned px_ = ((n >> 1) & 3) * 131072 + (n & 1) * 64 + g * 16;
      auto t3mm = [&](unsigned b3, unsigned po, unsigned pso) __attribute__((always_inline)) {
        v4u b[8];
#pragma unroll
        for (int kb = 0; kb < 8; ++kb) b[kb] = lds_ld<v4u>(b3 + kb * 64);
        SCHED();
        v4f d0 = z, d1 = z;
#pragma unroll
        for (int kb = 0; kb < 8; ++kb) { d0 = mfma(wf[kb], b[kb], d0); d1 = mfma(wf[8 + kb], b[kb], d1); }
#pragma unroll
        for (int i = 0; i < 4; ++i) d0[i] += dpp<0x128>(d1[i]);
        if (n < 8) st_dev(tagv(d0, tg), rws, po, pso);
        STAMP(12);
      };
      if (rG) {
        asm volatile("s_barrier" ::: "memory");
#pragma nounroll
        for (int f = ahead ? GB : 0; f < 16; ++f) { vmwait<GW - 1>(); wpiece(f); }
        const unsigned b3 = opq(L_AX + (n & 7) * 1056 + (n >> 3) * 528 + g * 16);
        const unsigned po = opq(n * 64 + g * 16);
        const unsigned pso = O_GP + (gl >> 3) * 32768 + (((gl >> 2) & 1) * 4 + (wave >> 1)) * 4096 + (gks * 2 + ((gl >> 1) & 1)) * 1024 + (wave & 1) * 512;
        const unsigned wa = opq(L_AX + wave * 1056 + lane * 16 + (lane >> 5) * 16), xo = O_X1 + xc * 16384 + wave * 2048;
        const unsigned sa_ = opq(L_SB + wave * 1024 + lane * 16);
        const v4u sbv = lds_ld<v4u>(sa_), cbv = lds_ld<v4u>(sa_ + 8192);
        float sf[8], pf_[8];
        unsigned cm[8];
#pragma unroll
        for (int e = 0; e < 4; ++e) {
          sf[2 * e] = fopq(bflo(sbv[e])); sf[2 * e + 1] = fopq(bfhi(sbv[e]));
          cm[2 * e] = fbits(fopq(bitsf(cbv[e] & 0xffffu))); cm[2 * e + 1] = fbits(fopq(bfhi(cbv[e])));
          pf_[2 * e] = fopq(0.f); pf_[2 * e + 1] = fopq(0.f);
        }
        const unsigned k10 = sopq(0x3f80u), k01 = sopq(0x3f800000u);
        vmwait<PW>();
        const unsigned pv = opq(lane * (2048 / PB) + (xj & 3) * 64);
        unsigned go = 0;
        probe_ld(la, rws, pv, xo);
        vmwait<0>();
        probe_first<true>(la, rws, lane * 16, xo + gks * 1024, xo + 1024 - gks * 1024, pv, xo, go);
        asm volatile("s_barrier" ::: "memory");
        STAMP(7);
        wfrag(0, 16);
        cfence();
        vmwait<0>();
        probe_first<false>(la, rws, lane * 16, xo + gks * 1024, xo + 1024 - gks * 1024, pv, xo, go);
        Watch w3 = watch_x(rws, lane * (2048 / WL3) + (xj & 3) * 128, xo, SENT, SENT, (1ull << WL3) - 1, WT3, go);
        late_first(la, rws, lane * 16, xo + gks * 1024, xo + 1024 - gks * 1024, go);
        {
          v4u xv[2];
          poll2s<TH3, true>(xv, la, rws, lane * 16, xo + gks * 1024, xo + 1024 - gks * 1024);
          keep_watch(w3);
          const unsigned xr[8] = {xv[0].x, xv[0].y, xv[0].z, xv[0].w, xv[1].x, xv[1].y, xv[1].z, xv[1].w};
          STAMP(11);
          const Stat st = ln_stat8(xr);
          v4u h2;
#pragma unroll
          for (int e = 0; e < 4; ++e) h2[e] = pk(bflo(xr[e]) - st.mb, bfhi(xr[e]) - st.mb);
          SCHED();
#pragma unroll
          for (int e = 0; e < 4; ++e) h2[e] = pk(bflo(h2[e]) * st.rd, bfhi(h2[e]) * st.rd);
          SCHED();
#pragma unroll
          for (int e = 0; e < 8; ++e) pf_[e] = dot2(h2[e >> 1], cm[e], pf_[e]);
          SCHED();
#pragma unroll
          for (int e = 0; e < 4; ++e) h2[e] = pk(pf_[2 * e], pf_[2 * e + 1]);
          SCHED();
#pragma unroll
          for (int e = 0; e < 8; ++e) sf[e] = dot2(h2[e >> 1], (e & 1) ? k01 : k10, sf[e]);
          SCHED();
#pragma unroll
          for (int e = 0; e < 4; ++e) h2[e] = pk(sf[2 * e], sf[2 * e + 1]);
          lds_st<v4u>(wa, h2);
          wg_barrier();
        }
        t3mm(b3, po, pso);
        land_keep2(la);
        FIN();
      }
      asm volatile("s_barrier" ::: "memory");
      if (DT) {
        const unsigned tt = t0 + DT;
#pragma nounroll
        for (int i = 0; i < 2000 && (int)((unsigned)__builtin_amdgcn_s_memrealtime() - tt) < 0; ++i) __builtin_amdgcn_s_sleep(2);
      }
      if (DD) __builtin_amdgcn_s_sleep(DD);
#pragma nounroll
      for (int f = 0; f < 16; ++f) { vmwait<DW - 1>(); wpiece(f); }
      vmwait<0>();
      asm volatile("s_barrier" ::: "memory");
      STAMP(7);
      if (rB) {
        wfrag(0, 16);
        t3mm(L_AX + (n & 7) * 1056 + (n >> 3) * 528 + g * 16, px_, O_GX + ((gl >> 1) * 8 + wave) * 2048 + (8 + gks) * 128);
        FIN();
      } else {
        cfence();
        wfrag(0, 16);
        const unsigned pd = opq(((n >> 1) & 3) * 131072 + (n >> 3) * 16384 + (n & 1) * 64 + g * 16);
        const unsigned wa = opq(L_AX + ((lane >> 2) & 7) * 544 + wave * 64 + (wave >> 2) * 16 + (lane >> 5) * 32 + (lane & 3) * 8);
        const unsigned bb = opq(L_AX + (n & 7) * 544 + (n >> 3) * 272 + g * 16);
        land_set(la, ~tb);
        float hf[4];
#pragma unroll
        for (int e = 0; e < 4; ++e) hf[e] = fopq(0.f);
        if (D4) __builtin_amdgcn_s_sleep(D4);
        v4u a[4];
        Watch w4 = {};
        if (WL4) w4 = watch_t(rws, lane * (4096 / (WL4 + !WL4)), O_GP + dft * 32768 + wave * 4096, tb.x, 255u, (1ull << WL4) - 1, WT4);
        poll4<TH4>(a, la, rws, lane * 16, O_GP + dft * 32768 + wave * 4096, tb);
        keep_watch(w4);
        STAMP(13);
        const v4f tgt = asf4(a[0]) + asf4(a[2]), tu = asf4(a[1]) + asf4(a[3]);
#pragma unroll
        for (int e = 0; e < 4; ++e) {
          const float r = __builtin_elementwise_maximum(tgt[e], 0.f);
          hf[e] = dot2((e & 1) ? pk(0.f, r) : pk(r, 0.f), pk(tu[e & 2], tu[e | 1]), hf[e]);
        }
        lds_st<v2u>(wa, (v2u){pk(hf[0], hf[1]), pk(hf[2], hf[3])});
        wg_barrier();
        v4u b[4];
#pragma unroll
        for (int kb = 0; kb < 4; ++kb) b[kb] = lds_ld<v4u>(bb + kb * 64);
        SCHED();
        v4f d[4];
#pragma unroll
        for (int kb = 0; kb < 4; ++kb)
#pragma unroll
          for (int t = 0; t < 4; ++t) d[t] = mfma(wf[4 * t + kb], b[kb], kb ? d[t] : z);
        const v4f x = sel4(hi, d[1], d[2]);
        v4f y = sel4(hi, d[3], d[0]);
#pragma unroll
        for (int i = 0; i < 4; ++i) y[i] += dpp<0x128>(x[i]);
        st_dev(tagv(y, tg), rws, pd, O_DP + (dj * 16 + wave) * 2048 + dft * 128);
        STAMP(14);
        land_keep(la);
        FIN();
      }
    }
    FIN();
  }

  const int cw = wave - 8;
  __builtin_amdgcn_s_setprio(3);
  unsigned cb_n = 0;
  const bool jq = !first && wg < 160;
  const int ks = wg & 1, jt = (wg - 32) >> 1;
  const int mt = MAP == 2 ? 2 * (1 - (xc >> 2) + 2 * (4 * ((xc >> 1) & 1) + (xj & 3))) + (xc & 1)
               : MAP == 3 ? (first ? wg : (wg + 2) & 31) : MAP == 4 ? (first ? wg : (wg + 4) & 31) : wg & 31;
  if (cw == 0 && lane < 32) lds_st<unsigned>(L_FL + 4 * lane, 0u);
  const unsigned h0 = (unsigned)__builtin_amdgcn_s_memrealtime();
  auto later = [&]() __attribute__((always_inline)) {
#pragma nounroll
    for (int i = 0; i < 1000 && (int)((unsigned)__builtin_amdgcn_s_memrealtime() - h0 - SD) < 0; ++i) __builtin_amdgcn_s_sleep(4);
  };
  auto early = [&]() __attribute__((always_inline)) { return (int)((unsigned)__builtin_amdgcn_s_memrealtime() - h0 - QT) < 0; };
  auto softmax_stats = [&](unsigned TAG) __attribute__((always_inline)) {
    lds_wait<8>(F_KML, 8);
    const float mw = lds_ld<float>(L_MX + lane * 4), lw = lds_ld<float>(L_LS + lane * 4);
    const float ms = max8up(mw);
    const float lsum = sum8up(lw * __expf(mw - ms));
    if (lane < 8) st_dev(tag4((v4f){ms, lsum, ms, lsum}, tagbytes(TAG)), rws, lane * 1024, O_ML + kvh * 8192 + ab * 128 + sp * 16);
    STAMP(9);
  };
  const unsigned qo = O_Q + ab * 32768 + kvh * 8192;
  if (rW) {
    asm volatile("s_waitcnt lgkmcnt(0)\n\ts_barrier\n\ts_load_dword %0, %1, 0x0\n\ts_waitcnt lgkmcnt(0) vmcnt(0)" : "=s"(ep) : "s"(pws), "v"(cpf) : "memory");
    const unsigned TAG = ep + 0x01010101u;
    later();
    const rsrc_t rt = mk(pwo, 4096 * 2048);
    const unsigned wv = g * 16384 + ((n ^ (g << 1) ^ ((cw & 1) << 3)) << 4);
#pragma nounroll
    for (int i = 0; i < 32; ++i) {
      vmwait<WH0 - 1>();
      const unsigned w = 2 * cw + ((i >> 2) & 1);
      dma16(rt, wv, (oh * 128 + w + (i >> 3) * 32) * 2048 + (wc & 1) * 1024 + (i & 3) * 256, (((i & 3) << 2) | (i >> 3)) * 8192 + w * 1024);
    }
    vmwait<0>();
    lds_signal(F_CBAR, lane); lds_wait<1>(F_CBAR, 4);
    v4u wf[2][16];
#pragma unroll
    for (int j = 0; j < 2; ++j) {
      const unsigned tw = (8 * g + tq) * 256 + (((4 * cw + 2 * j + (tp >> 1)) ^ ((tq | ((g & 1) << 2)) << 1)) << 4) + 8 * (tp & 1);
      const unsigned b0 = opq(tw), b1 = opq(tw + 65536);
#pragma unroll
      for (int f = 0; f < 16; ++f) wf[j][f] = trfrag((f & 8 ? b1 : b0) + (f & 7) * 8192, 1024);
    }
#pragma unroll
    for (int f = 0; f < 16; f += 4) TIE8(wf[0][f], wf[0][f + 1], wf[0][f + 2], wf[0][f + 3], wf[1][f], wf[1][f + 1], wf[1][f + 2], wf[1][f + 3]);
    lds_signal(F_HEAD, lane);
    STAMP(6);
    if (cw == 3) {
      const v4u tn = tagbytes(TAG) & 15u;
      if (early()) {
        Watch w = watch_q(rws, (lane & 31) * 256 + (sp & 1) * 128, qo, tn.x, 15u, 0xffffffffull, THW);
        release_watch(w);
      }
      STAMP(7);
#pragma nounroll
      for (int it = 0; it < POLL_MAX; ++it) {
#pragma unroll
        for (int k = 0; k < 8; ++k) dma16<true>(rws, lane * 16, qo + k * 1024, L_RED + k * 1024);
        vmwait<0>();
        unsigned bad = 0;
#pragma unroll
        for (int k = 0; k < 4; ++k) {
          const v4u a = lds_ld<v4u>(L_RED + k * 1024 + lane * 16) ^ tn, b = lds_ld<v4u>(L_RED + 4096 + k * 1024 + lane * 16) ^ tn;
          bad |= ((a.x | a.y) | (a.z | a.w)) | ((b.x | b.y) | (b.z | b.w));
          const v4f t = asf4(a) + asf4(b);
          lds_st<v2u>(L_QB + (2 * k + (lane >> 5)) * 288 + (lane & 31) * 8, (v2u){pk(t[0], t[1]), pk(t[2], t[3])});
          SCHED();
        }
        if (!__any((bad & 15u) != 0)) break;
      }
      lds_signal(F_Q, lane);
      STAMP(8);
      softmax_stats(TAG);
    }
    const unsigned bb = opq(L_AX + (n & 7) * 288 + g * 16);
    const unsigned po = opq(((n >> 1) & 3) * 131072 + (n >> 3) * 2048 + (n & 1) * 8 + g * 16);
    const unsigned so = sopq(O_WP + ((wc & 1) * 32 + 2 * cw) * 2048 + oh * 64);
    asm volatile("s_barrier\n\ts_barrier" ::: "memory");
    v4u b[4];
#pragma unroll
    for (int kb = 0; kb < 4; ++kb) b[kb] = lds_ld<v4u>(bb + kb * 64);
    SCHED();
    const v4f z = {0.f, 0.f, 0.f, 0.f};
#pragma unroll
    for (int h = 0; h < 2; ++h) {
      v4f d[4];
#pragma unroll
      for (int kb = 0; kb < 4; ++kb)
#pragma unroll
        for (int t = 0; t < 4; ++t) d[t] = mfma(wf[t & 1][8 * h + 4 * (t >> 1) + kb], b[kb], kb ? d[t] : z);
#pragma unroll
      for (int t = 0; t < 2; ++t) {
        const v4f e = sel4(hi, d[2 * t + 1], d[2 * t]);
        st_dev2((v2u){canon(pk(e[0], e[1])), canon(pk(e[2], e[3]))}, rws, po, so + (2 * h + t) * 16384);
      }
    }
    STAMP(10);
    FIN();
  }
  const bool slow = !first && !jq;
  if (jq) {
    const v4u e = {SENT, SENT, SENT, SENT};
    const unsigned so = O_X1 + ((wg - 32) * 4 + cw) * 2304;
    st_dev(e, rws, lane * 16, so);
    st_dev(e, rws, lane * 16, so + 1024);
    st_dev(e, rws, lane < 16 ? lane * 16 : 0x80000000u, so + 2048);
  }
  v4u in[4];
  {
    const rsrc_t ri = mk(jq ? px : pc, 16384);
    const unsigned vo = (n & 7) * 2048 + cw * 256 + (n >> 3) * 64 + g * 16;
    asm volatile("buffer_load_dwordx4 %0, %4, %5, %6 offen\n\tbuffer_load_dwordx4 %1, %4, %5, %6 offen offset:128\n\t"
                 "buffer_load_dwordx4 %2, %4, %5, %7 offen\n\tbuffer_load_dwordx4 %3, %4, %5, %7 offen offset:128"
                 : "=&v"(in[0]), "=&v"(in[1]), "=&v"(in[2]), "=&v"(in[3]) : "v"(vo), "s"(ri), "s"(ks * 1024), "s"(1024 - ks * 1024) : "memory");
  }
  const unsigned rowb = jq ? 8192 : 12288;
  const rsrc_t rh = mk(jq ? pwq : pwm, 1024 * rowb);
  const unsigned v0 = (lane >> 3) * rowb + sw64, v1 = v0 ^ 64;
  const unsigned col0 = jq ? (ks * 512 + cw * 128) * rowb + jt * 128 : ((ks ^ 1) * 512 + cw * 128) * rowb + (first ? mt : mt + 48) * 128;
  if (!slow) {
#pragma unroll
    for (int k = 0; k < 8; ++k) dma16(rh, (k & 1) ? v1 : v0, k * 8 * rowb + col0, cw * 32768 + k * 1024);
  }
  asm volatile("s_waitcnt lgkmcnt(0)\n\ts_barrier\n\ts_load_dword %0, %1, 0x0" : "=s"(ep) : "s"(pws) : "memory");
  if (first) {
#pragma unroll
    for (int k = 8; k < 16; ++k) dma16(rh, (k & 1) ? v1 : v0, k * 8 * rowb + col0, cw * 32768 + k * 1024);
    vmwait<16>();
  } else {
    if (slow) vmwait<0>();
    vmwait<8>();
  }
  asm volatile("" : "+v"(in[0]), "+v"(in[1]), "+v"(in[2]), "+v"(in[3]) : "v"(cpf) : "memory");
  STAMP(1);
  if (!jq) {
#pragma unroll
    for (int p = 0; p < 4; ++p) {
#pragma unroll
      for (int e = 0; e < 4; ++e) in[p][e] = pk(silu1(bflo(in[p][e])), silu1(bfhi(in[p][e])));
    }
  } else {
    float s = 0.f;
#pragma unroll
    for (int p = 0; p < 4; ++p)
#pragma unroll
      for (int e = 0; e < 4; ++e) s += bflo(in[p][e]) + bfhi(in[p][e]);
    s = sum8up(s);
    if (lane < 8) lds_st<float>(L_SX + lane * 16 + cw * 4, s);
    lds_signal(F_CBAR, lane); cb_n += 4; lds_wait<1>(F_CBAR, cb_n);
    v4f t = lds_ld<v4f>(L_SX + (n & 7) * 16);
    const float m = ((t[0] + t[1]) + (t[2] + t[3])) * (1.f / 1024.f);
    s = 0.f;
#pragma unroll
    for (int p = 0; p < 4; ++p)
#pragma unroll
      for (int e = 0; e < 4; ++e) { float a = bflo(in[p][e]) - m, b = bfhi(in[p][e]) - m; s = fmaf(a, a, s); s = fmaf(b, b, s); }
    s = sum8up(s);
    if (lane < 8) lds_st<float>(L_SX + 128 + lane * 16 + cw * 4, s);
    lds_signal(F_CBAR, lane); cb_n += 4; lds_wait<1>(F_CBAR, cb_n);
    t = lds_ld<v4f>(L_SX + 128 + (n & 7) * 16);
    const Stat st = mk_stat(m, ((t[0] + t[1]) + (t[2] + t[3])) * (1.f / 1024.f));
#pragma unroll
    for (int p = 0; p < 2; ++p)
#pragma unroll
      for (int e = 0; e < 4; ++e) in[p][e] = ln_pair(in[p][e], st);
  }
  STAMP(2);
  asm volatile("s_waitcnt lgkmcnt(0)" : "+s"(ep) : : "memory");
  const unsigned TAG = ep + 0x01010101u;
  const v4u tn = tagbytes(TAG) & 15u;
  if (jq) st_dev((v4u){TAG, TAG, TAG, TAG}, rws, lane ? 0x80000000u : 0u, O_RF + (wg - 32) * 64 + cw * 16);
  unsigned tba[4];
  {
    const unsigned sw = ((tq >> 1) & 1) | ((g & 1) << 1);
    const unsigned tb0 = cw * 32768 + (8 * g + tq) * 128 + (((tp >> 1) ^ (sw << 1)) << 4) + 8 * (tp & 1);
#pragma unroll
    for (int t = 0; t < 4; ++t) tba[t] = opq(tb0 ^ (t << 5));
  }
  v4u wf[4][4];
  v4f d[4];
  auto zero = [&]() __attribute__((always_inline)) {
#pragma unroll
    for (int t = 0; t < 4; ++t) d[t] = (v4f){0.f, 0.f, 0.f, 0.f};
  };
  auto frag = [&](int kb, unsigned off) __attribute__((always_inline)) {
#pragma unroll
    for (int t = 0; t < 4; ++t) wf[kb][t] = trfrag(tba[t] + off + kb * 4096, 512);
  };
  auto mm = [&](int kb, const v4u& x) __attribute__((always_inline)) {
    const v4u b = (kb & 1) ? dup_hi(x) : dup_lo(x);
#pragma unroll
    for (int t = 0; t < 4; ++t) d[t] = mfma(wf[kb][t], b, d[t]);
  };
  auto reduce = [&](unsigned buf, unsigned rb) __attribute__((always_inline)) {
    const unsigned ra = buf + cw * 2048 + (n >> 3) * 1024 + (n & 7) * 64 + g * 16;
    lds_st<v4f>(ra, sel4(hi, d[2], d[0]));
    lds_st<v4f>(ra + 512, sel4(hi, d[3], d[1]));
    lds_signal(F_CBAR, lane); cb_n += 4; lds_wait<0>(F_CBAR, cb_n);
    rb += buf + (lane >> 5) * 4096;
    v4f s = lds_ld<v4f>(rb) + lds_ld<v4f>(rb + 2048);
#pragma unroll
    for (int i = 0; i < 4; ++i) s[i] = x32sum(s[i]);
    return s;
  };
  if (!jq) {
    if (!first) {
      later();
#pragma nounroll
      for (int k = 0; k < 16; ++k) { vmwait<WH0 - 1>(); dma16(rh, v0 ^ ((k & 1) << 6), k * 8 * rowb + col0, cw * 32768 + k * 1024); }
    }
#pragma unroll
    for (int k = 0; k < 16; ++k) {
      if (!first) vmwait<WH0 - 1>();
      dma16(rh, (k & 1) ? v1 : v0, (ks * 1024 - 512 + k * 8) * rowb + col0, cw * 32768 + 16384 + k * 1024);
    }
    lds_signal(F_GO, lane);
    {
      const unsigned a = L_AX + (n & 7) * 1056 + cw * 256 + (cw >> 1) * 16 + (n >> 3) * 64 + g * 16;
      lds_st<v4u>(a, in[0]); lds_st<v4u>(a + 128, in[1]);
    }
    zero();
    vmwait<16>();
#pragma unroll
    for (int kb = 0; kb < 4; ++kb) frag(kb, 0);
#pragma unroll
    for (int kb = 0; kb < 4; ++kb) {
      mm(kb, in[2 + (kb >> 1)]);
      SCHED();
      if (kb == 0) vmwait<12>(); if (kb == 1) vmwait<8>(); if (kb == 2) vmwait<4>(); if (kb == 3) vmwait<0>();
      frag(kb, 16384);
      if (kb) mm(kb - 1, in[(kb - 1) >> 1]);
      SCHED();
    }
    mm(3, in[1]);
    lds_signal(F_HEAD, lane);
    if (first) {
      v4u a[1];
      gatherf<1, 4, ~0u>(a, rws, (lane & 3) * 16, O_RF + (wg * 4 + cw) * 64, 0, (v4u){TAG, TAG, TAG, TAG});
    }
    const v4f s = reduce(L_RED, cw * 512 + (lane & 31) * 16);
    st_dev((v4u){pk(s[0], s[1]), TAG, pk(s[2], s[3]), TAG}, rws,
           first ? (lane >> 5) * 65536 + (lane & 1) * 1024 + ((lane >> 1) & 1) * 256 + ((lane >> 2) & 7) * 16 : ((lane >> 2) & 7) * 8192 + (lane & 3) * 16,
           first ? O_SC + mt * 2048 + (cw & 1) * 512 + (cw >> 1) * 128 : O_MOD + mt * 256 + cw * 64);
    STAMP(11);
  } else {
#pragma unroll
    for (int k = 8; k < 16; ++k) {
      vmwait<WH1 - 1>();
      dma16(rh, (k & 1) ? v1 : v0, k * 8 * rowb + col0, cw * 32768 + k * 1024);
    }
    {
      const int gp = (wg - 32) >> 4, gc = wg & 15;
      v4u cv;
      asm volatile("buffer_load_dwordx4 %0, %1, %2, %3 offen" : "=v"(cv) : "v"((n & 7) * 2048 + g * 16), "s"(mk(pc, 16384)), "s"(gp * 256 + cw * 64) : "memory");
      const rsrc_t rm = mk(pwm, 1024 * 12288);
      const unsigned u0 = (lane >> 3) * 12288 + sw64;
#pragma unroll
      for (int k = 0; k < 4; ++k) {
        vmwait<WH1 - 1>();
        dma16(rm, u0 ^ ((k & 1) << 6), (gp * 128 + cw * 32 + k * 8) * 12288 + 4096 + gc * 128, cw * 32768 + 16384 + k * 1024);
      }
      asm volatile("s_waitcnt vmcnt(4)" : "+v"(cv) : : "memory");
      frag(1, 0); frag(2, 0); frag(3, 0);
#pragma unroll
      for (int e = 0; e < 4; ++e) cv[e] = pk(silu1(bflo(cv[e])), silu1(bfhi(cv[e])));
      vmwait<0>();
      frag(0, 16384);
      zero();
#pragma unroll
      for (int t = 0; t < 4; ++t) d[t] = mfma(wf[0][t], cv, d[t]);
      SCHED();
      frag(0, 0);
      asm volatile("s_waitcnt lgkmcnt(0)" ::: "memory");
      lds_signal(F_HEAD, lane);
      const v4f s = reduce(L_RED, cw * 512 + (lane & 31) * 16);
      if (lane < 32) st_dev(tag4(s, tagbytes(TAG)), rws, (lane >> 3) * 131072 + (lane & 7) * 16, O_GX + (gc * 4 + cw) * 2048 + gp * 128);
    }
    STAMP(3);
    {
      const unsigned so = O_SC + (jt & 1) * 65536 + ks * 16384 + cw * 4096;
      Watch w = watch_m(rws, ((lane & 2) ? 32768 : 0) + (lane & 1) * 2048 + 4, so, TAG, ~0u, 0xfull, THM);
      STAMP(4);
      v4u a[8];
      gather8<DMR>(a, rws, lane * 16, so, 32768, [&](v4u x) { return ((x.y ^ TAG) | (x.w ^ TAG)) != 0; });
      release_watch(w);
      STAMP(5);
#pragma unroll
      for (int p = 0; p < 2; ++p)
#pragma unroll
        for (int e = 0; e < 4; ++e) in[p][e] = mod_pair(in[p][e], onep(a[4 + 2 * p + (e >> 1)][(e & 1) * 2]), a[2 * p + (e >> 1)][(e & 1) * 2]);
    }
    zero();
#pragma unroll
    for (int kb = 0; kb < 4; ++kb) mm(kb, in[kb >> 1]);
    const unsigned r1 = (lane >> 4) & 1;
    v4f s = reduce(L_RED + 8192, (n >> 2) * 512 + (2 * cw + r1) * 64 + (n & 3) * 16);
    if (lane < 32) st_dev(tag4<15u>(s, tn), rws, r1 * 32768 + n * 16, O_Q + cw * 65536 + (jt >> 4) * 8192 + ks * 4096 + (jt & 15) * 256);
  }
  STAMP(6);
  lds_signal(F_CBAR, lane); cb_n += 4;
  if (cw == 3) {
    if (first) __builtin_amdgcn_s_sleep(DQ0);
    Watch w = {};
    if (!slow || early()) w = watch_q(rws, (lane & 31) * 256 + (sp & 1) * 128, qo, tn.x, 15u, 0xffffffffull, THQ);
    STAMP(7);
    v4u a[8];
    gather8<DMR>(a, rws, lane * 16, qo, 4096, [&](v4u x) { x ^= tn; return (((x.x | x.y) | (x.z | x.w)) & 15u) != 0; });
    release_watch(w);
#pragma unroll
    for (int k = 0; k < 4; ++k) {
      const v4f t = asf4(a[k] ^ tn) + asf4(a[4 + k] ^ tn);
      lds_st<v2u>(L_QB + (2 * k + (lane >> 5)) * 288 + (lane & 31) * 8, (v2u){pk(t[0], t[1]), pk(t[2], t[3])});
    }
    lds_signal(F_Q, lane);
    STAMP(8);
    softmax_stats(TAG);
  } else {
    if (rG && cw < 2) {
      lds_wait<32>(F_Q, 1);
      lds_wait<8>(F_CBAR, cb_n);
#pragma nounroll
      for (int h = 0; h < 2; ++h) {
        v4u a[8];
        gatherp<8, 32>(a, rws, lane * 16, O_MOD + cw * 4096 + gks * 2048 + h * 1024, 8192, TAG);
#pragma unroll
        for (int k = 0; k < 8; ++k)
          lds_st<v2u>(L_SB + cw * 8192 + k * 1024 + h * 512 + lane * 8, cw ? (v2u){onep(a[k].x), onep(a[k].z)} : (v2u){a[k].x, a[k].z});
      }
    }
    STAMP(8);
  }
  STAMP_DUMP();
  asm volatile("s_waitcnt vmcnt(0) lgkmcnt(0)" ::: "memory");
}

__global__ void code_end_mark() { asm volatile(".Lfk_z:"); }

extern "C" void kernel_launch(void* const* d_in, const int* in_sizes, int n_in, void* d_out, int out_size, void* d_ws, size_t ws_size,
                              hipStream_t stream) {
  (void)in_sizes; (void)n_in; (void)out_size; (void)ws_size;
  (void)hipFuncSetAttribute((const void*)fused_kernel, hipFuncAttributeMaxDynamicSharedMemorySize, L_END);
  fused_kernel<<<NWG, NTHR, L_END, stream>>>(d_in[0], d_in[1], d_in[2], d_in[3], d_in[4], d_in[6], d_in[7], d_in[8], d_in[9], d_in[10],
                                             d_out, d_ws);
}
```

```cpp
#include <hip/hip_runtime.h>
#include <stdint.h>

typedef unsigned v4u __attribute__((ext_vector_type(4)));
typedef unsigned v2u __attribute__((ext_vector_type(2)));
typedef float v4f __attribute__((ext_vector_type(4)));
typedef float v2f __attribute__((ext_vector_type(2)));
typedef __bf16 v2bf __attribute__((ext_vector_type(2)));
typedef __bf16 v8bf __attribute__((ext_vector_type(8)));
typedef short v4s __attribute__((ext_vector_type(4)));
#define LAS __attribute__((address_space(3)))
typedef __amdgpu_buffer_rsrc_t rsrc_t;
#define DEV __device__ __forceinline__

#define WS 2
#define WB 5
#define TAU 33
#define C0 330
#define KA0 8
#define WH1 8
#define WH0 8
#define DQ0 40
#define DQS 16
#define DMS 6
#define THM 2
#define THQ 8
#define DMR 2
#define D1 8
#define D2 24
#define D3 0
#define D4 0
#define D5 60
#define TH1 32
#define PB 2
#define PW 3
#define TL 40
#define PBT0 1
#define PBT 1
#define TH2 64
#define TH3 32
#define TH4 64
#define TH5 128
#define PSL 5
#define GB 6
#define GW 8
#define DW 3
#define GT 0
#define DT 0
#define QT 1350
#define THW 2
#define FW 8
#define SD 300
#define G5 POLL_MAX
#define GAP 1
#define CF 60
#define MAP 0
#define PL 0
#define PL2 (2 * PL)
#define WX 16
#define LX 120
#define ROT 0
#define GA 2
#define PX 0
#define DTS 4
#define WL2 0
#define WT2 0
#define WL3 2
#define WT3 1
#define WL4 2
#define WT4 1
#define WL5 0
#define WT5 0
#define DD 0

constexpr int NWG = 256, NTHR = 768;
constexpr unsigned SENT = 0xFFFFFFFFu;
constexpr unsigned WS_EPOCH = 0;
constexpr unsigned O_RF = 32768;
constexpr unsigned O_SC = 65536;
constexpr unsigned O_MOD = O_SC + 131072;
constexpr unsigned O_Q = O_MOD + 65536;
constexpr unsigned O_GX = O_Q + 262144;
constexpr unsigned O_ML = O_GX + 524288;
constexpr unsigned O_X1 = O_ML + 32768;
constexpr unsigned O_WP = O_X1 + 131072;
constexpr unsigned O_AO = O_WP + 524288;
constexpr unsigned O_GP = O_AO + 524288;
constexpr unsigned O_DP = O_GP + 14 * 32768;
constexpr unsigned O_STAMP = 65536 + 256 * 16384 + 1048576;
static_assert(O_DP + 524288 <= O_STAMP, "ws map");
constexpr unsigned WS_BYTES = O_STAMP + NWG * 12 * 16 * 8;
constexpr unsigned L_MG = 98304;
constexpr unsigned L_M = 131072;
constexpr unsigned L_RED = L_M;
constexpr unsigned L_SB = L_RED;
constexpr unsigned L_AX = L_RED + 16384;
constexpr unsigned L_QB = L_AX + 8448;
constexpr unsigned L_MX = L_QB + 2304;
constexpr unsigned L_LS = L_MX + 256;
constexpr unsigned L_SX = L_LS + 256;
constexpr unsigned L_FL = L_SX + 256;
constexpr unsigned L_ST = L_FL + 128;
constexpr unsigned L_END = L_ST + 12 * 16 * 8;
static_assert(L_END <= 163840, "lds");
static_assert(GB <= 8, "windows");
enum { F_CBAR, F_Q, F_KML, F_GO, F_HEAD, F_WT };

DEV float bflo(unsigned u) { return __builtin_bit_cast(float, u << 16); }
DEV float bfhi(unsigned u) { return __builtin_bit_cast(float, u & 0xffff0000u); }
DEV unsigned pk(float a, float b) {
  v2f f = {a, b};
  unsigned u = __builtin_bit_cast(unsigned, __builtin_convertvector(f, v2bf));
  asm("" : "+v"(u));
  return u;
}
DEV float rbf(float a) { return bflo(pk(a, a)); }
DEV unsigned fbits(float a) { return __builtin_bit_cast(unsigned, a); }
DEV float bitsf(unsigned a) { return __builtin_bit_cast(float, a); }
DEV unsigned canon(unsigned a) { return a < 0xFFFFFFFEu ? a : 0xFFFFFFFEu; }
DEV v4f sel4(bool c, v4f a, v4f b) { return (v4f){c ? a.x : b.x, c ? a.y : b.y, c ? a.z : b.z, c ? a.w : b.w}; }
DEV v4f asf4(v4u v) { return __builtin_bit_cast(v4f, v); }
DEV unsigned opq(unsigned a) { asm("" : "+v"(a)); return a; }
DEV v4u tagbytes(unsigned T) { return (v4u){T & 255u, (T >> 8) & 255u, (T >> 16) & 255u, T >> 24}; }
template <unsigned M = 255u> DEV v4u tag4(v4f v, v4u tb) {
  v4u o;
  for (int i = 0; i < 4; ++i) o[i] = ((fbits(v[i]) + (M + 1) / 2) & ~M) | tb[i];
  return o;
}

DEV v4u tagv(v4f v, unsigned tg) {
  v4u o;
  for (int i = 0; i < 4; ++i) o[i] = __builtin_amdgcn_perm(fbits(v[i]), tg, 0x07060500u + i);
  return o;
}

template <class T> DEV T lds_ld(unsigned a) { return *(LAS T*)(size_t)a; }
template <class T> DEV void lds_st(unsigned a, T v) { *(LAS T*)(size_t)a = v; }
DEV void cfence() { asm volatile("" ::: "memory"); }
#define SCHED() __builtin_amdgcn_sched_barrier(0)
DEV void lds_signal(int f, int lane) {
  cfence();
  if (lane == 0) __hip_atomic_fetch_add((LAS unsigned*)(size_t)(L_FL + 4 * f), 1u, __ATOMIC_RELAXED, __HIP_MEMORY_SCOPE_WORKGROUP);
  cfence();
}
DEV unsigned lds_flag(int f) { return __builtin_amdgcn_readfirstlane(*(volatile LAS unsigned*)(size_t)(L_FL + 4 * f)); }
template <int SLEEP> DEV void lds_wait(int f, unsigned target) {
  cfence();
#pragma nounroll
  for (int i = 0; i < 4000000; ++i) {
    if (lds_flag(f) >= target) break;
    if (SLEEP) __builtin_amdgcn_s_sleep(SLEEP);
  }
  cfence();
}
constexpr int POLL_MAX = 200000;

DEV rsrc_t mk(const void* p, unsigned bytes) { return __builtin_amdgcn_make_buffer_rsrc((void*)p, 0, bytes, 0x00020000); }
DEV v4u ld_nt(rsrc_t r, unsigned v, unsigned s) { return __builtin_amdgcn_raw_buffer_load_b128(r, v, s, 2); }
DEV v4u ld_dev(rsrc_t r, unsigned v, unsigned s) { return __builtin_amdgcn_raw_buffer_load_b128(r, v, s, 16); }
DEV void st_dev(v4u d, rsrc_t r, unsigned v, unsigned s) {
  asm volatile("s_nop 0\n\tbuffer_store_dwordx4 %0, %1, %2, %3 offen sc1\n\ts_nop 1" : : "v"(d), "v"(v), "s"(r), "s"(s) : "memory");
}
DEV void st_dev2(v2u d, rsrc_t r, unsigned v, unsigned s) {
  asm volatile("s_nop 0\n\tbuffer_store_dwordx2 %0, %1, %2, %3 offen sc1" : : "v"(d), "v"(v), "s"(r), "s"(s) : "memory");
}
template <bool DEVSCOPE = false> DEV void dma16(rsrc_t r, unsigned v, unsigned s, unsigned lds) {
  unsigned keep;
  if (DEVSCOPE)
    asm volatile("s_mov_b32 %0, m0\n\ts_mov_b32 m0, %4\n\ts_nop 0\n\tbuffer_load_dwordx4 %1, %2, %3 offen sc1 lds\n\ts_mov_b32 m0, %0"
                 : "=&s"(keep) : "v"(v), "s"(r), "s"(s), "s"(lds) : "memory");
  else
  asm volatile("s_mov_b32 %0, m0\n\ts_mov_b32 m0, %4\n\ts_nop 0\n\tbuffer_load_dwordx4 %1, %2, %3 offen nt lds\n\ts_mov_b32 m0, %0"
               : "=&s"(keep) : "v"(v), "s"(r), "s"(s), "s"(lds) : "memory");
}
#define LD_NT(d, r, v, s, off) asm volatile("buffer_load_dwordx4 %0, %1, %2, %3 offen offset:%4 nt" : "=v"(d) : "v"(v), "s"(r), "s"(s), "n"(off) : "memory")
#define TIE8(a, b, c, d, e, f, g, h) asm volatile("" : "+v"(a), "+v"(b), "+v"(c), "+v"(d), "+v"(e), "+v"(f), "+v"(g), "+v"(h) : : "memory")
template <int N> DEV void vmwait() { asm volatile("s_waitcnt vmcnt(%0)" ::"n"(N) : "memory"); }
DEV v8bf asbf(v4u v) { return __builtin_bit_cast(v8bf, v); }
DEV v4f mfma(v4u a, v4u b, v4f c) { return __builtin_amdgcn_mfma_f32_16x16x32_bf16(asbf(a), asbf(b), c, 0, 0, 0); }
DEV v2u trrd(unsigned a) { return __builtin_bit_cast(v2u, __builtin_amdgcn_ds_read_tr16_b64_v4i16((LAS v4s*)(size_t)a)); }
DEV v4u trfrag(unsigned a, unsigned step) { v2u x = trrd(a), y = trrd(a + step); return (v4u){x.x, x.y, y.x, y.y}; }

typedef unsigned long long u64;
#define GS(x) #x
#define GX(x) GS(x)
#define P_LD(k, a, d, so, off) "s_mov_b64 exec, %[m" #k "]\n\ts_cbranch_execz 1" #k "f\n\tbuffer_load_dwordx4 v[" #a ":" #d "], %[vo], %[rs], %[" #so "] offen offset:" #off " sc1\n1" #k ":\n\t"
#define P_CK(k, a, b, c, d, e, f, g, h) "s_mov_b64 exec, %[m" #k "]\n\t" \
  "v_xor_b32 v" #e ", %[t0], v" #a "\n\tv_xor_b32 v" #f ", %[t1], v" #b "\n\tv_xor_b32 v" #g ", %[t2], v" #c "\n\tv_xor_b32 v" #h ", %[t3], v" #d "\n\t" \
  "v_or3_b32 %[tm], v" #e ", v" #f ", v" #g "\n\tv_bitop3_b32 %[tm], %[tm], v" #h ", %[ff] bitop3:0xa8\n\tv_cmp_ne_u32 %[m" #k "], 0, %[tm]\n\t"
#define P_CS(k, a, b, c, d, e, f, g, h) "s_mov_b64 exec, %[m" #k "]\n\t" \
  "v_mov_b32 v" #e ", v" #a "\n\tv_mov_b32 v" #f ", v" #b "\n\tv_mov_b32 v" #g ", v" #c "\n\tv_mov_b32 v" #h ", v" #d "\n\t" \
  "v_max3_u32 %[tm], v" #e ", v" #f ", v" #g "\n\tv_max_u32 %[tm], %[tm], v" #h "\n\tv_cmp_eq_u32 %[m" #k "], -1, %[tm]\n\t"
#define P_CNT2 "s_bcnt1_i32_b64 %[bc], %[m0]\n\ts_bcnt1_i32_b64 %[b2], %[m1]\n\ts_add_u32 %[bc], %[bc], %[b2]\n\t"
#define P_CNT4 P_CNT2 "s_bcnt1_i32_b64 %[b2], %[m2]\n\ts_add_u32 %[bc], %[bc], %[b2]\n\ts_bcnt1_i32_b64 %[b2], %[m3]\n\ts_add_u32 %[bc], %[bc], %[b2]\n\t"
#define P_WAIT "s_cmpk_le_u32 %[bc], %[th]\n\ts_cbranch_scc1 2f\n\ts_waitcnt vmcnt(0)\n\ts_branch 3f\n2:\n\ts_sleep %[sl]\n3:\n\t"
#define P_OR2 "s_nop 3\n\ts_or_b64 %[ac], %[m0], %[m1]\n\t"
#define P_OR4 P_OR2 "s_or_b64 %[ac], %[ac], %[m2]\n\ts_or_b64 %[ac], %[ac], %[m3]\n\t"
#define P_WQ2 "s_cbranch_scc0 9f\n\ts_wqm_b64 %[m0], %[m0]\n\ts_wqm_b64 %[m1], %[m1]\n\t"
#define P_WQ4 P_WQ2 "s_wqm_b64 %[m2], %[m2]\n\ts_wqm_b64 %[m3], %[m3]\n\t"
#define P_END "s_sub_u32 %[cn], %[cn], 1\n\ts_cbranch_scc0 0b\n9:\n\ts_mov_b64 exec, -1\n\ts_nop 1"
#define P_DECL u64 m[4] = {~0ull, ~0ull, ~0ull, ~0ull}, ac; unsigned tm, cn = POLL_MAX, bc, b2
#define P_O2 "=&{v[112:115]}"(a[0]), "=&{v[116:119]}"(a[1]), "+{v[128:131]}"(w[0]), "+{v[132:135]}"(w[1]), [m0] "+&s"(m[0]), [m1] "+&s"(m[1]), \
  [tm] "=&v"(tm), [ac] "=&s"(ac), [cn] "+&s"(cn), [bc] "=&s"(bc), [b2] "=&s"(b2)
#define P_O4 P_O2, "=&{v[120:123]}"(a[2]), "=&{v[124:127]}"(a[3]), "+{v[136:139]}"(w[2]), "+{v[140:143]}"(w[3]), [m2] "+&s"(m[2]), [m3] "+&s"(m[3])
#define P_IN [vo] "v"(vo), [rs] "s"(r), [so] "s"(so), [th] "n"(TH), [sl] "n"(PSL)
#define P_TB [t0] "s"(tb.x), [t1] "s"(tb.y), [t2] "s"(tb.z), [t3] "s"(tb.w), [ff] "s"(255u)
#define P_C01(C) C(0, 128, 129, 130, 131, 112, 113, 114, 115) C(1, 132, 133, 134, 135, 116, 117, 118, 119)
template <int TH> DEV void poll4(v4u (&a)[4], v4u (&w)[4], rsrc_t r, unsigned vo, unsigned so, v4u tb) {
  P_DECL;
  asm volatile("0:\n\t" P_LD(0, 128, 131, so, 0) P_LD(1, 132, 135, so, 1024) P_LD(2, 136, 139, so, 2048) P_LD(3, 140, 143, so, 3072) P_CNT4 P_WAIT
               P_C01(P_CK) P_CK(2, 136, 137, 138, 139, 120, 121, 122, 123) P_CK(3, 140, 141, 142, 143, 124, 125, 126, 127) P_OR4 P_WQ4 P_END
               : P_O4 : P_IN, P_TB : "memory", "scc");
}
template <int TH> DEV void poll2(v4u (&a)[2], v4u (&w)[4], rsrc_t r, unsigned vo, unsigned so, v4u tb) {
  P_DECL;
  m[1] = 0xffffffffffffull;
  asm volatile("0:\n\t" P_LD(0, 128, 131, so, 0) P_LD(1, 132, 135, so, 1024) P_CNT2 P_WAIT P_C01(P_CK) P_OR2 P_WQ2 P_END
               : P_O2 : P_IN, P_TB : "memory", "scc");
}
template <int TH, bool PRE> DEV void poll2s(v4u (&a)[2], v4u (&w)[4], rsrc_t r, unsigned vo, unsigned so, unsigned s1) {
  P_DECL;
  asm volatile(".if %[pre]\n\ts_waitcnt vmcnt(0)\n\ts_branch 3f\n.endif\n0:\n\t" P_LD(0, 128, 131, so, 0) P_LD(1, 132, 135, s1, 0) P_CNT2 P_WAIT
               P_C01(P_CS) P_OR2 P_WQ2 P_END
               : P_O2 : P_IN, [s1] "s"(s1), [pre] "n"(PRE) : "memory", "scc");
}
DEV void poll2s_first(v4u (&w)[4], rsrc_t r, unsigned vo, unsigned so, unsigned s1) {
  asm volatile("buffer_load_dwordx4 v[128:131], %2, %3, %4 offen sc1\n\tbuffer_load_dwordx4 v[132:135], %2, %3, %5 offen sc1"
               : "=&{v[128:131]}"(w[0]), "=&{v[132:135]}"(w[1]) : "v"(vo), "s"(r), "s"(so), "s"(s1) : "memory");
}
DEV void probe_ld(v4u (&w)[4], rsrc_t r, unsigned vo, unsigned so) {
  asm volatile("s_mov_b64 exec, %5\n\tbuffer_load_dword v128, %2, %3, %4 offen sc1\n\ts_mov_b64 exec, -1"
               : "=&{v[128:131]}"(w[0]), "=&{v[132:135]}"(w[1]) : "v"(vo), "s"(r), "s"(so), "s"((1ull << PB) - 1) : "memory");
}
#define F_LD "buffer_load_dwordx4 v[128:131], %[vo], %[rs], %[so] offen sc1\n\tbuffer_load_dwordx4 v[132:135], %[vo], %[rs], %[s1] offen sc1\n1:"
template <bool AGAIN> DEV void probe_first(v4u (&w)[4], rsrc_t r, unsigned vo, unsigned so, unsigned s1, unsigned pv, unsigned ps, unsigned& go) {
  asm volatile("s_cmp_lg_u32 %[go], 0\n\ts_cbranch_scc1 1f\n\ts_mov_b64 exec, %[ln]\n\tv_cmp_eq_u32 vcc, -1, v128\n\ts_nop 3\n\ts_bcnt1_i32_b64 %[go], vcc\n\t"
               "s_cmp_le_u32 %[go], %[th]\n\ts_cselect_b32 %[go], 1, 0\n\ts_cbranch_scc1 2f\n\t"
               ".if %[ag]\n\tbuffer_load_dword v128, %[pv], %[rs], %[ps] offen sc1\n.endif\n\ts_mov_b64 exec, -1\n\ts_branch 1f\n"
               "2:\n\ts_mov_b64 exec, -1\n\t" F_LD
               : "+{v[128:131]}"(w[0]), "+{v[132:135]}"(w[1]), [go] "+&s"(go)
               : [vo] "v"(vo), [rs] "s"(r), [so] "s"(so), [s1] "s"(s1), [ln] "s"((1ull << PB) - 1), [th] "n"(AGAIN ? PBT0 : PBT), [ag] "n"(AGAIN),
                 [pv] "v"(pv), [ps] "s"(ps) : "memory", "scc", "vcc");
}
DEV void late_first(v4u (&w)[4], rsrc_t r, unsigned vo, unsigned so, unsigned s1, unsigned go) {
  asm volatile("s_cmp_lg_u32 %[go], 0\n\ts_cbranch_scc1 1f\n\t" F_LD
               : "+{v[128:131]}"(w[0]), "+{v[132:135]}"(w[1]) : [vo] "v"(vo), [rs] "s"(r), [so] "s"(so), [s1] "s"(s1), [go] "s"(go) : "memory", "scc");
}
#define W_REGS(c) c "{v[128:131]}"(w[0]), c "{v[132:135]}"(w[1]), c "{v[136:139]}"(w[2]), c "{v[140:143]}"(w[3])
DEV void land_drain(v4u (&w)[4]) { asm volatile("s_waitcnt vmcnt(0)" : : W_REGS("") : "memory"); }
DEV void land_set(v4u (&w)[4], v4u v) {
  for (int i = 0; i < 4; ++i) w[i] = v;
  asm volatile("" : W_REGS("+") : : "memory");
}
DEV void land_keep(v4u (&w)[4]) { asm volatile("" : : W_REGS("") : "memory"); }
DEV void land_set2(v4u (&w)[4], v4u v) { w[0] = v; w[1] = v; asm volatile("" : "+{v[128:131]}"(w[0]), "+{v[132:135]}"(w[1]) : : "memory"); }
DEV void land_keep2(v4u (&w)[4]) { asm volatile("" : : "{v[128:131]}"(w[0]), "{v[132:135]}"(w[1]) : "memory"); }
struct Watch { unsigned r[4]; };
#define W_STEP(k, sl, op) "s_waitcnt vmcnt(3)\n\tv_and_b32 %[r" #k "], %[mk], %[r" #k "]\n\tv_cmp_" op "_u32 vcc, %[tg], %[r" #k "]\n\ts_nop 3\n\ts_bcnt1_i32_b64 %[bc], vcc\n\ts_cmp_le_u32 %[bc], %[th]\n\ts_cbranch_scc1 9f\n\t" \
  "buffer_load_dword %[r" #k "], %[vo], %[rs], %[so] offen sc1\n\ts_sleep " GX(sl) "\n\t"
#define W_INIT(k, sl) "buffer_load_dword %[r" #k "], %[vo], %[rs], %[so] offen sc1\n\ts_sleep " GX(sl) "\n\t"
#define WATCH(name, sl, op) \
DEV Watch name(rsrc_t r, unsigned vo, unsigned so, unsigned tag, unsigned mask, u64 lanes, unsigned thr, unsigned skip = 0) { \
  Watch w; unsigned cn = POLL_MAX, bc; \
  asm volatile("s_cmp_lg_u32 %[sk], 0\n\ts_cbranch_scc1 9f\n\ts_mov_b64 exec, %[ln]\n\t" W_INIT(0, sl) W_INIT(1, sl) W_INIT(2, sl) W_INIT(3, sl) \
               "0:\n\t" W_STEP(0, sl, op) W_STEP(1, sl, op) W_STEP(2, sl, op) W_STEP(3, sl, op) \
               "s_sub_u32 %[cn], %[cn], 1\n\ts_cbranch_scc0 0b\n9:\n\ts_mov_b64 exec, -1\n\ts_nop 1" \
               : [r0] "=&v"(w.r[0]), [r1] "=&v"(w.r[1]), [r2] "=&v"(w.r[2]), [r3] "=&v"(w.r[3]), [cn] "+&s"(cn), [bc] "=&s"(bc) \
               : [vo] "v"(vo), [rs] "s"(r), [so] "s"(so), [tg] "s"(tag), [mk] "s"(mask), [ln] "s"(lanes), [th] "s"(thr), [sk] "s"(skip) : "memory", "scc", "vcc"); \
  return w; \
}
WATCH(watch_q, DQS, "ne")
WATCH(watch_m, DMS, "ne")
WATCH(watch_t, DTS, "ne")
WATCH(watch_x, DTS, "eq")
DEV void keep_watch(Watch& w) { asm volatile("" : : "v"(w.r[0]), "v"(w.r[1]), "v"(w.r[2]), "v"(w.r[3]) : "memory"); }
DEV void release_watch(Watch& w) { asm volatile("s_waitcnt vmcnt(0)" : : "v"(w.r[0]), "v"(w.r[1]), "v"(w.r[2]), "v"(w.r[3]) : "memory"); }
template <int N, int SL> DEV void gatherp(v4u (&a)[N], rsrc_t r, unsigned vo, unsigned so, unsigned step, unsigned tag) {
#pragma nounroll
  for (int it = 0; it < POLL_MAX; ++it) {
    cfence();
    unsigned bad = 0;
#pragma unroll
    for (int k = 0; k < N; ++k) a[k] = ld_dev(r, vo, so + k * step);
#pragma unroll
    for (int k = 0; k < N; ++k) bad |= (a[k].y ^ tag) | (a[k].w ^ tag);
    if (!__any(bad != 0)) break;
    __builtin_amdgcn_s_sleep(SL);
  }
  cfence();
}

template <int N, int SL, unsigned M> DEV bool gatherf(v4u (&a)[N], rsrc_t r, unsigned vo, unsigned so, unsigned step, v4u tb, int tries = POLL_MAX) {
  bool ok;
#pragma nounroll
  for (int it = 1;; ++it) {
    cfence();
    unsigned bad = 0;
#pragma unroll
    for (int k = 0; k < N; ++k) a[k] = ld_dev(r, vo, so + k * step);
#pragma unroll
    for (int k = 0; k < N; ++k) { a[k] ^= tb; bad |= (a[k].x | a[k].y) | (a[k].z | a[k].w); }
    ok = !__any((bad & M) != 0);
    if (ok || it >= tries) break;
    __builtin_amdgcn_s_sleep(SL);
  }
  cfence();
  return ok;
}

template <int SL, class F> DEV void gather8(v4u (&a)[8], rsrc_t r, unsigned vo, unsigned so, unsigned step, F bad) {
  unsigned need = 255u;
#pragma nounroll
  for (int it = 0; it < POLL_MAX; ++it) {
    cfence();
#pragma unroll
    for (int k = 0; k < 8; ++k) if (need & (1u << k)) a[k] = ld_dev(r, vo + (k & 3) * 1024, so + (k >> 2) * step);
    unsigned nn = 0;
#pragma unroll
    for (int k = 0; k < 8; ++k) nn |= __any(bad(a[k])) ? 1u << k : 0u;
    need = nn;
    if (!need) break;
    __builtin_amdgcn_s_sleep(SL);
  }
  cfence();
}

template <int CTRL> DEV float dpp(float x) { return __builtin_bit_cast(float, __builtin_amdgcn_mov_dpp(__builtin_bit_cast(int, x), CTRL, 0xf, 0xf, true)); }
DEV float x16sum(float x) { auto s = __builtin_amdgcn_permlane16_swap(fbits(x), fbits(x), false, false); return bitsf(s[0]) + bitsf(s[1]); }
DEV float x32sum(float x) { auto s = __builtin_amdgcn_permlane32_swap(fbits(x), fbits(x), false, false); return bitsf(s[0]) + bitsf(s[1]); }
DEV float x16max(float x) { auto s = __builtin_amdgcn_permlane16_swap(fbits(x), fbits(x), false, false); return fmaxf(bitsf(s[0]), bitsf(s[1])); }
DEV float x32max(float x) { auto s = __builtin_amdgcn_permlane32_swap(fbits(x), fbits(x), false, false); return fmaxf(bitsf(s[0]), bitsf(s[1])); }
DEV float wave_sum(float x) {
  x += dpp<0xB1>(x); x += dpp<0x4E>(x); x += dpp<0x141>(x); x += dpp<0x140>(x);
  return x32sum(x16sum(x));
}
DEV float s32(float a, float b) { auto s = __builtin_amdgcn_permlane32_swap(fbits(a), fbits(b), false, false); return bitsf(s[0]) + bitsf(s[1]); }
DEV float s16(float a, float b) { auto s = __builtin_amdgcn_permlane16_swap(fbits(a), fbits(b), false, false); return bitsf(s[0]) + bitsf(s[1]); }
DEV float sum8up(float x) { x += dpp<0x128>(x); return x32sum(x16sum(x)); }
DEV float max8up(float x) { x = fmaxf(x, dpp<0x128>(x)); return x32max(x16max(x)); }
DEV v4u dup_lo(v4u v) { v4u o; for (int i = 0; i < 4; ++i) o[i] = __builtin_amdgcn_update_dpp(v[i], v[i], 0x128, 0xf, 0xc, false); return o; }
DEV v4u dup_hi(v4u v) { v4u o; for (int i = 0; i < 4; ++i) o[i] = __builtin_amdgcn_update_dpp(v[i], v[i], 0x128, 0xf, 0x3, false); return o; }
DEV float fdiv(float a, float d, float rd) { float q = a * rd; return fmaf(fmaf(-q, d, a), rd, q); }
DEV float dot2(unsigned a, unsigned b, float c) { return __builtin_amdgcn_fdot2_f32_bf16(__builtin_bit_cast(v2bf, a), __builtin_bit_cast(v2bf, b), c, false); }
struct Stat { float mb, sd, rd; };
DEV Stat mk_stat(float m, float var) {
  Stat st;
  st.mb = rbf(m);
  st.sd = rbf(__builtin_amdgcn_sqrtf(rbf(rbf(var) + 1e-5f)));
  st.rd = __builtin_amdgcn_rcpf(st.sd);
  return st;
}
DEV Stat ln_stat8(const unsigned (&w)[8]) {
  float s = 0.f, s2 = 0.f, q = 0.f, q2 = 0.f;
#pragma unroll
  for (int i = 0; i < 8; i += 2) {
    s = dot2(w[i], 0x3f803f80u, s); q = dot2(w[i], w[i], q);
    s2 = dot2(w[i + 1], 0x3f803f80u, s2); q2 = dot2(w[i + 1], w[i + 1], q2);
  }
  float t = s32(s + s2, q + q2);
  t += dpp<0xB1>(t); t += dpp<0x4E>(t); t += dpp<0x141>(t); t += dpp<0x140>(t);
  t = x16sum(t) * (1.f / 1024.f);
  auto mq = __builtin_amdgcn_permlane32_swap(fbits(t), fbits(t), false, false);
  const float m = bitsf(mq[0]);
  float var = fmaf(-m, m, bitsf(mq[1]));
  if (__builtin_expect(__builtin_amdgcn_ballot_w64(!(m * m <= 16.f * var)) != 0, 0)) {
    q = 0.f;
#pragma unroll
    for (int i = 0; i < 8; ++i) { float a = bflo(w[i]) - m, b = bfhi(w[i]) - m; q = fmaf(a, a, q); q = fmaf(b, b, q); }
    var = wave_sum(q) * (1.f / 1024.f);
  }
  return mk_stat(m, var);
}
DEV unsigned ln_pair(unsigned x, const Stat& st) {
  unsigned d = pk(bflo(x) - st.mb, bfhi(x) - st.mb);
  return pk(bflo(d) * st.rd, bfhi(d) * st.rd);
}
DEV unsigned onep(unsigned c) { return pk(1.f + bflo(c), 1.f + bfhi(c)); }
DEV unsigned mod_pair(unsigned ln, unsigned c1, unsigned s) {
  unsigned p = pk(bflo(ln) * bflo(c1), bfhi(ln) * bfhi(c1));
  return pk(bflo(p) + bflo(s), bfhi(p) + bfhi(s));
}
DEV unsigned sopq(unsigned a) { asm("" : "+s"(a)); return a; }
DEV float fopq(float a) { asm volatile("" : "+v"(a)); return a; }
DEV unsigned res1(float x, unsigned g, float a, float zero) {
  const float p = dot2(pk(a, a), g, zero);
  const float r = dot2(pk(p, p), sopq(0x3f80u), x);
  return pk(r, r);
}
DEV float silu1(float v) {
  const float d = 1.f + __expf(-v);
  return __builtin_amdgcn_div_fixupf(fdiv(v, d, __builtin_amdgcn_rcpf(d)), d, v);
}

#define STAMP(k)
#define STAMP_DUMP()

#define FIN() do { STAMP_DUMP(); __builtin_amdgcn_endpgm(); } while (0)
DEV float unit_reduce(v4f a) {
  const float r = s16(s32(a[0], a[2]), s32(a[1], a[3]));
  return r + dpp<0x128>(r);
}
DEV void wg_barrier() { asm volatile("s_waitcnt lgkmcnt(0)\n\ts_barrier" ::: "memory"); }

typedef u64 v4l __attribute__((ext_vector_type(4)));
#define CPF 1
__global__ void __launch_bounds__(NTHR, 3)
fused_kernel(const void*, const void*, const void*, const void*, const void*, const void*, const void*, const void*, const void*, const void*,
             void*, void*) {
  const int lane = threadIdx.x & 63;
  const int wave = __builtin_amdgcn_readfirstlane(threadIdx.x >> 6);
  const int wg = blockIdx.x;
  const int n = lane & 15, g = lane >> 4;
  v4l a0, a1, a2;
  unsigned cpf;
  asm volatile("s_load_dwordx8 %0, %3, 0x0\n\ts_load_dwordx8 %1, %3, 0x20\n\ts_load_dwordx8 %2, %3, 0x40"
               : "=&s"(a0), "=&s"(a1), "=&s"(a2) : "s"(__builtin_amdgcn_kernarg_segment_ptr()));
  asm volatile("s_getpc_b64 s[92:93]\n.Lfk_a:\n\ts_and_b32 s93, s93, 0xffff\n\ts_cmp_eq_u32 %3, 0\n\ts_cselect_b32 s94, .Lfk_z-.Lfk_a, 0\n\t"
               "s_mov_b32 s95, 0x20000\n\tbuffer_load_dword %0, %1, s[92:95], %2 offen"
               : "=v"(cpf) : "v"(lane * 128), "s"((wg & 56) * 1024), "s"(CPF ? wave : 1) : "s92", "s93", "s94", "s95", "scc");
  asm volatile("s_waitcnt lgkmcnt(0)" : "+s"(a0), "+s"(a1), "+s"(a2));
  const char *px = (const char*)a0[0], *pk_ = (const char*)a0[1], *pv = (const char*)a0[2], *pwq = (const char*)a0[3];
  const char *pwo = (const char*)a1[0], *pwg = (const char*)a1[1], *pwu = (const char*)a1[2], *pwd = (const char*)a1[3];
  const char *pc = (const char*)a2[0], *pwm = (const char*)a2[1], *pout = (const char*)a2[2], *pws = (const char*)a2[3];
  const rsrc_t rws = mk(pws, WS_BYTES);
  const int bk = wg & 31, sp = wg >> 5;
  const int ab = bk >> 2, kvh = bk & 3;
  const bool first = wg < 32;
  const int xj = wg >> 3, xc = wg & 7;
  const bool rW = xj >= 24, rB = xj < 2, rG = xj >= 5 && xj < 19, rD = !rW && !rB && !rG;
  const int wc = xc * 8 + xj - 24, oh = wc >> 1;
  const int gl = rB ? wg : xc * 14 + xj - 5, gks = gl & 1;
  const int dl = xc * 7 + xj - (xj < 5 ? 2 : 17), dft = dl >> 2, dj = dl & 3;

  STAMP(0);
  const int tq = (lane >> 2) & 3, tp = lane & 3;
  const bool hi = n >= 8;
  const unsigned sw64 = (((lane & 7) ^ (((lane >> 4) & 1) << 1)) << 4);
  unsigned ep;

  if (wave < 8) {
    const rsrc_t rk = mk((const char*)pk_ + (size_t)bk * 2097152 + sp * 262144 + wave * 32768, 32768);
    const rsrc_t rv = mk((const char*)pv + (size_t)bk * 2097152 + sp * 262144 + wave * 32768, 32768);
    const unsigned rot = wave + wg;
    const unsigned t0 = (unsigned)__builtin_amdgcn_s_memrealtime();
    if (first) {
      asm volatile("s_barrier" ::: "memory");
      lds_wait<8>(F_GO, 4);
    }
    bool ahead = rG && (GA >> ((gl >> 2) & 3) & 1);
    int late;
    unsigned due = t0 + C0 + (KA0 - 1) * TAU - (ahead ? CF : 0);
    auto pace = [&]() __attribute__((always_inline)) {
      SCHED();
      late = (int)((unsigned)__builtin_amdgcn_s_memrealtime() - due);
      if (PL)
        asm volatile("s_setprio 0\n\ts_cmp_lt_i32 %0, %1\n\ts_cbranch_scc1 1f\n\ts_setprio 1\n\ts_cmp_lt_i32 %0, %2\n\ts_cbranch_scc1 1f\n\ts_setprio 2\n1:"
                     : : "s"(late), "n"(PL), "n"(PL2) : "scc");
      if (late <= 0) vmwait<WS - 1>();
      if (WX > WB) { if (late < LX) vmwait<WB - 1>(); vmwait<WX - 1>(); } else vmwait<WB - 1>();
      due += TAU;
      SCHED();
    };
    v4u kf[8][4];
    const unsigned kva = ((n & 3) * 4 + ((n >> 2) & 1)) * 256 + g * 16 + (n >> 3) * 64, kvb = kva ^ 64;
#pragma unroll
    for (int i = 0; i < 32; ++i) {
      const int o = i >> 2, t = (o & 4) | ((o & 1) << 1) | ((o >> 1) & 1);
      if (i == WS) {
        if (!first) asm volatile("s_barrier" ::: "memory");
        asm volatile("s_load_dword %0, %1, 0x0" : "=s"(ep) : "s"(pws) : "memory");
      }
      if (i >= KA0) pace(); else if (i >= WS) vmwait<WS - 1>();
      LD_NT(kf[t][i & 3], rk, (t & 2) ? kvb : kva, ((t + rot) & 7) * 4096, ((i >> 1) & 1) * 512 + (i & 1) * 128);
    }
    asm volatile("" : : "v"(cpf));
    STAMP(1);
    const unsigned vvo = lane * 16;
    lds_wait<4>(F_HEAD, 4);
#pragma nounroll
    for (int i = 0; i < 16; ++i) { pace(); dma16(rv, vvo, (((i >> 2) + rot) & 7) * 4096 + (i & 3) * 1024, i * 8192 + wave * 1024); }
    STAMP(3);
    lds_wait<2>(F_Q, 1);
    asm volatile("s_waitcnt vmcnt(16) lgkmcnt(0)" : "+s"(ep) : : "memory");
#pragma unroll
    for (int t = 0; t < 8; t += 2) TIE8(kf[t][0], kf[t][1], kf[t][2], kf[t][3], kf[t + 1][0], kf[t + 1][1], kf[t + 1][2], kf[t + 1][3]);
    const v4u tb = tagbytes(ep + 0x01010101u);
    const unsigned tg = opq(ep + 0x01010101u);
    STAMP(2);
    v4u qf[2];
    {
      const unsigned qa = opq(L_QB + (n & 7) * 288 + (n >> 3) * 64 + g * 16);
      qf[0] = lds_ld<v4u>(qa); qf[1] = lds_ld<v4u>(qa + 128);
    }
    v4u vf[4][8];
    auto vload = [&](int i) __attribute__((always_inline)) {
      pace();
      LD_NT(vf[2 + (i >> 3)][i & 7], rv, vvo, ((4 + (i >> 2) + rot) & 7) * 4096, (i & 3) * 1024);
      SCHED();
    };
    unsigned sc[4][2];
    float mx = -INFINITY;
    const bool va = (g >> 1) == (n >> 3);
#pragma unroll
    for (int q = 0; q < 4; ++q) {
      const int ta = (q >> 1) * 4 + (q & 1), tbb = ta + 2;
      const v4f z = {0.f, 0.f, 0.f, 0.f};
      v4f a0 = mfma(kf[ta][0], qf[0], z), b0 = mfma(kf[tbb][0], qf[0], z), a1 = mfma(kf[ta][2], qf[0], z), b1 = mfma(kf[tbb][2], qf[0], z);
      a0 = mfma(kf[ta][1], qf[1], a0); b0 = mfma(kf[tbb][1], qf[1], b0); a1 = mfma(kf[ta][3], qf[1], a1); b1 = mfma(kf[tbb][3], qf[1], b1);
      if (q & 1) vload(q >> 1);
      const v4f e0 = sel4(va, a0, b0), e1 = sel4(va, a1, b1);
      float s[4];
#pragma unroll
      for (int r = 0; r < 4; ++r) {
        auto w = __builtin_amdgcn_permlane32_swap(fbits(e0[r]), fbits(e1[r]), false, false);
        s[r] = bitsf(w[0]) + dpp<0x128>(bitsf(w[1]));
      }
#pragma unroll
      for (int r = 0; r < 2; ++r) {
        unsigned u = pk(s[2 * r], s[2 * r + 1]);
        u = pk(bflo(u) * 0.08838834764831845f, bfhi(u) * 0.08838834764831845f);
        sc[q][r] = u;
        mx = fmaxf(mx, fmaxf(bflo(u), bfhi(u)));
      }
    }
    mx = max8up(mx);
    float ls = 0.f;
    v4u pf[4];
#pragma unroll
    for (int b = 0; b < 2; ++b) {
      v4u p;
#pragma unroll
      for (int e = 0; e < 4; ++e) {
        const unsigned u = sc[2 * b + (e >> 1)][e & 1];
        const float p0 = __expf(bflo(u) - mx), p1 = __expf(bfhi(u) - mx);
        ls += p0 + p1;
        p[e] = pk(p0, p1);
      }
      pf[2 * b] = dup_lo(p); pf[2 * b + 1] = dup_hi(p);
    }
    ls = sum8up(ls);
    if (lane < 8) { lds_st<float>(L_MX + wave * 32 + lane * 4, mx); lds_st<float>(L_LS + wave * 32 + lane * 4, ls); }
    lds_signal(F_KML, lane);
    STAMP(4);
    v4f acc[8];
    auto piece = [&](int p, int i, bool first) __attribute__((always_inline)) {
      v4u a;
#pragma unroll
      for (int e = 0; e < 4; ++e) a[e] = __builtin_amdgcn_perm(vf[p][2 * e + 1][i >> 1], vf[p][2 * e][i >> 1], (i & 1) ? 0x07060302u : 0x05040100u);
      acc[i] = mfma(a, pf[p], first ? (v4f){0.f, 0.f, 0.f, 0.f} : acc[i]);
    };
    auto tie = [&](int p) __attribute__((always_inline)) { TIE8(vf[p][0], vf[p][1], vf[p][2], vf[p][3], vf[p][4], vf[p][5], vf[p][6], vf[p][7]); };
    const unsigned wp = rB ? 12288 : rG ? 7168 : 2048;
    const rsrc_t rt = mk(rD ? pwd : rB ? pwm : (gl & 2) ? pwu : pwg, (rD ? 3584 : 1024) * wp);
    const unsigned wsh = rD ? 3 : 4;
    const unsigned wb = rD ? (dft * 256 + wave * 4) * wp + dj * 512 : (gks * 512 + wave * 4) * wp + (rB ? 10240 + (gl >> 1) * 256 : (gl >> 2) * 256);
    unsigned wv = g * wp + ((n ^ (g << 1) ^ ((wave & 2) << 2)) << 4), wm = (1u << wsh) - 1, w32 = 32 * wp;
    asm volatile("" : "+v"(wv), "+s"(wm), "+s"(w32));
    auto wpiece = [&](unsigned f) __attribute__((always_inline)) { dma16(rt, wv, wb + (f & wm) * w32 + (f >> wsh) * 256, f * 8192 + wave * 1024); };
    v4u wf[16];
    const unsigned tw = (8 * g + tq) * 256 + (((2 * wave + (tp >> 1)) ^ ((tq | ((g & 1) << 2)) << 1)) << 4) + 8 * (tp & 1);
    auto wfrag = [&](int f0, int f1) __attribute__((always_inline)) {
      const unsigned b0 = opq(tw), b1 = opq(tw + 65536);
#pragma unroll
      for (int f = f0; f < f1; ++f) wf[f] = trfrag((f & 8 ? b1 : b0) + (f & 7) * 8192, 1024);
    };
    const int hd = n & 7, ci = lane & 31;
    unsigned wa0, wa1, ra[4];
    float fw;
    v4u ml[1];
    const unsigned mlo = (lane & 1) * 64 + (lane >> 4) * 16 + (rW ? 0u : 0x80000000u), mls = rW ? O_ML + oh * 1024 + wave * 128 : 0;
    auto prep = [&]() __attribute__((always_inline)) {
      wa0 = opq(L_MG + ((hd >> 1) | ((g & 1) << 2) | ((n >> 3) << 3) | ((g >> 1) << 4)) * 256 + ((wave ^ ((hd >> 1) | ((n >> 3) << 2))) << 5) + (hd & 1) * 16);
      wa1 = opq(wa0 ^ 16);
      const unsigned r0 = L_MG + ((wave >> 1) | (((ci >> 3) & 1) << 2) | (((ci >> 2) & 1) << 3) | ((ci >> 4) << 4) | ((ci & 3) << 5)) * 256 +
                          ((((ci >> 1) & 3) | ((((lane >> 5) ^ (ci >> 3)) & 1) << 2)) << 5) + ((wave ^ ci) & 1) * 16;
#pragma unroll
      for (int k = 0; k < 4; ++k) ra[k] = opq(r0 ^ (k << 5));
      lds_wait<1>(F_WT, 8);
      fw = __expf(mx - max8up(lds_ld<float>(opq(L_MX + lane * 4))));
      SCHED();
    };
    {
      const unsigned vl = opq(wave * 1024 + lane * 16), vl2 = opq(65536 + wave * 1024 + lane * 16);
#pragma unroll
      for (int i = 2; i < 16; ++i) {
        vload(i);
        if (i == 3 || i == 9) {
          const int p = i == 9;
          if (p) vmwait<10>(); else vmwait<12>();
#pragma unroll
          for (int j = 0; j < 8; ++j) vf[p][j] = lds_ld<v4u>((p ? vl2 : vl) + j * 8192);
        }
        if (i >= 4 && i < 8) { piece(0, 2 * i - 8, true); piece(0, 2 * i - 7, true); }
        if (i == 10) { tie(1); lds_signal(F_WT, lane); }
        if (i >= 10 && i < 14) { piece(1, 2 * i - 20, false); piece(1, 2 * i - 19, false); }
      }
      asm volatile("buffer_load_dwordx4 %0, %1, %2, %3 offen sc1" : "=v"(ml[0]) : "v"(mlo), "s"(rws), "s"(mls) : "memory");
      SCHED();
      vmwait<9>();
      tie(2);
#pragma unroll
      for (int k = 0; k < 8; ++k) piece(2, k, false);
      SCHED();
      prep();
      if (rG && late > TL) ahead = true;
      if (GB && ahead) {
#pragma unroll
        for (int f = 0; f < GB; ++f) wpiece(f);
        vmwait<GB + 1>();
      } else {
        vmwait<1>();
      }
      tie(3);
#pragma unroll
      for (int k = 0; k < 8; ++k) piece(3, k, false);
    }
    STAMP(5);
    {
#pragma unroll
      for (int i = 0; i < 4; ++i) lds_st<v4f>(((i & 1) ? wa1 : wa0) + i * 8192, sel4(hi, acc[i + 4], acc[i]) * fw);
      wg_barrier();
      v4f s = (lds_ld<v4f>(ra[0]) + lds_ld<v4f>(ra[1])) + (lds_ld<v4f>(ra[2]) + lds_ld<v4f>(ra[3]));
#pragma unroll
      for (int i = 0; i < 4; ++i) s[i] = x32sum(s[i]);
      if (lane < 32) st_dev2((v2u){canon(pk(s[0], s[1])), canon(pk(s[2], s[3]))}, rws, ci * 8, O_AO + (kvh * 8 + wave) * 16384 + ab * 2048 + sp * 256);
    }
    STAMP(6);
    v4u la[4];
    const v4f z = {0.f, 0.f, 0.f, 0.f};
    if (rW) {
      land_set(la, (v4u){SENT, SENT, SENT, SENT});
      const int un = wc * 4 + wave;
      const unsigned uo = ((lane >> 2) & 1) * 2048 + (lane & 3) * 8, us = (un >> 6) * 4096 + (un & 63) * 32;
      const unsigned oo = opq(uo + g * 2), xo = opq(uo + (lane >> 3) * 16384);
      v4u gv;
      unsigned xr;
      {
        const unsigned wa = opq(L_AX + wave * 288 + n * 16 + g * 4);
        asm volatile("s_waitcnt vmcnt(1)" : "+v"(ml[0]) : : "memory");
        ml[0] ^= tb;
        if (__builtin_expect(__any((((ml[0].x | ml[0].y) | (ml[0].z | ml[0].w)) & 255u) != 0), 0)) gatherf<1, 4, 255u>(ml, rws, mlo, mls, 0, tb);
        float M = fmaxf(bitsf(ml[0].x), dpp<0xB1>(bitsf(ml[0].x)));
        M = x32max(x16max(M));
        const float e = __expf(bitsf(ml[0].x) - M);
        float L = bitsf(ml[0].y) * e;
        L = x32sum(x16sum(L + dpp<0xB1>(L)));
        const float f = e / L;
        float f0 = dpp<0xA0>(f), f1 = dpp<0xF5>(f);
        asm volatile("" : "+v"(f0), "+v"(f1));
        const unsigned far = wave < 4 ? 0u : 0x80000000u;
        asm volatile("buffer_load_dwordx4 %0, %2, %3, %4 offen sc1\n\tbuffer_load_ushort %1, %5, %6, %7 offen"
                     : "=&v"(gv), "=&v"(xr) : "v"(lane * 16 + far), "s"(rws), "s"(O_GX + (un & 255) * 2048), "v"(oo + far), "s"(mk(px, 16384)),
                       "s"(us & 16383) : "memory");
        if (D1) __builtin_amdgcn_s_sleep(D1);
        v4u a[2];
        poll2s<TH1, false>(a, la, rws, lane * 16, O_AO + oh * 16384 + wave * 2048, O_AO + oh * 16384 + wave * 2048 + 1024);
        STAMP(7);
        float lo[4], up[4];
#pragma unroll
        for (int i = 0; i < 4; ++i) {
          lo[i] = bflo(a[0][i]) * f0 + bflo(a[1][i]) * f1;
          up[i] = bfhi(a[0][i]) * f0 + bfhi(a[1][i]) * f1;
        }
        lds_st<unsigned>(wa, pk(s16(s32(lo[0], lo[2]), s32(lo[1], lo[3])), s16(s32(up[0], up[2]), s32(up[1], up[3]))));
        wg_barrier();
      }
      if (wave < 4) {
        auto gate = [&](int k, int tries, bool& ok) __attribute__((always_inline)) {
          v4u a[1];
          ok = gatherf<1, 4, 255u>(a, rws, (lane & (k ? 15 : 63)) * 16, O_GX + un * 2048 + k * 1024, 0, tb, tries);
          return pk(unit_reduce(asf4(a[0])) * (k ? 0.25f : 1.f), 0.f);
        };
        land_drain(la);
        asm volatile("" : "+v"(gv), "+v"(xr));
        float xe = bflo(xr);
        gv ^= tb;
        bool ok = !__any((((gv.x | gv.y) | (gv.z | gv.w)) & 255u) != 0);
        unsigned gt = pk(unit_reduce(asf4(gv)), 0.f);
        land_set(la, (v4u){SENT, SENT, SENT, SENT});
        xe = fopq(xe);
        gt = fbits(fopq(bitsf(gt)));
        float zero = fopq(0.f);
        STAMP(8);
        if (D2) __builtin_amdgcn_s_sleep(D2);
        v4u p[2];
        poll2s<TH2, false>(p, la, rws, lane * 16, O_WP + un * 2048, O_WP + un * 2048 + 1024);
        STAMP(9);
        float s;
        {
          float r[2];
#pragma unroll
          for (int i = 0; i < 2; ++i)
            r[i] = s16(s32(bflo(p[0][2 * i]) + bflo(p[1][2 * i]), bflo(p[0][2 * i + 1]) + bflo(p[1][2 * i + 1])),
                       s32(bfhi(p[0][2 * i]) + bfhi(p[1][2 * i]), bfhi(p[0][2 * i + 1]) + bfhi(p[1][2 * i + 1])));
          const bool r1 = lane & 4;
          s = (r1 ? r[1] : r[0]) + dpp<0x124>(r1 ? r[0] : r[1]);
          s += dpp<0x128>(s);
        }
        if (__builtin_expect(!ok, 0)) gt = gate(0, POLL_MAX, ok);
        const unsigned x1 = res1(xe, gt, s, zero);
        auto h = __builtin_amdgcn_permlane16_swap(x1, x1, false, false);
        const unsigned x2 = canon(__builtin_amdgcn_perm(h[1], h[0], 0x07060100u));
        h = __builtin_amdgcn_permlane32_swap(x2, x2, false, false);
        st_dev2((v2u){h[0], h[1]}, rws, xo, O_X1 + us);
        STAMP(10);
        __builtin_amdgcn_s_sleep(D5);
        gt = gate(1, G5, ok);
        land_drain(la);
        land_set(la, ~tb);
        xe = fopq(bflo(x1));
        gt = fbits(fopq(bitsf(gt)));
        zero = fopq(0.f);
        const rsrc_t ro = mk(pout, 16384);
        STAMP(11);
        v4u c[2];
        Watch w5 = {};
        if (WL5) w5 = watch_t(rws, lane * 512 + (un & 1) * 128, O_DP + un * 2048, tb.x, 255u, (1ull << WL5) - 1, WT5);
        poll2<TH5>(c, la, rws, lane * 16, O_DP + un * 2048, tb);
        keep_watch(w5);
        STAMP(12);
        s = unit_reduce(asf4(c[0]) + sel4(lane >= 48, z, asf4(c[1])));
        if (__builtin_expect(!ok, 0)) gt = gate(1, POLL_MAX, ok);
        const unsigned o = res1(xe, gt, s, zero);
        if (n < 8) __builtin_amdgcn_raw_buffer_store_b16((unsigned short)o, ro, oo, us, 0);
        if (un == 0 && lane == 0) __builtin_amdgcn_raw_buffer_store_b32(ep + 0x01010101u, rws, 0, WS_EPOCH, 16);
        STAMP(15);
      }
      land_keep(la);
      FIN();
    } else if (xj != 19) {
      const unsigned px_ = ((n >> 1) & 3) * 131072 + (n & 1) * 64 + g * 16;
      auto t3mm = [&](unsigned b3, unsigned po, unsigned pso) __attribute__((always_inline)) {
        v4u b[8];
#pragma unroll
        for (int kb = 0; kb < 8; ++kb) b[kb] = lds_ld<v4u>(b3 + kb * 64);
        SCHED();
        v4f d0 = z, d1 = z;
#pragma unroll
        for (int kb = 0; kb < 8; ++kb) { d0 = mfma(wf[kb], b[kb], d0); d1 = mfma(wf[8 + kb], b[kb], d1); }
#pragma unroll
        for (int i = 0; i < 4; ++i) d0[i] += dpp<0x128>(d1[i]);
        if (n < 8) st_dev(tagv(d0, tg), rws, po, pso);
        STAMP(12);
      };
      if (rG) {
        asm volatile("s_barrier" ::: "memory");
#pragma nounroll
        for (int f = ahead ? GB : 0; f < 16; ++f) { vmwait<GW - 1>(); wpiece(f); }
        const unsigned b3 = opq(L_AX + (n & 7) * 1056 + (n >> 3) * 528 + g * 16);
        const unsigned po = opq(n * 64 + g * 16);
        const unsigned pso = O_GP + (gl >> 3) * 32768 + (((gl >> 2) & 1) * 4 + (wave >> 1)) * 4096 + (gks * 2 + ((gl >> 1) & 1)) * 1024 + (wave & 1) * 512;
        const unsigned wa = opq(L_AX + wave * 1056 + lane * 16 + (lane >> 5) * 16), xo = O_X1 + xc * 16384 + wave * 2048;
        const unsigned sa_ = opq(L_SB + wave * 1024 + lane * 16);
        const v4u sbv = lds_ld<v4u>(sa_), cbv = lds_ld<v4u>(sa_ + 8192);
        float sf[8], pf_[8];
        unsigned cm[8];
#pragma unroll
        for (int e = 0; e < 4; ++e) {
          sf[2 * e] = fopq(bflo(sbv[e])); sf[2 * e + 1] = fopq(bfhi(sbv[e]));
          cm[2 * e] = fbits(fopq(bitsf(cbv[e] & 0xffffu))); cm[2 * e + 1] = fbits(fopq(bfhi(cbv[e])));
          pf_[2 * e] = fopq(0.f); pf_[2 * e + 1] = fopq(0.f);
        }
        const unsigned k10 = sopq(0x3f80u), k01 = sopq(0x3f800000u);
        vmwait<PW>();
        const unsigned pv = opq(lane * (2048 / PB) + (xj & 3) * 64);
        unsigned go = 0;
        probe_ld(la, rws, pv, xo);
        vmwait<0>();
        probe_first<true>(la, rws, lane * 16, xo + gks * 1024, xo + 1024 - gks * 1024, pv, xo, go);
        asm volatile("s_barrier" ::: "memory");
        STAMP(7);
        wfrag(0, 16);
        cfence();
        vmwait<0>();
        probe_first<false>(la, rws, lane * 16, xo + gks * 1024, xo + 1024 - gks * 1024, pv, xo, go);
        Watch w3 = watch_x(rws, lane * (2048 / WL3) + (xj & 3) * 128, xo, SENT, SENT, (1ull << WL3) - 1, WT3, go);
        late_first(la, rws, lane * 16, xo + gks * 1024, xo + 1024 - gks * 1024, go);
        {
          v4u xv[2];
          poll2s<TH3, true>(xv, la, rws, lane * 16, xo + gks * 1024, xo + 1024 - gks * 1024);
          keep_watch(w3);
          const unsigned xr[8] = {xv[0].x, xv[0].y, xv[0].z, xv[0].w, xv[1].x, xv[1].y, xv[1].z, xv[1].w};
          STAMP(11);
          const Stat st = ln_stat8(xr);
          v4u h2;
#pragma unroll
          for (int e = 0; e < 4; ++e) h2[e] = pk(bflo(xr[e]) - st.mb, bfhi(xr[e]) - st.mb);
          SCHED();
#pragma unroll
          for (int e = 0; e < 4; ++e) h2[e] = pk(bflo(h2[e]) * st.rd, bfhi(h2[e]) * st.rd);
          SCHED();
#pragma unroll
          for (int e = 0; e < 8; ++e) pf_[e] = dot2(h2[e >> 1], cm[e], pf_[e]);
          SCHED();
#pragma unroll
          for (int e = 0; e < 4; ++e) h2[e] = pk(pf_[2 * e], pf_[2 * e + 1]);
          SCHED();
#pragma unroll
          for (int e = 0; e < 8; ++e) sf[e] = dot2(h2[e >> 1], (e & 1) ? k01 : k10, sf[e]);
          SCHED();
#pragma unroll
          for (int e = 0; e < 4; ++e) h2[e] = pk(sf[2 * e], sf[2 * e + 1]);
          lds_st<v4u>(wa, h2);
          wg_barrier();
        }
        t3mm(b3, po, pso);
        land_keep2(la);
        FIN();
      }
      asm volatile("s_barrier" ::: "memory");
      if (DT) {
        const unsigned tt = t0 + DT;
#pragma nounroll
        for (int i = 0; i < 2000 && (int)((unsigned)__builtin_amdgcn_s_memrealtime() - tt) < 0; ++i) __builtin_amdgcn_s_sleep(2);
      }
      if (DD) __builtin_amdgcn_s_sleep(DD);
#pragma nounroll
      for (int f = 0; f < 16; ++f) { vmwait<DW - 1>(); wpiece(f); }
      vmwait<0>();
      asm volatile("s_barrier" ::: "memory");
      STAMP(7);
      if (rB) {
        wfrag(0, 16);
        t3mm(L_AX + (n & 7) * 1056 + (n >> 3) * 528 + g * 16, px_, O_GX + ((gl >> 1) * 8 + wave) * 2048 + (8 + gks) * 128);
        FIN();
      } else {
        cfence();
        wfrag(0, 16);
        const unsigned pd = opq(((n >> 1) & 3) * 131072 + (n >> 3) * 16384 + (n & 1) * 64 + g * 16);
        const unsigned wa = opq(L_AX + ((lane >> 2) & 7) * 544 + wave * 64 + (wave >> 2) * 16 + (lane >> 5) * 32 + (lane & 3) * 8);
        const unsigned bb = opq(L_AX + (n & 7) * 544 + (n >> 3) * 272 + g * 16);
        land_set(la, ~tb);
        float hf[4];
#pragma unroll
        for (int e = 0; e < 4; ++e) hf[e] = fopq(0.f);
        if (D4) __builtin_amdgcn_s_sleep(D4);
        v4u a[4];
        Watch w4 = {};
        if (WL4) w4 = watch_t(rws, lane * (4096 / (WL4 + !WL4)), O_GP + dft * 32768 + wave * 4096, tb.x, 255u, (1ull << WL4) - 1, WT4);
        poll4<TH4>(a, la, rws, lane * 16, O_GP + dft * 32768 + wave * 4096, tb);
        keep_watch(w4);
        STAMP(13);
        const v4f tgt = asf4(a[0]) + asf4(a[2]), tu = asf4(a[1]) + asf4(a[3]);
#pragma unroll
        for (int e = 0; e < 4; ++e) {
          const float r = __builtin_elementwise_maximum(tgt[e], 0.f);
          hf[e] = dot2((e & 1) ? pk(0.f, r) : pk(r, 0.f), pk(tu[e & 2], tu[e | 1]), hf[e]);
        }
        lds_st<v2u>(wa, (v2u){pk(hf[0], hf[1]), pk(hf[2], hf[3])});
        wg_barrier();
        v4u b[4];
#pragma unroll
        for (int kb = 0; kb < 4; ++kb) b[kb] = lds_ld<v4u>(bb + kb * 64);
        SCHED();
        v4f d[4];
#pragma unroll
        for (int kb = 0; kb < 4; ++kb)
#pragma unroll
          for (int t = 0; t < 4; ++t) d[t] = mfma(wf[4 * t + kb], b[kb], kb ? d[t] : z);
        const v4f x = sel4(hi, d[1], d[2]);
        v4f y = sel4(hi, d[3], d[0]);
#pragma unroll
        for (int i = 0; i < 4; ++i) y[i] += dpp<0x128>(x[i]);
        st_dev(tagv(y, tg), rws, pd, O_DP + (dj * 16 + wave) * 2048 + dft * 128);
        STAMP(14);
        land_keep(la);
        FIN();
      }
    }
    FIN();
  }

  const int cw = wave - 8;
  __builtin_amdgcn_s_setprio(3);
  unsigned cb_n = 0;
  const bool jq = !first && wg < 160;
  const int ks = wg & 1, jt = (wg - 32) >> 1;
  const int mt = MAP == 2 ? 2 * (1 - (xc >> 2) + 2 * (4 * ((xc >> 1) & 1) + (xj & 3))) + (xc & 1)
               : MAP == 3 ? (first ? wg : (wg + 2) & 31) : MAP == 4 ? (first ? wg : (wg + 4) & 31) : wg & 31;
  if (cw == 0 && lane < 32) lds_st<unsigned>(L_FL + 4 * lane, 0u);
  const unsigned h0 = (unsigned)__builtin_amdgcn_s_memrealtime();
  auto later = [&]() __attribute__((always_inline)) {
#pragma nounroll
    for (int i = 0; i < 1000 && (int)((unsigned)__builtin_amdgcn_s_memrealtime() - h0 - SD) < 0; ++i) __builtin_amdgcn_s_sleep(4);
  };
  auto early = [&]() __attribute__((always_inline)) { return (int)((unsigned)__builtin_amdgcn_s_memrealtime() - h0 - QT) < 0; };
  auto softmax_stats = [&](unsigned TAG) __attribute__((always_inline)) {
    lds_wait<8>(F_KML, 8);
    const float mw = lds_ld<float>(L_MX + lane * 4), lw = lds_ld<float>(L_LS + lane * 4);
    const float ms = max8up(mw);
    const float lsum = sum8up(lw * __expf(mw - ms));
    if (lane < 8) st_dev(tag4((v4f){ms, lsum, ms, lsum}, tagbytes(TAG)), rws, lane * 1024, O_ML + kvh * 8192 + ab * 128 + sp * 16);
    STAMP(9);
  };
  const unsigned qo = O_Q + ab * 32768 + kvh * 8192;
  if (rW) {
    asm volatile("s_waitcnt lgkmcnt(0)\n\ts_barrier\n\ts_load_dword %0, %1, 0x0\n\ts_waitcnt lgkmcnt(0) vmcnt(0)" : "=s"(ep) : "s"(pws), "v"(cpf) : "memory");
    const unsigned TAG = ep + 0x01010101u;
    later();
    const rsrc_t rt = mk(pwo, 4096 * 2048);
    const unsigned wv = g * 16384 + ((n ^ (g << 1) ^ ((cw & 1) << 3)) << 4);
#pragma nounroll
    for (int i = 0; i < 32; ++i) {
      vmwait<WH0 - 1>();
      const unsigned w = 2 * cw + ((i >> 2) & 1);
      dma16(rt, wv, (oh * 128 + w + (i >> 3) * 32) * 2048 + (wc & 1) * 1024 + (i & 3) * 256, (((i & 3) << 2) | (i >> 3)) * 8192 + w * 1024);
    }
    vmwait<0>();
    lds_signal(F_CBAR, lane); lds_wait<1>(F_CBAR, 4);
    v4u wf[2][16];
#pragma unroll
    for (int j = 0; j < 2; ++j) {
      const unsigned tw = (8 * g + tq) * 256 + (((4 * cw + 2 * j + (tp >> 1)) ^ ((tq | ((g & 1) << 2)) << 1)) << 4) + 8 * (tp & 1);
      const unsigned b0 = opq(tw), b1 = opq(tw + 65536);
#pragma unroll
      for (int f = 0; f < 16; ++f) wf[j][f] = trfrag((f & 8 ? b1 : b0) + (f & 7) * 8192, 1024);
    }
#pragma unroll
    for (int f = 0; f < 16; f += 4) TIE8(wf[0][f], wf[0][f + 1], wf[0][f + 2], wf[0][f + 3], wf[1][f], wf[1][f + 1], wf[1][f + 2], wf[1][f + 3]);
    lds_signal(F_HEAD, lane);
    STAMP(6);
    if (cw == 3) {
      const v4u tn = tagbytes(TAG) & 15u;
      if (early()) {
        Watch w = watch_q(rws, (lane & 31) * 256 + (sp & 1) * 128, qo, tn.x, 15u, 0xffffffffull, THW);
        release_watch(w);
      }
      STAMP(7);
#pragma nounroll
      for (int it = 0; it < POLL_MAX; ++it) {
#pragma unroll
        for (int k = 0; k < 8; ++k) dma16<true>(rws, lane * 16, qo + k * 1024, L_RED + k * 1024);
        vmwait<0>();
        unsigned bad = 0;
#pragma unroll
        for (int k = 0; k < 4; ++k) {
          const v4u a = lds_ld<v4u>(L_RED + k * 1024 + lane * 16) ^ tn, b = lds_ld<v4u>(L_RED + 4096 + k * 1024 + lane * 16) ^ tn;
          bad |= ((a.x | a.y) | (a.z | a.w)) | ((b.x | b.y) | (b.z | b.w));
          const v4f t = asf4(a) + asf4(b);
          lds_st<v2u>(L_QB + (2 * k + (lane >> 5)) * 288 + (lane & 31) * 8, (v2u){pk(t[0], t[1]), pk(t[2], t[3])});
          SCHED();
        }
        if (!__any((bad & 15u) != 0)) break;
      }
      lds_signal(F_Q, lane);
      STAMP(8);
      softmax_stats(TAG);
    }
    const unsigned bb = opq(L_AX + (n & 7) * 288 + g * 16);
    const unsigned po = opq(((n >> 1) & 3) * 131072 + (n >> 3) * 2048 + (n & 1) * 8 + g * 16);
    const unsigned so = sopq(O_WP + ((wc & 1) * 32 + 2 * cw) * 2048 + oh * 64);
    asm volatile("s_barrier\n\ts_barrier" ::: "memory");
    v4u b[4];
#pragma unroll
    for (int kb = 0; kb < 4; ++kb) b[kb] = lds_ld<v4u>(bb + kb * 64);
    SCHED();
    const v4f z = {0.f, 0.f, 0.f, 0.f};
#pragma unroll
    for (int h = 0; h < 2; ++h) {
      v4f d[4];
#pragma unroll
      for (int kb = 0; kb < 4; ++kb)
#pragma unroll
        for (int t = 0; t < 4; ++t) d[t] = mfma(wf[t & 1][8 * h + 4 * (t >> 1) + kb], b[kb], kb ? d[t] : z);
#pragma unroll
      for (int t = 0; t < 2; ++t) {
        const v4f e = sel4(hi, d[2 * t + 1], d[2 * t]);
        st_dev2((v2u){canon(pk(e[0], e[1])), canon(pk(e[2], e[3]))}, rws, po, so + (2 * h + t) * 16384);
      }
    }
    STAMP(10);
    FIN();
  }
  const bool slow = !first && !jq;
  if (jq) {
    const v4u e = {SENT, SENT, SENT, SENT};
    const unsigned so = O_X1 + ((wg - 32) * 4 + cw) * 2304;
    st_dev(e, rws, lane * 16, so);
    st_dev(e, rws, lane * 16, so + 1024);
    st_dev(e, rws, lane < 16 ? lane * 16 : 0x80000000u, so + 2048);
  }
  v4u in[4];
  {
    const rsrc_t ri = mk(jq ? px : pc, 16384);
    const unsigned vo = (n & 7) * 2048 + cw * 256 + (n >> 3) * 64 + g * 16;
    asm volatile("buffer_load_dwordx4 %0, %4, %5, %6 offen\n\tbuffer_load_dwordx4 %1, %4, %5, %6 offen offset:128\n\t"
                 "buffer_load_dwordx4 %2, %4, %5, %7 offen\n\tbuffer_load_dwordx4 %3, %4, %5, %7 offen offset:128"
                 : "=&v"(in[0]), "=&v"(in[1]), "=&v"(in[2]), "=&v"(in[3]) : "v"(vo), "s"(ri), "s"(ks * 1024), "s"(1024 - ks * 1024) : "memory");
  }
  const unsigned rowb = jq ? 8192 : 12288;
  const rsrc_t rh = mk(jq ? pwq : pwm, 1024 * rowb);
  const unsigned v0 = (lane >> 3) * rowb + sw64, v1 = v0 ^ 64;
  const unsigned col0 = jq ? (ks * 512 + cw * 128) * rowb + jt * 128 : ((ks ^ 1) * 512 + cw * 128) * rowb + (first ? mt : mt + 48) * 128;
  if (!slow) {
#pragma unroll
    for (int k = 0; k < 8; ++k) dma16(rh, (k & 1) ? v1 : v0, k * 8 * rowb + col0, cw * 32768 + k * 1024);
  }
  asm volatile("s_waitcnt lgkmcnt(0)\n\ts_barrier\n\ts_load_dword %0, %1, 0x0" : "=s"(ep) : "s"(pws) : "memory");
  if (first) {
#pragma unroll
    for (int k = 8; k < 16; ++k) dma16(rh, (k & 1) ? v1 : v0, k * 8 * rowb + col0, cw * 32768 + k * 1024);
    vmwait<16>();
  } else {
    if (slow) vmwait<0>();
    vmwait<8>();
  }
  asm volatile("" : "+v"(in[0]), "+v"(in[1]), "+v"(in[2]), "+v"(in[3]) : "v"(cpf) : "memory");
  STAMP(1);
  if (!jq) {
#pragma unroll
    for (int p = 0; p < 4; ++p) {
#pragma unroll
      for (int e = 0; e < 4; ++e) in[p][e] = pk(silu1(bflo(in[p][e])), silu1(bfhi(in[p][e])));
    }
  } else {
    float s = 0.f;
#pragma unroll
    for (int p = 0; p < 4; ++p)
#pragma unroll
      for (int e = 0; e < 4; ++e) s += bflo(in[p][e]) + bfhi(in[p][e]);
    s = sum8up(s);
    if (lane < 8) lds_st<float>(L_SX + lane * 16 + cw * 4, s);
    lds_signal(F_CBAR, lane); cb_n += 4; lds_wait<1>(F_CBAR, cb_n);
    v4f t = lds_ld<v4f>(L_SX + (n & 7) * 16);
    const float m = ((t[0] + t[1]) + (t[2] + t[3])) * (1.f / 1024.f);
    s = 0.f;
#pragma unroll
    for (int p = 0; p < 4; ++p)
#pragma unroll
      for (int e = 0; e < 4; ++e) { float a = bflo(in[p][e]) - m, b = bfhi(in[p][e]) - m; s = fmaf(a, a, s); s = fmaf(b, b, s); }
    s = sum8up(s);
    if (lane < 8) lds_st<float>(L_SX + 128 + lane * 16 + cw * 4, s);
    lds_signal(F_CBAR, lane); cb_n += 4; lds_wait<1>(F_CBAR, cb_n);
    t = lds_ld<v4f>(L_SX + 128 + (n & 7) * 16);
    const Stat st = mk_stat(m, ((t[0] + t[1]) + (t[2] + t[3])) * (1.f / 1024.f));
#pragma unroll
    for (int p = 0; p < 2; ++p)
#pragma unroll
      for (int e = 0; e < 4; ++e) in[p][e] = ln_pair(in[p][e], st);
  }
  STAMP(2);
  asm volatile("s_waitcnt lgkmcnt(0)" : "+s"(ep) : : "memory");
  const unsigned TAG = ep + 0x01010101u;
  const v4u tn = tagbytes(TAG) & 15u;
  unsigned tba[4];
  {
    const unsigned sw = ((tq >> 1) & 1) | ((g & 1) << 1);
    const unsigned tb0 = cw * 32768 + (8 * g + tq) * 128 + (((tp >> 1) ^ (sw << 1)) << 4) + 8 * (tp & 1);
#pragma unroll
    for (int t = 0; t < 4; ++t) tba[t] = opq(tb0 ^ (t << 5));
  }
  v4u wf[4][4];
  v4f d[4];
  auto zero = [&]() __attribute__((always_inline)) {
#pragma unroll
    for (int t = 0; t < 4; ++t) d[t] = (v4f){0.f, 0.f, 0.f, 0.f};
  };
  auto frag = [&](int kb, unsigned off) __attribute__((always_inline)) {
#pragma unroll
    for (int t = 0; t < 4; ++t) wf[kb][t] = trfrag(tba[t] + off + kb * 4096, 512);
  };
  auto mm = [&](int kb, const v4u& x) __attribute__((always_inline)) {
    const v4u b = (kb & 1) ? dup_hi(x) : dup_lo(x);
#pragma unroll
    for (int t = 0; t < 4; ++t) d[t] = mfma(wf[kb][t], b, d[t]);
  };
  auto reduce = [&](unsigned buf, unsigned rb) __attribute__((always_inline)) {
    const unsigned ra = buf + cw * 2048 + (n >> 3) * 1024 + (n & 7) * 64 + g * 16;
    lds_st<v4f>(ra, sel4(hi, d[2], d[0]));
    lds_st<v4f>(ra + 512, sel4(hi, d[3], d[1]));
    lds_signal(F_CBAR, lane); cb_n += 4; lds_wait<0>(F_CBAR, cb_n);
    rb += buf + (lane >> 5) * 4096;
    v4f s = lds_ld<v4f>(rb) + lds_ld<v4f>(rb + 2048);
#pragma unroll
    for (int i = 0; i < 4; ++i) s[i] = x32sum(s[i]);
    return s;
  };
  if (!jq) {
    if (!first) {
      later();
#pragma nounroll
      for (int k = 0; k < 16; ++k) { vmwait<WH0 - 1>(); dma16(rh, v0 ^ ((k & 1) << 6), k * 8 * rowb + col0, cw * 32768 + k * 1024); }
    }
#pragma unroll
    for (int k = 0; k < 16; ++k) {
      if (!first) vmwait<WH0 - 1>();
      dma16(rh, (k & 1) ? v1 : v0, (ks * 1024 - 512 + k * 8) * rowb + col0, cw * 32768 + 16384 + k * 1024);
    }
    lds_signal(F_GO, lane);
    {
      const unsigned a = L_AX + (n & 7) * 1056 + cw * 256 + (cw >> 1) * 16 + (n >> 3) * 64 + g * 16;
      lds_st<v4u>(a, in[0]); lds_st<v4u>(a + 128, in[1]);
    }
    zero();
    vmwait<16>();
#pragma unroll
    for (int kb = 0; kb < 4; ++kb) frag(kb, 0);
#pragma unroll
    for (int kb = 0; kb < 4; ++kb) {
      mm(kb, in[2 + (kb >> 1)]);
      SCHED();
      if (kb == 0) vmwait<12>(); if (kb == 1) vmwait<8>(); if (kb == 2) vmwait<4>(); if (kb == 3) vmwait<0>();
      frag(kb, 16384);
      if (kb) mm(kb - 1, in[(kb - 1) >> 1]);
      SCHED();
    }
    mm(3, in[1]);
    lds_signal(F_HEAD, lane);
    if (first) {
      v4u a[1];
      gatherf<1, 4, ~0u>(a, rws, (lane & 3) * 16, O_RF + (wg * 4 + cw) * 64, 0, (v4u){TAG, TAG, TAG, TAG});
    }
    const v4f s = reduce(L_RED, cw * 512 + (lane & 31) * 16);
    st_dev((v4u){pk(s[0], s[1]), TAG, pk(s[2], s[3]), TAG}, rws,
           first ? (lane >> 5) * 65536 + (lane & 1) * 1024 + ((lane >> 1) & 1) * 256 + ((lane >> 2) & 7) * 16 : ((lane >> 2) & 7) * 8192 + (lane & 3) * 16,
           first ? O_SC + mt * 2048 + (cw & 1) * 512 + (cw >> 1) * 128 : O_MOD + mt * 256 + cw * 64);
    STAMP(11);
  } else {
    vmwait<0>();
    st_dev((v4u){TAG, TAG, TAG, TAG}, rws, lane ? 0x80000000u : 0u, O_RF + (wg - 32) * 64 + cw * 16);
#pragma unroll
    for (int k = 8; k < 16; ++k) {
      vmwait<WH1 - 1>();
      dma16(rh, (k & 1) ? v1 : v0, k * 8 * rowb + col0, cw * 32768 + k * 1024);
    }
    {
      const int gp = (wg - 32) >> 4, gc = wg & 15;
      v4u cv;
      asm volatile("buffer_load_dwordx4 %0, %1, %2, %3 offen" : "=v"(cv) : "v"((n & 7) * 2048 + g * 16), "s"(mk(pc, 16384)), "s"(gp * 256 + cw * 64) : "memory");
      const rsrc_t rm = mk(pwm, 1024 * 12288);
      const unsigned u0 = (lane >> 3) * 12288 + sw64;
#pragma unroll
      for (int k = 0; k < 4; ++k) {
        vmwait<WH1 - 1>();
        dma16(rm, u0 ^ ((k & 1) << 6), (gp * 128 + cw * 32 + k * 8) * 12288 + 4096 + gc * 128, cw * 32768 + 16384 + k * 1024);
      }
      asm volatile("s_waitcnt vmcnt(4)" : "+v"(cv) : : "memory");
      frag(1, 0); frag(2, 0); frag(3, 0);
#pragma unroll
      for (int e = 0; e < 4; ++e) cv[e] = pk(silu1(bflo(cv[e])), silu1(bfhi(cv[e])));
      vmwait<0>();
      frag(0, 16384);
      zero();
#pragma unroll
      for (int t = 0; t < 4; ++t) d[t] = mfma(wf[0][t], cv, d[t]);
      SCHED();
      frag(0, 0);
      asm volatile("s_waitcnt lgkmcnt(0)" ::: "memory");
      lds_signal(F_HEAD, lane);
      const v4f s = reduce(L_RED, cw * 512 + (lane & 31) * 16);
      if (lane < 32) st_dev(tag4(s, tagbytes(TAG)), rws, (lane >> 3) * 131072 + (lane & 7) * 16, O_GX + (gc * 4 + cw) * 2048 + gp * 128);
    }
    STAMP(3);
    {
      const unsigned so = O_SC + (jt & 1) * 65536 + ks * 16384 + cw * 4096;
      Watch w = watch_m(rws, ((lane & 2) ? 32768 : 0) + (lane & 1) * 2048 + 4, so, TAG, ~0u, 0xfull, THM);
      STAMP(4);
      v4u a[8];
      gather8<DMR>(a, rws, lane * 16, so, 32768, [&](v4u x) { return ((x.y ^ TAG) | (x.w ^ TAG)) != 0; });
      release_watch(w);
      STAMP(5);
#pragma unroll
      for (int p = 0; p < 2; ++p)
#pragma unroll
        for (int e = 0; e < 4; ++e) in[p][e] = mod_pair(in[p][e], onep(a[4 + 2 * p + (e >> 1)][(e & 1) * 2]), a[2 * p + (e >> 1)][(e & 1) * 2]);
    }
    zero();
#pragma unroll
    for (int kb = 0; kb < 4; ++kb) mm(kb, in[kb >> 1]);
    const unsigned r1 = (lane >> 4) & 1;
    v4f s = reduce(L_RED + 8192, (n >> 2) * 512 + (2 * cw + r1) * 64 + (n & 3) * 16);
    if (lane < 32) st_dev(tag4<15u>(s, tn), rws, r1 * 32768 + n * 16, O_Q + cw * 65536 + (jt >> 4) * 8192 + ks * 4096 + (jt & 15) * 256);
  }
  STAMP(6);
  lds_signal(F_CBAR, lane); cb_n += 4;
  if (cw == 3) {
    if (first) __builtin_amdgcn_s_sleep(DQ0);
    Watch w = {};
    if (!slow || early()) w = watch_q(rws, (lane & 31) * 256 + (sp & 1) * 128, qo, tn.x, 15u, 0xffffffffull, THQ);
    STAMP(7);
    v4u a[8];
    gather8<DMR>(a, rws, lane * 16, qo, 4096, [&](v4u x) { x ^= tn; return (((x.x | x.y) | (x.z | x.w)) & 15u) != 0; });
    release_watch(w);
#pragma unroll
    for (int k = 0; k < 4; ++k) {
      const v4f t = asf4(a[k] ^ tn) + asf4(a[4 + k] ^ tn);
      lds_st<v2u>(L_QB + (2 * k + (lane >> 5)) * 288 + (lane & 31) * 8, (v2u){pk(t[0], t[1]), pk(t[2], t[3])});
    }
    lds_signal(F_Q, lane);
    STAMP(8);
    softmax_stats(TAG);
  } else {
    if (rG && cw < 2) {
      lds_wait<32>(F_Q, 1);
      lds_wait<8>(F_CBAR, cb_n);
#pragma nounroll
      for (int h = 0; h < 2; ++h) {
        v4u a[8];
        gatherp<8, 32>(a, rws, lane * 16, O_MOD + cw * 4096 + gks * 2048 + h * 1024, 8192, TAG);
#pragma unroll
        for (int k = 0; k < 8; ++k)
          lds_st<v2u>(L_SB + cw * 8192 + k * 1024 + h * 512 + lane * 8, cw ? (v2u){onep(a[k].x), onep(a[k].z)} : (v2u){a[k].x, a[k].z});
      }
    }
    STAMP(8);
  }
  STAMP_DUMP();
  asm volatile("s_waitcnt vmcnt(0) lgkmcnt(0)" ::: "memory");
}

__global__ void code_end_mark() { asm volatile(".Lfk_z:"); }

extern "C" void kernel_launch(void* const* d_in, const int* in_sizes, int n_in, void* d_out, int out_size, void* d_ws, size_t ws_size,
                              hipStream_t stream) {
  (void)in_sizes; (void)n_in; (void)out_size; (void)ws_size;
  (void)hipFuncSetAttribute((const void*)fused_kernel, hipFuncAttributeMaxDynamicSharedMemorySize, L_END);
  fused_kernel<<<NWG, NTHR, L_END, stream>>>(d_in[0], d_in[1], d_in[2], d_in[3], d_in[4], d_in[6], d_in[7], d_in[8], d_in[9], d_in[10],
                                             d_out, d_ws);
}
```
